```python
import torch

def _softmax(x, axis=-1):
    return torch.softmax(x, dim=axis)
def _gelu(x):
    return torch.nn.functional.gelu(x, approximate="tanh")
def _silu(x):
    return torch.nn.functional.silu(x)
def _sigmoid(x):
    return torch.sigmoid(x)
def _layernorm(x, axis=-1, eps=1e-5):
    m = x.mean(dim=axis, keepdim=True)
    v = x.var(dim=axis, keepdim=True, unbiased=False)
    return (x - m) / torch.sqrt(v + eps)
def _scatter_add(tgt, idx, src, axis=0):
    return tgt.clone().index_add_(axis, idx, src)
def _segment_sum(src, seg, n):
    out = torch.zeros((n,) + src.shape[1:], dtype=src.dtype)
    return out.index_add_(0, seg, src)
def _pad(x, pw):
    flat = tuple(v for pair in reversed(pw) for v in pair)
    return torch.nn.functional.pad(x, flat)

kernel_name = 'gated_mix__softmax_full_mha__swiglu_ffn__prompt'

def setup_inputs():

    _w = (lambda a: (lambda t: t.to(torch.bfloat16) if t.is_floating_point() else t)(torch.as_tensor(a)))
    def _A_setup():
        B, S, D = 1, 2048, 1024
        return {
            "x": _w((lambda *s: torch.randn(*s, dtype=torch.bfloat16) * 0.05)(B, S, D)),
            "wqkv": _w((lambda *s: torch.randn(*s, dtype=torch.bfloat16) * 0.8)(D, 3 * D)),
            "wo": _w((lambda *s: torch.randn(*s, dtype=torch.bfloat16) * 0.05)(D, D)),
        }
    _A_in = _A_setup()

    def _B_setup():
        B, S, D, F = 1, 2048, 1024, 3584
        return {
            "x": _w((lambda *s: torch.randn(*s, dtype=torch.bfloat16) * 0.05)(B, S, D)),
            "w_gate": _w((lambda *s: torch.randn(*s, dtype=torch.bfloat16) * 0.1)(D, F)),
            "w_up": _w((lambda *s: torch.randn(*s, dtype=torch.bfloat16) * 0.1)(D, F)),
            "w_down": _w((lambda *s: torch.randn(*s, dtype=torch.bfloat16) * 0.1)(F, D)),
        }
    _B_in = _B_setup()

    return {
        "A_x": _A_in["x"],
        "A_wqkv": _A_in["wqkv"],
        "A_wo": _A_in["wo"],
        "B_x": _B_in["x"],
        "B_w_gate": _B_in["w_gate"],
        "B_w_up": _B_in["w_up"],
        "B_w_down": _B_in["w_down"],
        "gate_w": _w((lambda *s: torch.randn(*s, dtype=torch.bfloat16) * 0.05)(1024) * 16.0),
    }

def reference(A_x, A_wqkv, A_wo, B_x, B_w_gate, B_w_up, B_w_down, gate_w):
    def _A_ref(x, wqkv, wo):
        B, S, D = x.shape
        H = 32; Dh = D // H
        qkv = (lambda x, *d: x.permute(*d))((x @ wqkv).reshape(B, S, 3, H, Dh), 2, 0, 3, 1, 4)
        q, k, v = qkv[0], qkv[1], qkv[2]
        scale = Dh ** -0.5
        scores = torch.einsum("bhqd,bhkd->bhqk", q, k) * scale
        p = _softmax(scores, axis=-1)
        o = torch.einsum("bhqk,bhkd->bhqd", p, v)
        return (lambda x, *d: x.permute(*d))(o, 0, 2, 1, 3).reshape(B, S, D) @ wo

    def _B_ref(x, w_gate, w_up, w_down):
        g = _silu(x @ w_gate)
        u = x @ w_up
        return (g * u) @ w_down

    x = A_x
    g = _sigmoid((x * gate_w).sum(-1, keepdims=True))
    a = _A_ref(x, A_wqkv, A_wo)
    b = _B_ref(x, B_w_gate, B_w_up, B_w_down)
    return g * a + (1 - g) * b
```

```cpp
#include <hip/hip_runtime.h>
#include <hip/hip_bf16.h>
typedef __hip_bfloat16 bf16;
__device__ inline float b2f(bf16 x){ return __bfloat162float(x); }
__global__ void gemm_naive(const bf16* A, const bf16* W, float* C, int M, int N, int K){
  __shared__ float sa[16][17], sw[16][17];
  int n = blockIdx.x*16+threadIdx.x, m = blockIdx.y*16+threadIdx.y;
  float acc=0;
  for(int k0=0;k0<K;k0+=16){
    sa[threadIdx.y][threadIdx.x]=b2f(A[(size_t)m*K+k0+threadIdx.x]);
    sw[threadIdx.y][threadIdx.x]=b2f(W[(size_t)(k0+threadIdx.y)*N+n]);
    __syncthreads();
    for(int k=0;k<16;k++) acc+=sa[threadIdx.y][k]*sw[k][threadIdx.x];
    __syncthreads();
  }
  C[(size_t)m*N+n]=acc;
}
__global__ void to_bf16(const float* x, bf16* y, size_t n){ size_t i=blockIdx.x*(size_t)blockDim.x+threadIdx.x; if(i<n) y[i]=__float2bfloat16(x[i]); }
__global__ void swiglu(const float* g, const float* u, bf16* h, size_t n){ size_t i=blockIdx.x*(size_t)blockDim.x+threadIdx.x; if(i<n){ float a=g[i]; h[i]=__float2bfloat16(a/(1.f+__expf(-a))*u[i]); } }
__global__ void attn_naive(const bf16* qkv, bf16* o){
  int h=blockIdx.y, q=blockIdx.x; __shared__ float s[2048]; __shared__ float red[256]; __shared__ float qq[32];
  int t=threadIdx.x;
  if(t<32) qq[t]=b2f(qkv[(size_t)q*3072+h*32+t]);
  __syncthreads();
  float mx=-1e30f;
  for(int k=t;k<2048;k+=256){ float a=0; for(int d=0;d<32;d++) a+=qq[d]*b2f(qkv[(size_t)k*3072+1024+h*32+d]); a*=0.17677669529f; s[k]=a; mx=fmaxf(mx,a);}
  red[t]=mx; __syncthreads();
  for(int o2=128;o2>0;o2>>=1){ if(t<o2) red[t]=fmaxf(red[t],red[t+o2]); __syncthreads(); }
  mx=red[0]; __syncthreads();
  float sum=0; for(int k=t;k<2048;k+=256){ float e=__expf(s[k]-mx); s[k]=e; sum+=e; }
  red[t]=sum; __syncthreads();
  for(int o2=128;o2>0;o2>>=1){ if(t<o2) red[t]+=red[t+o2]; __syncthreads(); }
  sum=red[0]; __syncthreads();
  int d=t&31, part=t>>5; float a=0;
  for(int k=part;k<2048;k+=8) a+=s[k]*b2f(qkv[(size_t)k*3072+2048+h*32+d]);
  red[t]=a; __syncthreads();
  if(t<32){ float r=0; for(int p=0;p<8;p++) r+=red[p*32+t]; o[(size_t)q*1024+h*32+t]=__float2bfloat16(r/sum); }
}
__global__ void final_mix(const bf16* x, const bf16* gw, const float* a, const float* b, bf16* out){
  int r=blockIdx.x, t=threadIdx.x; __shared__ float red[256];
  float s=0; for(int i=t;i<1024;i+=256) s+=b2f(x[(size_t)r*1024+i])*b2f(gw[i]);
  red[t]=s; __syncthreads();
  for(int o2=128;o2>0;o2>>=1){ if(t<o2) red[t]+=red[t+o2]; __syncthreads(); }
  float g=1.f/(1.f+__expf(-red[0]));
  for(int i=t;i<1024;i+=256) out[(size_t)r*1024+i]=__float2bfloat16(g*a[(size_t)r*1024+i]+(1.f-g)*b[(size_t)r*1024+i]);
}
extern "C" void kernel_launch(void* const* d_in, const int* in_sizes, int n_in, void* d_out, int out_size, void* d_ws, size_t ws_size, hipStream_t stream){
  const bf16* Ax=(const bf16*)d_in[0]; const bf16* wqkv=(const bf16*)d_in[1]; const bf16* wo=(const bf16*)d_in[2];
  const bf16* Bx=(const bf16*)d_in[3]; const bf16* wg=(const bf16*)d_in[4]; const bf16* wu=(const bf16*)d_in[5]; const bf16* wd=(const bf16*)d_in[6]; const bf16* gw=(const bf16*)d_in[7];
  char* ws=(char*)d_ws;
  float* f0=(float*)ws; float* f1=(float*)(ws+(32<<20)); bf16* qkv=(bf16*)(ws+(64<<20)); bf16* o=(bf16*)(ws+(80<<20)); bf16* h=(bf16*)(ws+(96<<20));
  float* fa=(float*)(ws+(112<<20)); float* fb=(float*)(ws+(128<<20));
  dim3 b(16,16);
  gemm_naive<<<dim3(3072/16,2048/16),b,0,stream>>>(Ax,wqkv,f0,2048,3072,1024);
  to_bf16<<<(2048*3072+255)/256,256,0,stream>>>(f0,qkv,(size_t)2048*3072);
  attn_naive<<<dim3(2048,32),256,0,stream>>>(qkv,o);
  gemm_naive<<<dim3(1024/16,2048/16),b,0,stream>>>(o,wo,fa,2048,1024,1024);
  gemm_naive<<<dim3(3584/16,2048/16),b,0,stream>>>(Ax,wg,f0,2048,3584,1024);
  gemm_naive<<<dim3(3584/16,2048/16),b,0,stream>>>(Ax,wu,f1,2048,3584,1024);
  swiglu<<<(2048*3584+255)/256,256,0,stream>>>(f0,f1,h,(size_t)2048*3584);
  gemm_naive<<<dim3(1024/16,2048/16),b,0,stream>>>(h,wd,fb,2048,1024,3584);
  final_mix<<<2048,256,0,stream>>>(Ax,gw,fa,fb,(bf16*)d_out);
}
```

```cpp
#include <hip/hip_runtime.h>
#include <hip/hip_runtime.h>
#include <stdint.h>
#define LAS __attribute__((address_space(3)))
#define DI __device__ __forceinline__
typedef unsigned short bf16_t;
typedef short bf16x8 __attribute__((ext_vector_type(8)));
typedef short s16x4 __attribute__((ext_vector_type(4)));
typedef short v4i16_t __attribute__((ext_vector_type(4)));
typedef float f32x4 __attribute__((ext_vector_type(4)));
typedef float f32x16 __attribute__((ext_vector_type(16)));
typedef unsigned u32x4 __attribute__((ext_vector_type(4)));
typedef unsigned u32x2 __attribute__((ext_vector_type(2)));
typedef float f32x2 __attribute__((ext_vector_type(2)));
typedef __bf16 bf16x2_t __attribute__((ext_vector_type(2)));
#define SBAR() __builtin_amdgcn_sched_barrier(0)

constexpr int S_ = 2048, D_ = 1024, H_ = 32, DH_ = 32, F_ = 3584, HO_LD = F_ + D_;

DI unsigned cvtpk(float lo, float hi) { f32x2 v = {lo, hi}; bf16x2_t b = __builtin_convertvector(v, bf16x2_t); return __builtin_bit_cast(unsigned, b); }
DI float bf2f(unsigned short b) { return __uint_as_float(((unsigned)b) << 16); }
DI void glds16(unsigned voff, const void* sbase, unsigned lds_dst) {
    asm volatile("s_mov_b32 m0, %2\n\ts_nop 0\n\tglobal_load_lds_dwordx4 %0, %1" :: "v"(voff), "s"(sbase), "s"(lds_dst) : "memory");
}
DI s16x4 ldtr(const LAS char* p) { return __builtin_bit_cast(s16x4, __builtin_amdgcn_ds_read_tr16_b64_v4i16((LAS v4i16_t*)p)); }
DI bf16x8 ld128(const LAS char* p) { return *(const LAS bf16x8*)p; }
DI bf16x8 cat8(s16x4 lo, s16x4 hi) { return __builtin_shufflevector(lo, hi, 0, 1, 2, 3, 4, 5, 6, 7); }
#define MFMA16(a, b, c) __builtin_amdgcn_mfma_f32_16x16x32_bf16(a, b, c, 0, 0, 0)
#define MFMA16A(acc, a, b) asm volatile("v_mfma_f32_16x16x32_bf16 %0, %1, %2, %0" : "+v"(acc) : "v"(a), "v"(b))
#define MFMA32(a, b, c) __builtin_amdgcn_mfma_f32_32x32x16_bf16(a, b, c, 0, 0, 0)

#define ABL_NODMA 0
#define ABL_NOLDS 0
#define ABL_NOBAR 0
#define K1_WSTAMP0()
#define K1_WSTAMP1()
#define K1_TRACE(t)
#define KSTAMP(k, i)
#define K1_STAMP(i)
namespace k1 {
constexpr int A_STAGE = 32768, B_STAGE = 49152, B_BASE = 65536, LDS_BYTES = 163840;
constexpr int NT = D_ / 64;
constexpr float QSCALE = 0.17677669529663687f * 1.4426950408889634f;

struct Args { const bf16_t* x; const bf16_t* wqkv; const bf16_t* wg; const bf16_t* wu; const bf16_t* gw; bf16_t* qkv; bf16_t* h; float* g; };

DI void epilogue(const Args& a, f32x4 (&acc)[4][10], const int lane, const int w, const int rb, const int ct);
template <int PD, int DPS>
DI void body(const Args& a, char* smem) {
    const unsigned lds0 = (unsigned)(uintptr_t)smem;
    const int tid = threadIdx.x, lane = tid & 63, w = __builtin_amdgcn_readfirstlane(tid >> 6);
    const int wr = w & 3, wc = w >> 2;
    const int xg = blockIdx.x & 7, j = blockIdx.x >> 3, rb = j & 7, ct = xg * 4 + (j >> 3);
    const unsigned voffA = (unsigned)((lane >> 3) * 2048 + (((lane & 7) ^ (lane >> 3)) * 16));
    const int sB = (lane >> 4) | (((w >> 1) & 1) << 2);
    int chq = (lane & 15) ^ (sB << 1), chg = chq;
    if (chq >= 12) chq -= 4;
    if (chg >= 14) chg -= 2;
    const unsigned voffQ = (unsigned)((lane >> 4) * 6144 + chq * 16), voffG = (unsigned)((lane >> 4) * 7168 + chg * 16);
    const char* gA = (const char*)a.x + (size_t)(rb * 256 + w * 32) * 2048;
    const char* gQ = (const char*)a.wqkv + (size_t)(w * 4) * 6144 + ct * 192;
    const char* gG = (const char*)a.wg + (size_t)(w * 4) * 7168 + ct * 224;
    const char* gU = (const char*)a.wu + (size_t)(w * 4) * 7168 + ct * 224;
    const unsigned dA = lds0 + w * 4096, dB = lds0 + B_BASE + w * 1024;
#define K1_DMA(p, t, st) do { if (ABL_NODMA && (t) > 1) break; \
        if ((p) < 4) glds16(voffA, gA + (size_t)(t) * 128 + (p) * 8 * 2048, dA + (st) * A_STAGE + (p) * 1024); \
        else if ((p) == 4 || (p) == 7) glds16(voffQ, gQ + (size_t)(t) * (64 * 6144) + ((p) == 7 ? 32 * 6144 : 0), dB + (st) * B_STAGE + ((p) == 7 ? 8192 : 0)); \
        else if ((p) == 5 || (p) == 8) glds16(voffG, gG + (size_t)(t) * (64 * 7168) + ((p) == 8 ? 32 * 7168 : 0), dB + (st) * B_STAGE + 16384 + ((p) == 8 ? 8192 : 0)); \
        else glds16(voffG, gU + (size_t)(t) * (64 * 7168) + ((p) == 9 ? 32 * 7168 : 0), dB + (st) * B_STAGE + 32768 + ((p) == 9 ? 8192 : 0)); } while (0)
    const int fr = lane & 15, fq = lane >> 4;
    const unsigned rA0 = lds0 + (wr * 64 + fr) * 128 + ((fq ^ (fr & 7)) * 16);
    const unsigned rA1 = lds0 + (wr * 64 + fr) * 128 + (((fq + 4) ^ (fr & 7)) * 16);
    const int q = (lane & 15) >> 2, p = lane & 3, sR = q | ((fq & 1) << 2);
    const int bbase = (8 * fq + q) * 256 + 32 * sR + 8 * p;
    unsigned rB[6];
#pragma unroll
    for (int jj = 0; jj < 4; ++jj) rB[jj] = lds0 + B_BASE + (bbase ^ ((3 * wc + jj) << 5));
    rB[4] = wc ? rB[3] + 16384 : rB[0];
    rB[5] = wc ? rB[3] + 32768 : rB[3];
    f32x4 acc[4][10];
#pragma unroll
    for (int m = 0; m < 4; ++m)
#pragma unroll
        for (int n = 0; n < 10; ++n) acc[m][n] = (f32x4){0.f, 0.f, 0.f, 0.f};
    bf16x8 Af[2][4];
    bf16x8 Bf[PD + 1];
#define LP(x) ((const LAS char*)(size_t)(x))
#define K1_BADDR(n) ((n) < 6 ? LP(rB[(n) >> 1]) + (((n) & 1) ? 32768 : 16384) : (n) == 6 ? LP(rB[1]) : (n) == 7 ? LP(rB[2]) : (n) == 8 ? LP(rB[4]) : LP(rB[5]))
#define K1_LDB(dst, n, ks) do { if (ABL_NOLDS && ablon) { asm volatile("" : "+v"(dst)); break; } const LAS char* _p = K1_BADDR(n) + (ks) * 8192; dst = cat8(ldtr(_p), ldtr(_p + 1024)); } while (0)
#define K1_LDA(m, ks, so) do { if (ABL_NOLDS && ablon) { asm volatile("" : "+v"(Af[ks][m])); break; } Af[ks][m] = ld128(LP((ks) ? rA1 : rA0) + (so) + (m) * 2048); } while (0)
#define K1_TOGGLE(CUR) do { _Pragma("unroll") for (int jj = 0; jj < 6; ++jj) { rB[jj] += (CUR) ? -B_STAGE : B_STAGE; asm volatile("" : "+v"(rB[jj])); } } while (0)
#define K1_SYNC(N) do { K1_WSTAMP0(); \
        if (ABL_NOBAR) asm volatile("s_waitcnt vmcnt(" #N ") lgkmcnt(0)" ::: "memory"); else asm volatile("s_waitcnt vmcnt(" #N ") lgkmcnt(0)\n\ts_barrier" ::: "memory"); \
        K1_WSTAMP1(); } while (0)

    bool ablon = false; (void)ablon; long long wsum = 0, wt0 = 0; (void)wsum; (void)wt0;
#pragma unroll
    for (int pc = 0; pc < 10; ++pc) K1_DMA(pc, 0, 0);
#pragma unroll
    for (int pc = 0; pc < 10; ++pc) K1_DMA(pc, 1, 1);
    asm volatile("s_waitcnt vmcnt(13)\n\ts_barrier" ::: "memory");
    K1_STAMP(1);
#pragma unroll
    for (int m = 0; m < 4; ++m) K1_LDA(m, 0, 0);
#pragma unroll
    for (int s = 0; s < PD; ++s) K1_LDB(Bf[s], s, 0);

    constexpr int SM = 10 - PD, SE = 20 - PD;
#define K1_W1SLOT(r) ((r) == 0 ? 0 : (r) == 1 ? 1 : (r) == 3 ? 2 : (r) == 4 ? 3 : (r) == 6 ? 4 : (r) == 7 ? 5 : (r) == 8 ? 6 : -1)
#define K1_W2SLOT(r) ((r) == 0 ? 7 : (r) == 3 ? 8 : (r) == 6 ? 9 : -1)
#define K1_ISSUE(s, t, CUR, W1ON, W2ON, W2PREV, SH) do { \
            if ((W1ON) && (s) >= SM && (s) < SE) { const int pc = K1_W1SLOT((s) - SM - (SH)); if (pc >= 0) K1_DMA(pc, (t) + 2, (CUR)); } \
            if ((W2ON) && (s) >= SE) { const int pc = K1_W2SLOT((s) - SE - (SH)); if (pc >= 0) K1_DMA(pc, (t) + 2, (CUR)); } \
            if ((W2PREV) && (s) < SM) { const int pc = K1_W2SLOT((s) + 20 - SE - (SH)); if (pc >= 0) K1_DMA(pc, (t) + 1, 1 - (CUR)); } } while (0)
#define K1_TILE(t, CUR, VM_M, VM_E, LAST, W1ON, W2ON, W2PREV) do { K1_TRACE(t); \
        _Pragma("unroll") for (int s = 0; s < 20; ++s) { \
            const int ks = s / 10, n = s % 10; \
            if (s == SM) K1_SYNC(VM_M); \
            if (s == SE && !(LAST)) { K1_SYNC(VM_E); K1_TOGGLE(CUR); } \
            { const int sp = s + PD; \
              if (sp < 20) K1_LDB(Bf[sp % (PD + 1)], sp % 10, sp / 10); \
              else if (!(LAST)) K1_LDB(Bf[sp % (PD + 1)], sp - 20, 0); } \
            if (s < SM && s >= SM - 7 && ((s - (SM - 7)) & 1) == 0) K1_LDA((s - (SM - 7)) / 2, 1, (CUR) * A_STAGE); \
            if (!(LAST) && s >= SE) { \
                if (s == SE) { K1_LDA(0, 0, (1 - (CUR)) * A_STAGE); K1_LDA(1, 0, (1 - (CUR)) * A_STAGE); } \
                if (s == SE + 1) K1_LDA(2, 0, (1 - (CUR)) * A_STAGE); \
                if (s == SE + 2) K1_LDA(3, 0, (1 - (CUR)) * A_STAGE); } \
            if (DPS == 1) { if (wc) K1_ISSUE(s, t, CUR, W1ON, W2ON, W2PREV, 1); else K1_ISSUE(s, t, CUR, W1ON, W2ON, W2PREV, 0); } \
            else K1_ISSUE(s, t, CUR, W1ON, W2ON, W2PREV, 0); \
            SBAR(); \
            _Pragma("unroll") for (int m = 0; m < 4; ++m) acc[m][n] = MFMA16(Bf[s % (PD + 1)], Af[ks][m], acc[m][n]); \
            SBAR(); \
        } } while (0)
    static_assert(20 % (PD + 1) == 0, "ring index must be static across tiles");
    static_assert(PD == 3, "schedule tables assume PD == 3");
    K1_TILE(0, 0, 10, 10, false, true, true, false);
    ablon = true;
    for (int t = 1; t < NT - 3; t += 2) {
        K1_TILE(t, 1, 10, 10, false, true, true, true);
        K1_TILE(t + 1, 0, 10, 10, false, true, true, true);
    }
    K1_TILE(NT - 3, 1, 10, 10, false, true, true, true);
    K1_TILE(NT - 2, 0, 10, 3, false, false, false, true);
    K1_TILE(NT - 1, 1, 0, 0, true, false, false, false);
    K1_STAMP(2); K1_TRACE(16);

    int lane2; asm volatile("v_mbcnt_lo_u32_b32 %0, -1, 0\n\tv_mbcnt_hi_u32_b32 %0, -1, %0" : "=v"(lane2));
    epilogue(a, acc, lane2, w, rb, ct);
}
DI void epilogue(const Args& a, f32x4 (&acc)[4][10], const int lane, const int w, const int rb, const int ct) {
    const int wr = w & 3, wc = w >> 2, fr = lane & 15, fq = lane >> 4;
    const int grow = rb * 256 + ct * 8 + w;
    u32x4 gx0, gx1, gg0, gg1;
    { const u32x4* xr = (const u32x4*)(a.x + (size_t)grow * D_) + lane * 2; const u32x4* gr = (const u32x4*)(a.gw) + lane * 2;
      gx0 = xr[0]; gx1 = xr[1]; gg0 = gr[0]; gg1 = gr[1]; }

    const int row0 = rb * 256 + wr * 64 + fr;
    const int cperm = ((fq & 1) << 4) | ((fq >> 1) << 3);
    auto swiglu = [&](int m, int ng, unsigned (&o)[2]) {
        float hv[4];
#pragma unroll
        for (int e = 0; e < 4; ++e) { const float gt = acc[m][ng][e], up = acc[m][ng + 1][e]; hv[e] = gt * up * __builtin_amdgcn_rcpf(1.0f + __builtin_amdgcn_exp2f(-1.4426950408889634f * gt)); }
        o[0] = cvtpk(hv[0], hv[1]); o[1] = cvtpk(hv[2], hv[3]);
    };
    {
        bf16_t* hb = a.h + (size_t)row0 * HO_LD + ct * 112 + wc * 48;
#pragma unroll
        for (int m = 0; m < 4; ++m) {
            unsigned p0[2], p1[2], p2[2], p3[2];
            swiglu(m, 0, p0); swiglu(m, 2, p1); swiglu(m, 4, p2);
            bf16_t* hp = hb + (size_t)m * 16 * HO_LD;
            {   auto r0 = __builtin_amdgcn_permlane16_swap(p0[0], p1[0], false, false);
                auto r1 = __builtin_amdgcn_permlane16_swap(p0[1], p1[1], false, false);
                *(u32x4*)(hp + cperm) = (u32x4){r0[0], r1[0], r0[1], r1[1]}; }
            if (wc) {
                swiglu(m, 8, p3);
                auto r0 = __builtin_amdgcn_permlane16_swap(p2[0], p3[0], false, false);
                auto r1 = __builtin_amdgcn_permlane16_swap(p2[1], p3[1], false, false);
                *(u32x4*)(hp + 32 + cperm) = (u32x4){r0[0], r1[0], r0[1], r1[1]};
            } else {
                *(u32x2*)(hp + 32 + 4 * fq) = (u32x2){p2[0], p2[1]};
            }
        }
    }
    {
        auto put = [&](int slot, int nlo, int nhi) {
            const float sc = slot < 32 ? QSCALE : 1.0f;
            bf16_t* qb = a.qkv + (size_t)slot * (S_ * 32) + (size_t)row0 * 32 + cperm;
#pragma unroll
            for (int m = 0; m < 4; ++m) {
                const f32x4 lo = acc[m][nlo] * sc, hi = acc[m][nhi] * sc;
                auto r0 = __builtin_amdgcn_permlane16_swap(cvtpk(lo[0], lo[1]), cvtpk(hi[0], hi[1]), false, false);
                auto r1 = __builtin_amdgcn_permlane16_swap(cvtpk(lo[2], lo[3]), cvtpk(hi[2], hi[3]), false, false);
                *(u32x4*)(qb + m * 16 * 32) = (u32x4){r0[0], r1[0], r0[1], r1[1]};
            } };
        if (wc) put(3 * ct + 2, 6, 7);
        else { put(3 * ct, 8, 6); put(3 * ct + 1, 7, 9); }
    }
    {
        float s = 0.f;
#pragma unroll
        for (int i = 0; i < 4; ++i) {
            s += __uint_as_float(gx0[i] << 16) * __uint_as_float(gg0[i] << 16) + __uint_as_float(gx0[i] & 0xffff0000u) * __uint_as_float(gg0[i] & 0xffff0000u);
            s += __uint_as_float(gx1[i] << 16) * __uint_as_float(gg1[i] << 16) + __uint_as_float(gx1[i] & 0xffff0000u) * __uint_as_float(gg1[i] & 0xffff0000u);
        }
#pragma unroll
        for (int o = 32; o >= 1; o >>= 1) s += __shfl_xor(s, o);
        if (lane == 0) a.g[grow] = 1.0f / (1.0f + __expf(-s));
    }
    K1_STAMP(3); K1_TRACE(17);
#undef K1_DMA
#undef K1_BADDR
#undef K1_LDB
#undef K1_LDA
#undef K1_TILE
#undef K1_TOGGLE
}
}

namespace k2 {
constexpr int SLOT = 16384, LDS_FLAG = 4 * SLOT, LDS_BYTES = 4 * SLOT + 16, NTILE = S_ / 128;
struct Args { const bf16_t* qkv; const float* g; bf16_t* o; };

DI unsigned pk2(float lo, float hi) { return cvtpk(lo, hi); }

template <bool SAFE>
DI bool attn_pass(const Args& a, const unsigned lds0, const int h, const int qb, const int w, const int lane, f32x16& o, float& lsum) {
    const int r = lane & 31, hh = lane >> 5;
    const char* gK = (const char*)(a.qkv + (size_t)(32 + h) * (S_ * 32)) + w * 1024;
    const char* gV = (const char*)(a.qkv + (size_t)(64 + h) * (S_ * 32)) + w * 1024;
    const unsigned voffK = (unsigned)((lane >> 2) * 64 + (((lane & 3) ^ ((lane >> 4) & 3)) * 16)), voffV = (unsigned)(lane * 16);
    const unsigned dK = lds0 + w * 1024, dV = lds0 + 8192 + w * 1024;
#define K2_DMA(T) do { glds16(voffK, gK + (size_t)(T) * 8192, dK + ((T) & 3) * SLOT); glds16(voffV, gV + (size_t)(T) * 8192, dV + ((T) & 3) * SLOT); } while (0)
    K2_DMA(0); K2_DMA(1); K2_DMA(2);
    const bf16_t* qp = a.qkv + (size_t)h * (S_ * 32) + (size_t)(qb * 256 + w * 32 + r) * 32 + hh * 8;
    const bf16x8 q0 = *(const bf16x8*)qp, q1 = *(const bf16x8*)(qp + 16);
    const unsigned rK0 = lds0 + r * 64 + (((0 + hh) ^ ((r >> 2) & 3)) * 16), rK1 = lds0 + r * 64 + (((2 + hh) ^ ((r >> 2) & 3)) * 16);
    const unsigned rV = lds0 + 8192 + (4 * hh + ((lane & 15) >> 2)) * 64 + ((lane >> 4) & 1) * 32 + (lane & 3) * 8;
    const short one = (short)0x3f80; const bool sel = ((lane & 15) == ((lane >> 4) & 1));
    const short sv = sel ? one : (short)0; const bf16x8 selA = {sv, sv, sv, sv, sv, sv, sv, sv};
    f32x4 ls = {0.f, 0.f, 0.f, 0.f};
#pragma unroll
    for (int i = 0; i < 16; ++i) o[i] = 0.f;
#define LP(x) ((const LAS char*)(size_t)(x))
#define K2_LDK(k0, k1, off) do { k0 = ld128(LP(rK0) + (off)); k1 = ld128(LP(rK1) + (off)); } while (0)
#define K2_LDV(v0, v1, off) do { v0 = cat8(ldtr(LP(rV) + (off)), ldtr(LP(rV) + (off) + 512)); v1 = cat8(ldtr(LP(rV) + (off) + 1024), ldtr(LP(rV) + (off) + 1536)); } while (0)
    asm volatile("s_waitcnt vmcnt(4)\n\ts_barrier" ::: "memory");
    if constexpr (!SAFE) {
        KSTAMP(1, 1);
        bf16x8 k0, k1, v0, v1;
        u32x4 p0, p1;
        f32x16 negm, sA, sB;
        K2_LDK(k0, k1, 0);
        {
            f32x16 z;
#pragma unroll
            for (int i = 0; i < 16; ++i) z[i] = 0.f;
            f32x16 s = MFMA32(k0, q0, z); s = MFMA32(k1, q1, s);
            float m = s[0];
#pragma unroll
            for (int i = 1; i < 16; ++i) m = __builtin_fmaxf(m, s[i]);
            auto rr = __builtin_amdgcn_permlane32_swap(__float_as_uint(m), __float_as_uint(m), false, false);
            m = __builtin_fmaxf(__uint_as_float(rr[0]), __uint_as_float(rr[1]));
#pragma unroll
            for (int i = 0; i < 16; ++i) negm[i] = -m;
            asm volatile("" : "+v"(negm));
        }
        sA = MFMA32(k0, q0, negm); sA = MFMA32(k1, q1, sA);
        K2_LDK(k0, k1, 2048);
        v0 = cat8(ldtr(LP(rV)), ldtr(LP(rV) + 512));
        p1 = (u32x4){0u, 0u, 0u, 0u}; v1 = v0;
        SBAR();
#define EX(C, i) C[i] = __builtin_amdgcn_exp2f(C[i])
#define CV(dst, C, i) dst = pk2(C[i], C[(i) + 1])
#define PIN4(x) asm volatile("" : "+v"(x))
#define K2_STEP(CUR, NXT, voff, koff, vnoff, DOQK, LOADK, LOADV0, SYNC) do { \
            EX(CUR, 0); EX(CUR, 1); SBAR(); \
            o = MFMA32(v1, __builtin_bit_cast(bf16x8, p1), o); SBAR(); \
            EX(CUR, 2); ls = MFMA16(selA, __builtin_bit_cast(bf16x8, p1), ls); SBAR(); \
            v1 = cat8(ldtr(LP(rV) + (voff) + 1024), ldtr(LP(rV) + (voff) + 1536)); \
            EX(CUR, 3); CV(p0[0], CUR, 0); EX(CUR, 4); EX(CUR, 5); SBAR(); \
            if (DOQK) NXT = MFMA32(k0, q0, negm); \
            SBAR(); \
            CV(p0[1], CUR, 2); EX(CUR, 6); EX(CUR, 7); CV(p0[2], CUR, 4); SBAR(); \
            if (DOQK) NXT = MFMA32(k1, q1, NXT); \
            SBAR(); \
            SYNC; \
            if (LOADK) K2_LDK(k0, k1, koff); \
            EX(CUR, 8); CV(p0[3], CUR, 6); EX(CUR, 9); EX(CUR, 10); SBAR(); \
            o = MFMA32(v0, __builtin_bit_cast(bf16x8, p0), o); SBAR(); \
            if (LOADV0) v0 = cat8(ldtr(LP(rV) + (vnoff)), ldtr(LP(rV) + (vnoff) + 512)); \
            EX(CUR, 11); CV(p1[0], CUR, 8); EX(CUR, 12); EX(CUR, 13); SBAR(); \
            ls = MFMA16(selA, __builtin_bit_cast(bf16x8, p0), ls); SBAR(); \
            EX(CUR, 14); EX(CUR, 15); CV(p1[1], CUR, 10); CV(p1[2], CUR, 12); CV(p1[3], CUR, 14); SBAR(); \
        } while (0)
#define K2_TILE(T, SL, VM, MORE, DMAON) do { \
            K2_STEP(sA, sB, (SL) * SLOT + 0 * 2048, (SL) * SLOT + 2 * 2048, (SL) * SLOT + 1 * 2048, true, true, true, (void)0); \
            K2_STEP(sB, sA, (SL) * SLOT + 1 * 2048, (SL) * SLOT + 3 * 2048, (SL) * SLOT + 2 * 2048, true, true, true, (void)0); \
            K2_STEP(sA, sB, (SL) * SLOT + 2 * 2048, (((SL) + 1) & 3) * SLOT + 0 * 2048, (SL) * SLOT + 3 * 2048, true, MORE, true, \
                    do { if (MORE) { asm volatile("s_waitcnt vmcnt(" #VM ")\n\ts_barrier" ::: "memory"); if (DMAON) K2_DMA_S((T) + 3, ((SL) + 3) & 3); } } while (0)); \
            K2_STEP(sB, sA, (SL) * SLOT + 3 * 2048, (((SL) + 1) & 3) * SLOT + 1 * 2048, (((SL) + 1) & 3) * SLOT + 0 * 2048, MORE, MORE, MORE, (void)0); \
        } while (0)
#define K2_DMA_S(T, SL) do { glds16(voffK, gK + (size_t)(T) * 8192, dK + (SL) * SLOT); glds16(voffV, gV + (size_t)(T) * 8192, dV + (SL) * SLOT); } while (0)
        for (int T = 0; T < NTILE - 4; T += 4) {
            K2_TILE(T, 0, 2, true, true); K2_TILE(T + 1, 1, 2, true, true); K2_TILE(T + 2, 2, 2, true, true); K2_TILE(T + 3, 3, 2, true, true);
        }
        K2_TILE(NTILE - 4, 0, 2, true, true); K2_TILE(NTILE - 3, 1, 2, true, false); K2_TILE(NTILE - 2, 2, 0, true, false); K2_TILE(NTILE - 1, 3, 0, false, false);
        o = MFMA32(v1, __builtin_bit_cast(bf16x8, p1), o); ls = MFMA16(selA, __builtin_bit_cast(bf16x8, p1), ls);
        KSTAMP(1, 2);
#undef EX
#undef CV
#undef K2_STEP
#undef K2_TILE
#undef K2_DMA_S
    } else {
        float m = -INFINITY, l = 0.f;
        for (int T = 0; T < NTILE; ++T) {
            const unsigned cur = (T & 3) * SLOT;
            asm volatile("s_waitcnt vmcnt(0)\n\ts_barrier" ::: "memory");
            if (T + 3 < NTILE) K2_DMA(T + 3);
#pragma unroll 1
            for (int u = 0; u < 4; ++u) {
                bf16x8 k0, k1, v0, v1;
                K2_LDK(k0, k1, cur + u * 2048); K2_LDV(v0, v1, cur + u * 2048);
                f32x16 z;
#pragma unroll
                for (int i = 0; i < 16; ++i) z[i] = 0.f;
                f32x16 s = MFMA32(k0, q0, z); s = MFMA32(k1, q1, s);
                float mx = s[0];
#pragma unroll
                for (int i = 1; i < 16; ++i) mx = __builtin_fmaxf(mx, s[i]);
                auto rr = __builtin_amdgcn_permlane32_swap(__float_as_uint(mx), __float_as_uint(mx), false, false);
                mx = __builtin_fmaxf(__uint_as_float(rr[0]), __uint_as_float(rr[1]));
                const float mn = __builtin_fmaxf(m, mx);
                const float al = (mn == m) ? 1.0f : __builtin_amdgcn_exp2f(m - mn);
                const float mref = (mn == -INFINITY) ? 0.f : mn;
                m = mn;
                float ps = 0.f; u32x4 p0, p1;
#pragma unroll
                for (int i = 0; i < 16; ++i) { s[i] = __builtin_amdgcn_exp2f(s[i] - mref); }
#pragma unroll
                for (int i = 0; i < 4; ++i) { p0[i] = pk2(s[2 * i], s[2 * i + 1]); p1[i] = pk2(s[8 + 2 * i], s[8 + 2 * i + 1]); }
#pragma unroll
                for (int i = 0; i < 4; ++i) ps += __uint_as_float(p0[i] << 16) + __uint_as_float(p0[i] & 0xffff0000u) + __uint_as_float(p1[i] << 16) + __uint_as_float(p1[i] & 0xffff0000u);
                l = l * al + ps;
#pragma unroll
                for (int i = 0; i < 16; ++i) o[i] *= al;
                o = MFMA32(v0, __builtin_bit_cast(bf16x8, p0), o);
                o = MFMA32(v1, __builtin_bit_cast(bf16x8, p1), o);
            }
        }
        auto rr = __builtin_amdgcn_permlane32_swap(__float_as_uint(l), __float_as_uint(l), false, false);
        lsum = __uint_as_float(rr[0]) + __uint_as_float(rr[1]);
        return true;
    }
    const float l0 = __shfl(ls[0], r & 15), l1 = __shfl(ls[1], r & 15);
    lsum = (r < 16) ? l0 : l1;
    bool ok = lsum < 3.0e38f;
#pragma unroll
    for (int i = 0; i < 16; ++i) ok = ok && (__builtin_fabsf(o[i]) < 3.0e38f);
    return ok;
#undef K2_DMA
#undef K2_LDK
#undef K2_LDV
#undef K2_STEP
}

DI void body(const Args& a, char* smem) {
    const unsigned lds0 = (unsigned)(uintptr_t)smem;
    const int tid = threadIdx.x, lane = tid & 63, w = __builtin_amdgcn_readfirstlane(tid >> 6);
    const int xg = blockIdx.x & 7, j = blockIdx.x >> 3, h = xg * 4 + (j >> 3), qb = j & 7;
    f32x16 o; float lsum;
    volatile LAS unsigned* flag = (volatile LAS unsigned*)(size_t)(lds0 + LDS_FLAG);
    if (tid == 0) *flag = 0u;
    const bool ok = attn_pass<false>(a, lds0, h, qb, w, lane, o, lsum);
    if (!ok) *flag = 1u;
    asm volatile("s_waitcnt lgkmcnt(0)\n\ts_barrier" ::: "memory");
    if (__builtin_amdgcn_readfirstlane(*flag)) {
        attn_pass<true>(a, lds0, h, qb, w, lane, o, lsum);
    }
    const int r = lane & 31, hh = lane >> 5;
    const int row = qb * 256 + w * 32 + r;
    const float sc = a.g[row] / lsum;
    unsigned pk[4][2];
#pragma unroll
    for (int G = 0; G < 4; ++G) { pk[G][0] = cvtpk(o[4 * G] * sc, o[4 * G + 1] * sc); pk[G][1] = cvtpk(o[4 * G + 2] * sc, o[4 * G + 3] * sc); }
    bf16_t* op = a.o + (size_t)row * HO_LD + h * 32 + hh * 8;
#pragma unroll
    for (int G = 0; G < 4; G += 2) {
        auto r0 = __builtin_amdgcn_permlane32_swap(pk[G][0], pk[G + 1][0], false, false);
        auto r1 = __builtin_amdgcn_permlane32_swap(pk[G][1], pk[G + 1][1], false, false);
        *(u32x4*)(op + G * 8) = (u32x4){r0[0], r1[0], r0[1], r1[1]};
    }
    KSTAMP(1, 3);
}
}

namespace k3 {
#define K3_MAP 1
#define K3_NODMA 0
constexpr int NS = 5, STAGE = 32768, NKT = 36;
constexpr int LDS_BYTES = NS * STAGE;
struct Args { const bf16_t* ho; const bf16_t* wd; const bf16_t* wo; const float* g; float* xch; unsigned* flags; bf16_t* out; };

DI void st16_sc1(void* p, f32x4 v) { asm volatile("global_store_dwordx4 %0, %1, off sc1\n\ts_nop 1" :: "v"(p), "v"(v) : "memory"); }

DI void body(const Args& a, char* smem) {
    const unsigned lds0 = (unsigned)(uintptr_t)smem;
    const int tid = threadIdx.x, lane = tid & 63, w = __builtin_amdgcn_readfirstlane(tid >> 6);
    const int xg = blockIdx.x & 7, j = blockIdx.x >> 3;
    const int kh = K3_MAP == 0 ? (j & 1) : (xg & 1);
    const int rb = K3_MAP == 0 ? (xg >> 1) * 4 + ((j >> 1) & 3) : K3_MAP == 1 ? (xg >> 2) * 8 + (j & 7) : (xg >> 1) * 4 + (j & 3);
    const int cb = K3_MAP == 0 ? (xg & 1) * 4 + (j >> 3) : K3_MAP == 1 ? ((xg >> 1) & 1) * 4 + (j >> 3) : (j >> 2);
    const int tile = rb * 8 + cb;
    const int nH = kh ? 20 : 36;
    if (w >= 4) {
        const int pw = w - 4;
        const unsigned voffA = (unsigned)((lane >> 3) * (HO_LD * 2) + (((lane & 7) ^ (lane >> 3)) * 16));
        const unsigned voffB0 = (unsigned)((lane >> 4) * 2048 + (((lane & 15) ^ ((lane >> 4) << 1)) * 16));
        const unsigned voffB1 = (unsigned)((lane >> 4) * 2048 + (((lane & 15) ^ (((lane >> 4) | 4) << 1)) * 16));
        const char* gA = (const char*)a.ho + (size_t)(rb * 128 + pw * 32) * (HO_LD * 2) + (kh ? 2304 * 2 : 0);
        const char* gWd = (const char*)a.wd + (size_t)((kh ? 2304 : 0) + pw * 16) * 2048 + cb * 256;
        const char* gWo = (const char*)a.wo + (size_t)(pw * 16) * 2048 + cb * 256 - (size_t)nH * (64 * 2048);
        const unsigned dA = lds0 + pw * 4096, dB = lds0 + 16384 + pw * 4096;
        auto issue = [&](int t, unsigned so) {
            if (K3_NODMA && t >= NS) return;
            const char* sa = gA + (size_t)t * 128; const char* sb = (t < nH ? gWd : gWo) + (size_t)t * (64 * 2048);
#pragma unroll
            for (int i = 0; i < 4; ++i) glds16(voffA, sa + (size_t)i * 8 * (HO_LD * 2), dA + so + i * 1024);
#pragma unroll
            for (int i = 0; i < 4; ++i) glds16((i & 2) ? voffB1 : voffB0, sb + i * 4 * 2048, dB + so + i * 1024); };
#pragma unroll
        for (int t = 0; t < NS - 1; ++t) issue(t, t * STAGE);
        unsigned so = (NS - 1) * STAGE;
        for (int t = 0; t < NKT - (NS - 2); ++t) {
            asm volatile("s_waitcnt vmcnt(24)\n\ts_barrier" ::: "memory");
            if (t + NS - 1 < NKT) issue(t + NS - 1, so);
            so = (so == (NS - 1) * STAGE) ? 0u : so + STAGE;
        }
        asm volatile("s_waitcnt vmcnt(16)\n\ts_barrier" ::: "memory");
        asm volatile("s_waitcnt vmcnt(8)\n\ts_barrier" ::: "memory");
        asm volatile("s_waitcnt vmcnt(0)\n\ts_barrier" ::: "memory");
        asm volatile("s_barrier" ::: "memory");
        return;
    }
    const int wr = w & 1, wc = w >> 1, fr = lane & 15, fq = lane >> 4;
    const int q = (lane & 15) >> 2, p4 = lane & 3, sR = q | ((fq & 1) << 2);
    unsigned rA[3][2], rB[3][4];
#pragma unroll
    for (int sp = 0; sp < 3; ++sp) {
#pragma unroll
        for (int ks = 0; ks < 2; ++ks) { rA[sp][ks] = lds0 + sp * 2 * STAGE + (wr * 64 + fr) * 128 + (((fq + 4 * ks) ^ (fr & 7)) * 16); asm volatile("" : "+v"(rA[sp][ks])); }
#pragma unroll
        for (int n = 0; n < 4; ++n) { rB[sp][n] = lds0 + sp * 2 * STAGE + 16384 + (((8 * fq + q) * 256 + 32 * sR + 8 * p4) ^ ((wc * 4 + n) << 5)); asm volatile("" : "+v"(rB[sp][n])); }
    }
    f32x4 acc[4][4];
#pragma unroll
    for (int m = 0; m < 4; ++m)
#pragma unroll
        for (int n = 0; n < 4; ++n) acc[m][n] = (f32x4){0.f, 0.f, 0.f, 0.f};
    bf16x8 Af[2][4]; s16x4 Bh[2][4][2];
#define LP(x) ((const LAS char*)(size_t)(x))
#define K3_RD(buf, i, SL, ks) do { if ((i) < 4) Af[buf][i] = ld128(LP(rA[(SL) >> 1][ks]) + ((SL) & 1) * STAGE + (i) * 2048); \
        else Bh[buf][((i) - 4) >> 1][((i) - 4) & 1] = ldtr(LP(rB[(SL) >> 1][((i) - 4) >> 1]) + ((SL) & 1) * STAGE + (ks) * 8192 + (((i) - 4) & 1) * 1024); } while (0)
    const int row0 = rb * 128 + wr * 64 + fr;
    float omg[4];
#pragma unroll
    for (int m = 0; m < 4; ++m) omg[m] = 1.0f - a.g[row0 + m * 16];
    asm volatile("s_barrier" ::: "memory");
    KSTAMP(2, 1);
#pragma unroll
    for (int i = 0; i < 12; ++i) K3_RD(0, i, 0, 0);
#define K3_TILE(t, SL, MORE) do { \
        _Pragma("unroll") for (int i = 0; i < 16; ++i) { \
            const int n = i >> 2, m = i & 3; \
            MFMA16A(acc[m][n], cat8(Bh[0][n][0], Bh[0][n][1]), Af[0][m]); \
            SBAR(); \
            if (i < 12) K3_RD(1, i, SL, 1); \
            SBAR(); } \
        if (MORE) asm volatile("s_waitcnt lgkmcnt(0)\n\ts_barrier" ::: "memory");         \
        SBAR(); \
        _Pragma("unroll") for (int i = 0; i < 16; ++i) { \
            const int n = i >> 2, m = i & 3; \
            MFMA16A(acc[m][n], cat8(Bh[1][n][0], Bh[1][n][1]), Af[1][m]); \
            SBAR(); \
            if (i < 12 && (MORE)) K3_RD(0, i, ((SL) + 1) % NS, 0); \
            SBAR(); } \
        if ((t) + 1 == nH) { asm volatile("s_nop 7\n\ts_nop 7" ::: "memory"); SBAR(); _Pragma("unroll") for (int m = 0; m < 4; ++m) _Pragma("unroll") for (int n = 0; n < 4; ++n) { acc[m][n] *= omg[m]; asm volatile("" : "+v"(acc[m][n])); } \
            asm volatile("s_nop 3" ::: "memory"); SBAR(); } \
    } while (0)
    for (int t = 0; t < 35; t += 5) { K3_TILE(t, 0, true); K3_TILE(t + 1, 1, true); K3_TILE(t + 2, 2, true); K3_TILE(t + 3, 3, true); K3_TILE(t + 4, 4, true); }
    K3_TILE(35, 0, false);
    asm volatile("s_nop 7\n\ts_nop 7" ::: "memory"); SBAR();
    KSTAMP(2, 2);
    if (wc != kh) {
        float* xs = a.xch + ((size_t)(tile * 2 + kh) * 2 + wr) * 4096 + lane * 4;
#pragma unroll
        for (int m = 0; m < 4; ++m)
#pragma unroll
            for (int n = 0; n < 4; ++n) st16_sc1(xs + (m * 4 + n) * 256, acc[m][n]);
        asm volatile("s_waitcnt vmcnt(0)" ::: "memory");
    }
    asm volatile("s_barrier" ::: "memory");
    if (tid == 0) __hip_atomic_store(a.flags + (tile * 2 + kh) * 32, 1u, __ATOMIC_RELAXED, __HIP_MEMORY_SCOPE_AGENT);
    if (wc == kh) {
        const unsigned* pf = a.flags + (tile * 2 + (kh ^ 1)) * 32;
        unsigned spins = 0;
        while (__builtin_amdgcn_readfirstlane(__hip_atomic_load(pf, __ATOMIC_RELAXED, __HIP_MEMORY_SCOPE_AGENT)) == 0u) { __builtin_amdgcn_s_sleep(1); if (++spins > (1u << 22)) break; }
        asm volatile("" ::: "memory");
        const float* xr = a.xch + ((size_t)(tile * 2 + (kh ^ 1)) * 2 + wr) * 4096 + lane * 4;
        f32x4 pv[4][4];
#pragma unroll
        for (int m = 0; m < 4; ++m)
#pragma unroll
            for (int n = 0; n < 4; ++n) asm volatile("global_load_dwordx4 %0, %1, off sc1" : "=v"(pv[m][n]) : "v"(xr + (m * 4 + n) * 256) : "memory");
        asm volatile("s_waitcnt vmcnt(0)" : "+v"(pv[0][0]), "+v"(pv[0][1]), "+v"(pv[0][2]), "+v"(pv[0][3]), "+v"(pv[1][0]), "+v"(pv[1][1]), "+v"(pv[1][2]), "+v"(pv[1][3]),
                     "+v"(pv[2][0]), "+v"(pv[2][1]), "+v"(pv[2][2]), "+v"(pv[2][3]), "+v"(pv[3][0]), "+v"(pv[3][1]), "+v"(pv[3][2]), "+v"(pv[3][3]) :: "memory");
        const int cperm = ((fq & 1) << 4) | ((fq >> 1) << 3);
        bf16_t* ob = a.out + (size_t)row0 * D_ + cb * 128 + wc * 64 + cperm;
#pragma unroll
        for (int m = 0; m < 4; ++m)
#pragma unroll
            for (int n = 0; n < 4; n += 2) {
                const f32x4 lo = acc[m][n] + pv[m][n], hi = acc[m][n + 1] + pv[m][n + 1];
                auto r0 = __builtin_amdgcn_permlane16_swap(cvtpk(lo[0], lo[1]), cvtpk(hi[0], hi[1]), false, false);
                auto r1 = __builtin_amdgcn_permlane16_swap(cvtpk(lo[2], lo[3]), cvtpk(hi[2], hi[3]), false, false);
                *(u32x4*)(ob + (size_t)m * 16 * D_ + n * 16) = (u32x4){r0[0], r1[0], r0[1], r1[1]};
            }
    }
    KSTAMP(2, 3);
#undef K3_RD
#undef K3_TILE
}
}

__global__ __launch_bounds__(512, 2) void k1_kernel(k1::Args a, unsigned* flags) {
    extern __shared__ __attribute__((aligned(16))) char smem[];
    if (threadIdx.x == 0) flags[blockIdx.x * 32] = 0u;
    KSTAMP(0, 0);
    k1::body<3, 0>(a, smem);
}
__global__ __launch_bounds__(512, 2) void k2_kernel(k2::Args a) {
    extern __shared__ __attribute__((aligned(16))) char smem[];
    KSTAMP(1, 0);
    k2::body(a, smem);
}
__global__ __launch_bounds__(512, 2) void k3_kernel(k3::Args a) {
    extern __shared__ __attribute__((aligned(16))) char smem[];
    KSTAMP(2, 0);
    k3::body(a, smem);
}
extern "C" void kernel_launch(void* const* d_in, const int* in_sizes, int n_in, void* d_out, int out_size, void* d_ws, size_t ws_size, hipStream_t stream) {
    char* ws = (char*)d_ws;
    bf16_t* qkv = (bf16_t*)ws; bf16_t* h = (bf16_t*)(ws + (16 << 20)); float* g = (float*)(ws + (36 << 20)); bf16_t* o = h + F_;
    float* xch = (float*)(ws + (40 << 20)); unsigned* flags = (unsigned*)(ws + (50 << 20));
    k1::Args a1; a1.x = (const bf16_t*)d_in[0]; a1.wqkv = (const bf16_t*)d_in[1]; a1.wg = (const bf16_t*)d_in[4]; a1.wu = (const bf16_t*)d_in[5]; a1.gw = (const bf16_t*)d_in[7];
    a1.qkv = qkv; a1.h = h; a1.g = g;
    k2::Args a2; a2.qkv = qkv; a2.g = g; a2.o = o;
    k3::Args a3; a3.ho = h; a3.wd = (const bf16_t*)d_in[6]; a3.wo = (const bf16_t*)d_in[2]; a3.g = g; a3.xch = xch; a3.flags = flags; a3.out = (bf16_t*)d_out;
    (void)hipFuncSetAttribute((const void*)k1_kernel, hipFuncAttributeMaxDynamicSharedMemorySize, k1::LDS_BYTES);
    (void)hipFuncSetAttribute((const void*)k2_kernel, hipFuncAttributeMaxDynamicSharedMemorySize, k2::LDS_BYTES);
    (void)hipFuncSetAttribute((const void*)k3_kernel, hipFuncAttributeMaxDynamicSharedMemorySize, k3::LDS_BYTES);
#define ONLY 7
    if (ONLY & 1) k1_kernel<<<256, 512, k1::LDS_BYTES, stream>>>(a1, flags);
    if (ONLY & 2) k2_kernel<<<256, 512, k2::LDS_BYTES, stream>>>(a2);
    if (ONLY & 4) k3_kernel<<<256, 512, k3::LDS_BYTES, stream>>>(a3);
}
```

```cpp
#include <hip/hip_runtime.h>
#include <hip/hip_runtime.h>
#include <stdint.h>
#define LAS __attribute__((address_space(3)))
#define DI __device__ __forceinline__
typedef unsigned short bf16_t;
typedef short bf16x8 __attribute__((ext_vector_type(8)));
typedef short s16x4 __attribute__((ext_vector_type(4)));
typedef short v4i16_t __attribute__((ext_vector_type(4)));
typedef float f32x4 __attribute__((ext_vector_type(4)));
typedef float f32x16 __attribute__((ext_vector_type(16)));
typedef unsigned u32x4 __attribute__((ext_vector_type(4)));
typedef unsigned u32x2 __attribute__((ext_vector_type(2)));
typedef float f32x2 __attribute__((ext_vector_type(2)));
typedef __bf16 bf16x2_t __attribute__((ext_vector_type(2)));
#define SBAR() __builtin_amdgcn_sched_barrier(0)

constexpr int S_ = 2048, D_ = 1024, H_ = 32, DH_ = 32, F_ = 3584, HO_LD = F_ + D_;

DI unsigned cvtpk(float lo, float hi) { f32x2 v = {lo, hi}; bf16x2_t b = __builtin_convertvector(v, bf16x2_t); return __builtin_bit_cast(unsigned, b); }
DI float bf2f(unsigned short b) { return __uint_as_float(((unsigned)b) << 16); }
DI void glds16(unsigned voff, const void* sbase, unsigned lds_dst) {
    asm volatile("s_mov_b32 m0, %2\n\ts_nop 0\n\tglobal_load_lds_dwordx4 %0, %1" :: "v"(voff), "s"(sbase), "s"(lds_dst) : "memory");
}
#define ST_POLICY 0
DI void st16g(void* p, u32x4 v) {
    *(u32x4*)p = v;
}
DI void st8g(void* p, u32x2 v) {
    *(u32x2*)p = v;
}
DI s16x4 ldtr(const LAS char* p) { return __builtin_bit_cast(s16x4, __builtin_amdgcn_ds_read_tr16_b64_v4i16((LAS v4i16_t*)p)); }
DI bf16x8 ld128(const LAS char* p) { return *(const LAS bf16x8*)p; }
DI bf16x8 cat8(s16x4 lo, s16x4 hi) { return __builtin_shufflevector(lo, hi, 0, 1, 2, 3, 4, 5, 6, 7); }
#define MFMA16(a, b, c) __builtin_amdgcn_mfma_f32_16x16x32_bf16(a, b, c, 0, 0, 0)
#define MFMA16A(acc, a, b) asm volatile("v_mfma_f32_16x16x32_bf16 %0, %1, %2, %0" : "+v"(acc) : "v"(a), "v"(b))
#define MFMA32(a, b, c) __builtin_amdgcn_mfma_f32_32x32x16_bf16(a, b, c, 0, 0, 0)

#define ABL_NODMA 0
#define ABL_NOLDS 0
#define ABL_NOBAR 0
#define K1_WSTAMP0()
#define K1_WSTAMP1()
#define K1_TRACE(t)
#define KSTAMP(k, i)
#define K1_STAMP(i)
namespace k1 {
constexpr int A_STAGE = 32768, B_STAGE = 49152, B_BASE = 65536, LDS_BYTES = 163840;
constexpr int NT = D_ / 64;
constexpr float QSCALE = 0.17677669529663687f * 1.4426950408889634f;

struct Args { const bf16_t* x; const bf16_t* wqkv; const bf16_t* wg; const bf16_t* wu; const bf16_t* gw; bf16_t* qkv; bf16_t* h; float* g; };

DI void epilogue(const Args& a, f32x4 (&acc)[4][10], const int lane, const int w, const int rb, const int ct);
template <int PD, int DPS>
DI void body(const Args& a, char* smem) {
    const unsigned lds0 = (unsigned)(uintptr_t)smem;
    const int tid = threadIdx.x, lane = tid & 63, w = __builtin_amdgcn_readfirstlane(tid >> 6);
    const int wr = w & 3, wc = w >> 2;
    const int xg = blockIdx.x & 7, j = blockIdx.x >> 3, rb = j & 7, ct = xg * 4 + (j >> 3);
    const unsigned voffA = (unsigned)((lane >> 3) * 2048 + (((lane & 7) ^ (lane >> 3)) * 16));
    const int sB = (lane >> 4) | (((w >> 1) & 1) << 2);
    int chq = (lane & 15) ^ (sB << 1), chg = chq;
    if (chq >= 12) chq -= 4;
    if (chg >= 14) chg -= 2;
    const unsigned voffQ = (unsigned)((lane >> 4) * 6144 + chq * 16), voffG = (unsigned)((lane >> 4) * 7168 + chg * 16);
    const char* gA = (const char*)a.x + (size_t)(rb * 256 + w * 32) * 2048;
    const char* gQ = (const char*)a.wqkv + (size_t)(w * 4) * 6144 + ct * 192;
    const char* gG = (const char*)a.wg + (size_t)(w * 4) * 7168 + ct * 224;
    const char* gU = (const char*)a.wu + (size_t)(w * 4) * 7168 + ct * 224;
    const unsigned dA = lds0 + w * 4096, dB = lds0 + B_BASE + w * 1024;
#define K1_DMA(p, t, st) do { if (ABL_NODMA && (t) > 1) break; \
        if ((p) < 4) glds16(voffA, gA + (size_t)(t) * 128 + (p) * 8 * 2048, dA + (st) * A_STAGE + (p) * 1024); \
        else if ((p) == 4 || (p) == 7) glds16(voffQ, gQ + (size_t)(t) * (64 * 6144) + ((p) == 7 ? 32 * 6144 : 0), dB + (st) * B_STAGE + ((p) == 7 ? 8192 : 0)); \
        else if ((p) == 5 || (p) == 8) glds16(voffG, gG + (size_t)(t) * (64 * 7168) + ((p) == 8 ? 32 * 7168 : 0), dB + (st) * B_STAGE + 16384 + ((p) == 8 ? 8192 : 0)); \
        else glds16(voffG, gU + (size_t)(t) * (64 * 7168) + ((p) == 9 ? 32 * 7168 : 0), dB + (st) * B_STAGE + 32768 + ((p) == 9 ? 8192 : 0)); } while (0)
    const int fr = lane & 15, fq = lane >> 4;
    const unsigned rA0 = lds0 + (wr * 64 + fr) * 128 + ((fq ^ (fr & 7)) * 16);
    const unsigned rA1 = lds0 + (wr * 64 + fr) * 128 + (((fq + 4) ^ (fr & 7)) * 16);
    const int q = (lane & 15) >> 2, p = lane & 3, sR = q | ((fq & 1) << 2);
    const int bbase = (8 * fq + q) * 256 + 32 * sR + 8 * p;
    unsigned rB[6];
#pragma unroll
    for (int jj = 0; jj < 4; ++jj) rB[jj] = lds0 + B_BASE + (bbase ^ ((3 * wc + jj) << 5));
    rB[4] = wc ? rB[3] + 16384 : rB[0];
    rB[5] = wc ? rB[3] + 32768 : rB[3];
    f32x4 acc[4][10];
#pragma unroll
    for (int m = 0; m < 4; ++m)
#pragma unroll
        for (int n = 0; n < 10; ++n) acc[m][n] = (f32x4){0.f, 0.f, 0.f, 0.f};
    bf16x8 Af[2][4];
    bf16x8 Bf[PD + 1];
#define LP(x) ((const LAS char*)(size_t)(x))
#define K1_BADDR(n) ((n) < 6 ? LP(rB[(n) >> 1]) + (((n) & 1) ? 32768 : 16384) : (n) == 6 ? LP(rB[1]) : (n) == 7 ? LP(rB[2]) : (n) == 8 ? LP(rB[4]) : LP(rB[5]))
#define K1_LDB(dst, n, ks) do { if (ABL_NOLDS && ablon) { asm volatile("" : "+v"(dst)); break; } const LAS char* _p = K1_BADDR(n) + (ks) * 8192; dst = cat8(ldtr(_p), ldtr(_p + 1024)); } while (0)
#define K1_LDA(m, ks, so) do { if (ABL_NOLDS && ablon) { asm volatile("" : "+v"(Af[ks][m])); break; } Af[ks][m] = ld128(LP((ks) ? rA1 : rA0) + (so) + (m) * 2048); } while (0)
#define K1_TOGGLE(CUR) do { _Pragma("unroll") for (int jj = 0; jj < 6; ++jj) { rB[jj] += (CUR) ? -B_STAGE : B_STAGE; asm volatile("" : "+v"(rB[jj])); } } while (0)
#define K1_SYNC(N) do { K1_WSTAMP0(); \
        if (ABL_NOBAR) asm volatile("s_waitcnt vmcnt(" #N ") lgkmcnt(0)" ::: "memory"); else asm volatile("s_waitcnt vmcnt(" #N ") lgkmcnt(0)\n\ts_barrier" ::: "memory"); \
        K1_WSTAMP1(); } while (0)

    bool ablon = false; (void)ablon; long long wsum = 0, wt0 = 0; (void)wsum; (void)wt0;
#pragma unroll
    for (int pc = 0; pc < 10; ++pc) K1_DMA(pc, 0, 0);
#pragma unroll
    for (int pc = 0; pc < 10; ++pc) K1_DMA(pc, 1, 1);
    asm volatile("s_waitcnt vmcnt(13)\n\ts_barrier" ::: "memory");
    K1_STAMP(1);
#pragma unroll
    for (int m = 0; m < 4; ++m) K1_LDA(m, 0, 0);
#pragma unroll
    for (int s = 0; s < PD; ++s) K1_LDB(Bf[s], s, 0);

    constexpr int SM = 10 - PD, SE = 20 - PD;
#define K1_W1SLOT(r) ((r) == 0 ? 0 : (r) == 1 ? 1 : (r) == 3 ? 2 : (r) == 4 ? 3 : (r) == 6 ? 4 : (r) == 7 ? 5 : (r) == 8 ? 6 : -1)
#define K1_W2SLOT(r) ((r) == 0 ? 7 : (r) == 3 ? 8 : (r) == 6 ? 9 : -1)
#define K1_ISSUE(s, t, CUR, W1ON, W2ON, W2PREV, SH) do { \
            if ((W1ON) && (s) >= SM && (s) < SE) { const int pc = K1_W1SLOT((s) - SM - (SH)); if (pc >= 0) K1_DMA(pc, (t) + 2, (CUR)); } \
            if ((W2ON) && (s) >= SE) { const int pc = K1_W2SLOT((s) - SE - (SH)); if (pc >= 0) K1_DMA(pc, (t) + 2, (CUR)); } \
            if ((W2PREV) && (s) < SM) { const int pc = K1_W2SLOT((s) + 20 - SE - (SH)); if (pc >= 0) K1_DMA(pc, (t) + 1, 1 - (CUR)); } } while (0)
#define K1_TILE(t, CUR, VM_M, VM_E, LAST, W1ON, W2ON, W2PREV) do { K1_TRACE(t); \
        _Pragma("unroll") for (int s = 0; s < 20; ++s) { \
            const int ks = s / 10, n = s % 10; \
            if (s == SM) K1_SYNC(VM_M); \
            if (s == SE && !(LAST)) { K1_SYNC(VM_E); K1_TOGGLE(CUR); } \
            { const int sp = s + PD; \
              if (sp < 20) K1_LDB(Bf[sp % (PD + 1)], sp % 10, sp / 10); \
              else if (!(LAST)) K1_LDB(Bf[sp % (PD + 1)], sp - 20, 0); } \
            if (s < SM && s >= SM - 7 && ((s - (SM - 7)) & 1) == 0) K1_LDA((s - (SM - 7)) / 2, 1, (CUR) * A_STAGE); \
            if (!(LAST) && s >= SE) { \
                if (s == SE) { K1_LDA(0, 0, (1 - (CUR)) * A_STAGE); K1_LDA(1, 0, (1 - (CUR)) * A_STAGE); } \
                if (s == SE + 1) K1_LDA(2, 0, (1 - (CUR)) * A_STAGE); \
                if (s == SE + 2) K1_LDA(3, 0, (1 - (CUR)) * A_STAGE); } \
            if (DPS == 1) { if (wc) K1_ISSUE(s, t, CUR, W1ON, W2ON, W2PREV, 1); else K1_ISSUE(s, t, CUR, W1ON, W2ON, W2PREV, 0); } \
            else K1_ISSUE(s, t, CUR, W1ON, W2ON, W2PREV, 0); \
            SBAR(); \
            _Pragma("unroll") for (int m = 0; m < 4; ++m) acc[m][n] = MFMA16(Bf[s % (PD + 1)], Af[ks][m], acc[m][n]); \
            SBAR(); \
        } } while (0)
    static_assert(20 % (PD + 1) == 0, "ring index must be static across tiles");
    static_assert(PD == 3, "schedule tables assume PD == 3");
    K1_TILE(0, 0, 10, 10, false, true, true, false);
    ablon = true;
    for (int t = 1; t < NT - 3; t += 2) {
        K1_TILE(t, 1, 10, 10, false, true, true, true);
        K1_TILE(t + 1, 0, 10, 10, false, true, true, true);
    }
    K1_TILE(NT - 3, 1, 10, 10, false, true, true, true);
    K1_TILE(NT - 2, 0, 10, 3, false, false, false, true);
    K1_TILE(NT - 1, 1, 0, 0, true, false, false, false);
    K1_STAMP(2); K1_TRACE(16);

    int lane2; asm volatile("v_mbcnt_lo_u32_b32 %0, -1, 0\n\tv_mbcnt_hi_u32_b32 %0, -1, %0" : "=v"(lane2));
    epilogue(a, acc, lane2, w, rb, ct);
}
DI void epilogue(const Args& a, f32x4 (&acc)[4][10], const int lane, const int w, const int rb, const int ct) {
    const int wr = w & 3, wc = w >> 2, fr = lane & 15, fq = lane >> 4;
    const int grow = rb * 256 + ct * 8 + w;
    u32x4 gx0, gx1, gg0, gg1;
    { const u32x4* xr = (const u32x4*)(a.x + (size_t)grow * D_) + lane * 2; const u32x4* gr = (const u32x4*)(a.gw) + lane * 2;
      gx0 = xr[0]; gx1 = xr[1]; gg0 = gr[0]; gg1 = gr[1]; }

    const int row0 = rb * 256 + wr * 64 + fr;
    const int cperm = ((fq & 1) << 4) | ((fq >> 1) << 3);
    auto swiglu = [&](int m, int ng, unsigned (&o)[2]) {
        float hv[4];
#pragma unroll
        for (int e = 0; e < 4; ++e) { const float gt = acc[m][ng][e], up = acc[m][ng + 1][e]; hv[e] = gt * up * __builtin_amdgcn_rcpf(1.0f + __builtin_amdgcn_exp2f(-1.4426950408889634f * gt)); }
        o[0] = cvtpk(hv[0], hv[1]); o[1] = cvtpk(hv[2], hv[3]);
    };
    {
        bf16_t* hb = a.h + (size_t)row0 * HO_LD + ct * 112 + wc * 48;
#pragma unroll
        for (int m = 0; m < 4; ++m) {
            unsigned p0[2], p1[2], p2[2], p3[2];
            swiglu(m, 0, p0); swiglu(m, 2, p1); swiglu(m, 4, p2);
            bf16_t* hp = hb + (size_t)m * 16 * HO_LD;
            {   auto r0 = __builtin_amdgcn_permlane16_swap(p0[0], p1[0], false, false);
                auto r1 = __builtin_amdgcn_permlane16_swap(p0[1], p1[1], false, false);
                st16g(hp + cperm, (u32x4){r0[0], r1[0], r0[1], r1[1]}); }
            if (wc) {
                swiglu(m, 8, p3);
                auto r0 = __builtin_amdgcn_permlane16_swap(p2[0], p3[0], false, false);
                auto r1 = __builtin_amdgcn_permlane16_swap(p2[1], p3[1], false, false);
                st16g(hp + 32 + cperm, (u32x4){r0[0], r1[0], r0[1], r1[1]});
            } else {
                st8g(hp + 32 + 4 * fq, (u32x2){p2[0], p2[1]});
            }
        }
    }
    {
        auto put = [&](int slot, int nlo, int nhi) {
            const float sc = slot < 32 ? QSCALE : 1.0f;
            bf16_t* qb = a.qkv + (size_t)slot * (S_ * 32) + (size_t)row0 * 32 + cperm;
#pragma unroll
            for (int m = 0; m < 4; ++m) {
                const f32x4 lo = acc[m][nlo] * sc, hi = acc[m][nhi] * sc;
                auto r0 = __builtin_amdgcn_permlane16_swap(cvtpk(lo[0], lo[1]), cvtpk(hi[0], hi[1]), false, false);
                auto r1 = __builtin_amdgcn_permlane16_swap(cvtpk(lo[2], lo[3]), cvtpk(hi[2], hi[3]), false, false);
                st16g(qb + m * 16 * 32, (u32x4){r0[0], r1[0], r0[1], r1[1]});
            } };
        if (wc) put(3 * ct + 2, 6, 7);
        else { put(3 * ct, 8, 6); put(3 * ct + 1, 7, 9); }
    }
    {
        float s = 0.f;
#pragma unroll
        for (int i = 0; i < 4; ++i) {
            s += __uint_as_float(gx0[i] << 16) * __uint_as_float(gg0[i] << 16) + __uint_as_float(gx0[i] & 0xffff0000u) * __uint_as_float(gg0[i] & 0xffff0000u);
            s += __uint_as_float(gx1[i] << 16) * __uint_as_float(gg1[i] << 16) + __uint_as_float(gx1[i] & 0xffff0000u) * __uint_as_float(gg1[i] & 0xffff0000u);
        }
#pragma unroll
        for (int o = 32; o >= 1; o >>= 1) s += __shfl_xor(s, o);
        if (lane == 0) a.g[grow] = 1.0f / (1.0f + __expf(-s));
    }
    K1_STAMP(3); K1_TRACE(17);
#undef K1_DMA
#undef K1_BADDR
#undef K1_LDB
#undef K1_LDA
#undef K1_TILE
#undef K1_TOGGLE
}
}

namespace k2 {
constexpr int SLOT = 16384, LDS_FLAG = 4 * SLOT, LDS_BYTES = 4 * SLOT + 16, NTILE = S_ / 128;
struct Args { const bf16_t* qkv; const float* g; bf16_t* o; const bf16_t* wd; const bf16_t* wo; };

DI unsigned pk2(float lo, float hi) { return cvtpk(lo, hi); }

template <bool SAFE>
DI bool attn_pass(const Args& a, const unsigned lds0, const int h, const int qb, const int w, const int lane, f32x16& o, float& lsum) {
    const int r = lane & 31, hh = lane >> 5;
    const char* gK = (const char*)(a.qkv + (size_t)(32 + h) * (S_ * 32)) + w * 1024;
    const char* gV = (const char*)(a.qkv + (size_t)(64 + h) * (S_ * 32)) + w * 1024;
    const unsigned voffK = (unsigned)((lane >> 2) * 64 + (((lane & 3) ^ ((lane >> 4) & 3)) * 16)), voffV = (unsigned)(lane * 16);
    const unsigned dK = lds0 + w * 1024, dV = lds0 + 8192 + w * 1024;
#define K2_DMA(T) do { glds16(voffK, gK + (size_t)(T) * 8192, dK + ((T) & 3) * SLOT); glds16(voffV, gV + (size_t)(T) * 8192, dV + ((T) & 3) * SLOT); } while (0)
    K2_DMA(0); K2_DMA(1); K2_DMA(2);
    const bf16_t* qp = a.qkv + (size_t)h * (S_ * 32) + (size_t)(qb * 256 + w * 32 + r) * 32 + hh * 8;
    const bf16x8 q0 = *(const bf16x8*)qp, q1 = *(const bf16x8*)(qp + 16);
    const unsigned rK0 = lds0 + r * 64 + (((0 + hh) ^ ((r >> 2) & 3)) * 16), rK1 = lds0 + r * 64 + (((2 + hh) ^ ((r >> 2) & 3)) * 16);
    const unsigned rV = lds0 + 8192 + (4 * hh + ((lane & 15) >> 2)) * 64 + ((lane >> 4) & 1) * 32 + (lane & 3) * 8;
    const short one = (short)0x3f80; const bool sel = ((lane & 15) == ((lane >> 4) & 1));
    const short sv = sel ? one : (short)0; const bf16x8 selA = {sv, sv, sv, sv, sv, sv, sv, sv};
    f32x4 ls = {0.f, 0.f, 0.f, 0.f};
#pragma unroll
    for (int i = 0; i < 16; ++i) o[i] = 0.f;
#define LP(x) ((const LAS char*)(size_t)(x))
#define K2_LDK(k0, k1, off) do { k0 = ld128(LP(rK0) + (off)); k1 = ld128(LP(rK1) + (off)); } while (0)
#define K2_LDV(v0, v1, off) do { v0 = cat8(ldtr(LP(rV) + (off)), ldtr(LP(rV) + (off) + 512)); v1 = cat8(ldtr(LP(rV) + (off) + 1024), ldtr(LP(rV) + (off) + 1536)); } while (0)
    asm volatile("s_waitcnt vmcnt(4)\n\ts_barrier" ::: "memory");
    if constexpr (!SAFE) {
        KSTAMP(1, 1);
        bf16x8 k0, k1, v0, v1;
        u32x4 p0, p1;
        f32x16 negm, sA, sB;
        K2_LDK(k0, k1, 0);
        {
            f32x16 z;
#pragma unroll
            for (int i = 0; i < 16; ++i) z[i] = 0.f;
            f32x16 s = MFMA32(k0, q0, z); s = MFMA32(k1, q1, s);
            float m = s[0];
#pragma unroll
            for (int i = 1; i < 16; ++i) m = __builtin_fmaxf(m, s[i]);
            auto rr = __builtin_amdgcn_permlane32_swap(__float_as_uint(m), __float_as_uint(m), false, false);
            m = __builtin_fmaxf(__uint_as_float(rr[0]), __uint_as_float(rr[1]));
#pragma unroll
            for (int i = 0; i < 16; ++i) negm[i] = -m;
            asm volatile("" : "+v"(negm));
        }
        sA = MFMA32(k0, q0, negm); sA = MFMA32(k1, q1, sA);
        K2_LDK(k0, k1, 2048);
        v0 = cat8(ldtr(LP(rV)), ldtr(LP(rV) + 512));
        p1 = (u32x4){0u, 0u, 0u, 0u}; v1 = v0;
        SBAR();
#define EX(C, i) C[i] = __builtin_amdgcn_exp2f(C[i])
#define CV(dst, C, i) dst = pk2(C[i], C[(i) + 1])
#define PIN4(x) asm volatile("" : "+v"(x))
#define K2_STEP(CUR, NXT, voff, koff, vnoff, DOQK, LOADK, LOADV0, SYNC) do { \
            EX(CUR, 0); EX(CUR, 1); SBAR(); \
            o = MFMA32(v1, __builtin_bit_cast(bf16x8, p1), o); SBAR(); \
            EX(CUR, 2); ls = MFMA16(selA, __builtin_bit_cast(bf16x8, p1), ls); SBAR(); \
            v1 = cat8(ldtr(LP(rV) + (voff) + 1024), ldtr(LP(rV) + (voff) + 1536)); \
            EX(CUR, 3); CV(p0[0], CUR, 0); EX(CUR, 4); EX(CUR, 5); SBAR(); \
            if (DOQK) NXT = MFMA32(k0, q0, negm); \
            SBAR(); \
            CV(p0[1], CUR, 2); EX(CUR, 6); EX(CUR, 7); CV(p0[2], CUR, 4); SBAR(); \
            if (DOQK) NXT = MFMA32(k1, q1, NXT); \
            SBAR(); \
            SYNC; \
            if (LOADK) K2_LDK(k0, k1, koff); \
            EX(CUR, 8); CV(p0[3], CUR, 6); EX(CUR, 9); EX(CUR, 10); SBAR(); \
            o = MFMA32(v0, __builtin_bit_cast(bf16x8, p0), o); SBAR(); \
            if (LOADV0) v0 = cat8(ldtr(LP(rV) + (vnoff)), ldtr(LP(rV) + (vnoff) + 512)); \
            EX(CUR, 11); CV(p1[0], CUR, 8); EX(CUR, 12); EX(CUR, 13); SBAR(); \
            ls = MFMA16(selA, __builtin_bit_cast(bf16x8, p0), ls); SBAR(); \
            EX(CUR, 14); EX(CUR, 15); CV(p1[1], CUR, 10); CV(p1[2], CUR, 12); CV(p1[3], CUR, 14); SBAR(); \
        } while (0)
#define K2_TILE(T, SL, VM, MORE, DMAON) do { \
            K2_STEP(sA, sB, (SL) * SLOT + 0 * 2048, (SL) * SLOT + 2 * 2048, (SL) * SLOT + 1 * 2048, true, true, true, (void)0); \
            K2_STEP(sB, sA, (SL) * SLOT + 1 * 2048, (SL) * SLOT + 3 * 2048, (SL) * SLOT + 2 * 2048, true, true, true, (void)0); \
            K2_STEP(sA, sB, (SL) * SLOT + 2 * 2048, (((SL) + 1) & 3) * SLOT + 0 * 2048, (SL) * SLOT + 3 * 2048, true, MORE, true, \
                    do { if (MORE) { asm volatile("s_waitcnt vmcnt(" #VM ")\n\ts_barrier" ::: "memory"); if (DMAON) K2_DMA_S((T) + 3, ((SL) + 3) & 3); } } while (0)); \
            K2_STEP(sB, sA, (SL) * SLOT + 3 * 2048, (((SL) + 1) & 3) * SLOT + 1 * 2048, (((SL) + 1) & 3) * SLOT + 0 * 2048, MORE, MORE, MORE, (void)0); \
        } while (0)
        unsigned pfd = 0;
        const char* pfp; { const int li = blockIdx.x * 288 + w * 64 + lane; const int lc = li < 73728 ? li : 73727; pfp = lc < 57344 ? (const char*)a.wd + (size_t)lc * 128 : (const char*)a.wo + (size_t)(lc - 57344) * 128; }
#define K2_PF() do { if (w < 5) asm volatile("global_load_dword %0, %1, off" : "+v"(pfd) : "v"(pfp) : "memory"); } while (0)
#define K2_DMA_S(T, SL) do { glds16(voffK, gK + (size_t)(T) * 8192, dK + (SL) * SLOT); glds16(voffV, gV + (size_t)(T) * 8192, dV + (SL) * SLOT); } while (0)
        for (int T = 0; T < NTILE - 4; T += 4) {
            K2_TILE(T, 0, 2, true, true); if (T == 0) K2_PF(); K2_TILE(T + 1, 1, 2, true, true); K2_TILE(T + 2, 2, 2, true, true); K2_TILE(T + 3, 3, 2, true, true);
        }
        K2_TILE(NTILE - 4, 0, 2, true, true); K2_TILE(NTILE - 3, 1, 2, true, false); K2_TILE(NTILE - 2, 2, 0, true, false); K2_TILE(NTILE - 1, 3, 0, false, false);
        o = MFMA32(v1, __builtin_bit_cast(bf16x8, p1), o); ls = MFMA16(selA, __builtin_bit_cast(bf16x8, p1), ls);
        asm volatile("s_waitcnt vmcnt(0)" : "+v"(pfd) :: "memory");
        KSTAMP(1, 2);
#undef EX
#undef CV
#undef K2_STEP
#undef K2_TILE
#undef K2_DMA_S
    } else {
        float m = -INFINITY, l = 0.f;
        for (int T = 0; T < NTILE; ++T) {
            const unsigned cur = (T & 3) * SLOT;
            asm volatile("s_waitcnt vmcnt(0)\n\ts_barrier" ::: "memory");
            if (T + 3 < NTILE) K2_DMA(T + 3);
#pragma unroll 1
            for (int u = 0; u < 4; ++u) {
                bf16x8 k0, k1, v0, v1;
                K2_LDK(k0, k1, cur + u * 2048); K2_LDV(v0, v1, cur + u * 2048);
                f32x16 z;
#pragma unroll
                for (int i = 0; i < 16; ++i) z[i] = 0.f;
                f32x16 s = MFMA32(k0, q0, z); s = MFMA32(k1, q1, s);
                float mx = s[0];
#pragma unroll
                for (int i = 1; i < 16; ++i) mx = __builtin_fmaxf(mx, s[i]);
                auto rr = __builtin_amdgcn_permlane32_swap(__float_as_uint(mx), __float_as_uint(mx), false, false);
                mx = __builtin_fmaxf(__uint_as_float(rr[0]), __uint_as_float(rr[1]));
                const float mn = __builtin_fmaxf(m, mx);
                const float al = (mn == m) ? 1.0f : __builtin_amdgcn_exp2f(m - mn);
                const float mref = (mn == -INFINITY) ? 0.f : mn;
                m = mn;
                float ps = 0.f; u32x4 p0, p1;
#pragma unroll
                for (int i = 0; i < 16; ++i) { s[i] = __builtin_amdgcn_exp2f(s[i] - mref); }
#pragma unroll
                for (int i = 0; i < 4; ++i) { p0[i] = pk2(s[2 * i], s[2 * i + 1]); p1[i] = pk2(s[8 + 2 * i], s[8 + 2 * i + 1]); }
#pragma unroll
                for (int i = 0; i < 4; ++i) ps += __uint_as_float(p0[i] << 16) + __uint_as_float(p0[i] & 0xffff0000u) + __uint_as_float(p1[i] << 16) + __uint_as_float(p1[i] & 0xffff0000u);
                l = l * al + ps;
#pragma unroll
                for (int i = 0; i < 16; ++i) o[i] *= al;
                o = MFMA32(v0, __builtin_bit_cast(bf16x8, p0), o);
                o = MFMA32(v1, __builtin_bit_cast(bf16x8, p1), o);
            }
        }
        auto rr = __builtin_amdgcn_permlane32_swap(__float_as_uint(l), __float_as_uint(l), false, false);
        lsum = __uint_as_float(rr[0]) + __uint_as_float(rr[1]);
        return true;
    }
    const float l0 = __shfl(ls[0], r & 15), l1 = __shfl(ls[1], r & 15);
    lsum = (r < 16) ? l0 : l1;
    bool ok = lsum < 3.0e38f;
#pragma unroll
    for (int i = 0; i < 16; ++i) ok = ok && (__builtin_fabsf(o[i]) < 3.0e38f);
    return ok;
#undef K2_DMA
#undef K2_LDK
#undef K2_LDV
#undef K2_STEP
}

DI void body(const Args& a, char* smem) {
    const unsigned lds0 = (unsigned)(uintptr_t)smem;
    const int tid = threadIdx.x, lane = tid & 63, w = __builtin_amdgcn_readfirstlane(tid >> 6);
    const int xg = blockIdx.x & 7, j = blockIdx.x >> 3, h = xg * 4 + (j >> 3), qb = j & 7;
    f32x16 o; float lsum;
    volatile LAS unsigned* flag = (volatile LAS unsigned*)(size_t)(lds0 + LDS_FLAG);
    if (tid == 0) *flag = 0u;
    const bool ok = attn_pass<false>(a, lds0, h, qb, w, lane, o, lsum);
    if (!ok) *flag = 1u;
    asm volatile("s_waitcnt lgkmcnt(0)\n\ts_barrier" ::: "memory");
    if (__builtin_amdgcn_readfirstlane(*flag)) {
        attn_pass<true>(a, lds0, h, qb, w, lane, o, lsum);
    }
    const int r = lane & 31, hh = lane >> 5;
    const int row = qb * 256 + w * 32 + r;
    const float sc = a.g[row] / lsum;
    unsigned pk[4][2];
#pragma unroll
    for (int G = 0; G < 4; ++G) { pk[G][0] = cvtpk(o[4 * G] * sc, o[4 * G + 1] * sc); pk[G][1] = cvtpk(o[4 * G + 2] * sc, o[4 * G + 3] * sc); }
    bf16_t* op = a.o + (size_t)row * HO_LD + h * 32 + hh * 8;
#pragma unroll
    for (int G = 0; G < 4; G += 2) {
        auto r0 = __builtin_amdgcn_permlane32_swap(pk[G][0], pk[G + 1][0], false, false);
        auto r1 = __builtin_amdgcn_permlane32_swap(pk[G][1], pk[G + 1][1], false, false);
        *(u32x4*)(op + G * 8) = (u32x4){r0[0], r1[0], r0[1], r1[1]};
    }
    KSTAMP(1, 3);
}
}

namespace k3 {
#define K3_MAP 1
#define K3_NODMA 0
constexpr int NS = 5, STAGE = 32768, NKT = 36;
constexpr int LDS_BYTES = NS * STAGE;
struct Args { const bf16_t* ho; const bf16_t* wd; const bf16_t* wo; const float* g; float* xch; unsigned* flags; bf16_t* out; };

DI void st16_sc1(void* p, f32x4 v) { asm volatile("global_store_dwordx4 %0, %1, off sc1\n\ts_nop 1" :: "v"(p), "v"(v) : "memory"); }

DI void body(const Args& a, char* smem) {
    const unsigned lds0 = (unsigned)(uintptr_t)smem;
    const int tid = threadIdx.x, lane = tid & 63, w = __builtin_amdgcn_readfirstlane(tid >> 6);
    const int xg = blockIdx.x & 7, j = blockIdx.x >> 3;
    const int kh = K3_MAP == 0 ? (j & 1) : (xg & 1);
    const int rb = K3_MAP == 0 ? (xg >> 1) * 4 + ((j >> 1) & 3) : K3_MAP == 1 ? (xg >> 2) * 8 + (j & 7) : (xg >> 1) * 4 + (j & 3);
    const int cb = K3_MAP == 0 ? (xg & 1) * 4 + (j >> 3) : K3_MAP == 1 ? ((xg >> 1) & 1) * 4 + (j >> 3) : (j >> 2);
    const int tile = rb * 8 + cb;
    const int nH = kh ? 20 : 36;
    if (w >= 4) {
        const int pw = w - 4;
        const unsigned voffA = (unsigned)((lane >> 3) * (HO_LD * 2) + (((lane & 7) ^ (lane >> 3)) * 16));
        const unsigned voffB0 = (unsigned)((lane >> 4) * 2048 + (((lane & 15) ^ ((lane >> 4) << 1)) * 16));
        const unsigned voffB1 = (unsigned)((lane >> 4) * 2048 + (((lane & 15) ^ (((lane >> 4) | 4) << 1)) * 16));
        const char* gA = (const char*)a.ho + (size_t)(rb * 128 + pw * 32) * (HO_LD * 2) + (kh ? 2304 * 2 : 0);
        const char* gWd = (const char*)a.wd + (size_t)((kh ? 2304 : 0) + pw * 16) * 2048 + cb * 256;
        const char* gWo = (const char*)a.wo + (size_t)(pw * 16) * 2048 + cb * 256 - (size_t)nH * (64 * 2048);
        const unsigned dA = lds0 + pw * 4096, dB = lds0 + 16384 + pw * 4096;
        auto issue = [&](int t, unsigned so) {
            if (K3_NODMA && t >= NS) return;
            const char* sa = gA + (size_t)t * 128; const char* sb = (t < nH ? gWd : gWo) + (size_t)t * (64 * 2048);
#pragma unroll
            for (int i = 0; i < 4; ++i) glds16(voffA, sa + (size_t)i * 8 * (HO_LD * 2), dA + so + i * 1024);
#pragma unroll
            for (int i = 0; i < 4; ++i) glds16((i & 2) ? voffB1 : voffB0, sb + i * 4 * 2048, dB + so + i * 1024); };
#pragma unroll
        for (int t = 0; t < NS - 1; ++t) issue(t, t * STAGE);
        unsigned so = (NS - 1) * STAGE;
        for (int t = 0; t < NKT - (NS - 2); ++t) {
            asm volatile("s_waitcnt vmcnt(24)\n\ts_barrier" ::: "memory");
            if (t + NS - 1 < NKT) issue(t + NS - 1, so);
            so = (so == (NS - 1) * STAGE) ? 0u : so + STAGE;
        }
        asm volatile("s_waitcnt vmcnt(16)\n\ts_barrier" ::: "memory");
        asm volatile("s_waitcnt vmcnt(8)\n\ts_barrier" ::: "memory");
        asm volatile("s_waitcnt vmcnt(0)\n\ts_barrier" ::: "memory");
        asm volatile("s_barrier" ::: "memory");
        return;
    }
    const int wr = w & 1, wc = w >> 1, fr = lane & 15, fq = lane >> 4;
    const int q = (lane & 15) >> 2, p4 = lane & 3, sR = q | ((fq & 1) << 2);
    unsigned rA[3][2], rB[3][4];
#pragma unroll
    for (int sp = 0; sp < 3; ++sp) {
#pragma unroll
        for (int ks = 0; ks < 2; ++ks) { rA[sp][ks] = lds0 + sp * 2 * STAGE + (wr * 64 + fr) * 128 + (((fq + 4 * ks) ^ (fr & 7)) * 16); asm volatile("" : "+v"(rA[sp][ks])); }
#pragma unroll
        for (int n = 0; n < 4; ++n) { rB[sp][n] = lds0 + sp * 2 * STAGE + 16384 + (((8 * fq + q) * 256 + 32 * sR + 8 * p4) ^ ((wc * 4 + n) << 5)); asm volatile("" : "+v"(rB[sp][n])); }
    }
    f32x4 acc[4][4];
#pragma unroll
    for (int m = 0; m < 4; ++m)
#pragma unroll
        for (int n = 0; n < 4; ++n) acc[m][n] = (f32x4){0.f, 0.f, 0.f, 0.f};
    bf16x8 Af[2][4]; s16x4 Bh[2][4][2];
#define LP(x) ((const LAS char*)(size_t)(x))
#define K3_RD(buf, i, SL, ks) do { if ((i) < 4) Af[buf][i] = ld128(LP(rA[(SL) >> 1][ks]) + ((SL) & 1) * STAGE + (i) * 2048); \
        else Bh[buf][((i) - 4) >> 1][((i) - 4) & 1] = ldtr(LP(rB[(SL) >> 1][((i) - 4) >> 1]) + ((SL) & 1) * STAGE + (ks) * 8192 + (((i) - 4) & 1) * 1024); } while (0)
    const int row0 = rb * 128 + wr * 64 + fr;
    float omg[4];
#pragma unroll
    for (int m = 0; m < 4; ++m) omg[m] = 1.0f - a.g[row0 + m * 16];
    asm volatile("s_barrier" ::: "memory");
    KSTAMP(2, 1);
#pragma unroll
    for (int i = 0; i < 12; ++i) K3_RD(0, i, 0, 0);
#define K3_TILE(t, SL, MORE) do { \
        _Pragma("unroll") for (int i = 0; i < 16; ++i) { \
            const int n = i >> 2, m = i & 3; \
            MFMA16A(acc[m][n], cat8(Bh[0][n][0], Bh[0][n][1]), Af[0][m]); \
            SBAR(); \
            if (i < 12) K3_RD(1, i, SL, 1); \
            SBAR(); } \
        if (MORE) asm volatile("s_waitcnt lgkmcnt(0)\n\ts_barrier" ::: "memory");         \
        SBAR(); \
        _Pragma("unroll") for (int i = 0; i < 16; ++i) { \
            const int n = i >> 2, m = i & 3; \
            MFMA16A(acc[m][n], cat8(Bh[1][n][0], Bh[1][n][1]), Af[1][m]); \
            SBAR(); \
            if (i < 12 && (MORE)) K3_RD(0, i, ((SL) + 1) % NS, 0); \
            SBAR(); } \
        if ((t) + 1 == nH) { asm volatile("s_nop 7\n\ts_nop 7" ::: "memory"); SBAR(); _Pragma("unroll") for (int m = 0; m < 4; ++m) _Pragma("unroll") for (int n = 0; n < 4; ++n) { acc[m][n] *= omg[m]; asm volatile("" : "+v"(acc[m][n])); } \
            asm volatile("s_nop 3" ::: "memory"); SBAR(); } \
    } while (0)
    for (int t = 0; t < 35; t += 5) { K3_TILE(t, 0, true); K3_TILE(t + 1, 1, true); K3_TILE(t + 2, 2, true); K3_TILE(t + 3, 3, true); K3_TILE(t + 4, 4, true); }
    K3_TILE(35, 0, false);
    asm volatile("s_nop 7\n\ts_nop 7" ::: "memory"); SBAR();
    KSTAMP(2, 2);
    if (wc != kh) {
        float* xs = a.xch + ((size_t)(tile * 2 + kh) * 2 + wr) * 4096 + lane * 4;
#pragma unroll
        for (int m = 0; m < 4; ++m)
#pragma unroll
            for (int n = 0; n < 4; ++n) st16_sc1(xs + (m * 4 + n) * 256, acc[m][n]);
        asm volatile("s_waitcnt vmcnt(0)" ::: "memory");
    }
    asm volatile("s_barrier" ::: "memory");
    if (tid == 0) __hip_atomic_store(a.flags + (tile * 2 + kh) * 32, 1u, __ATOMIC_RELAXED, __HIP_MEMORY_SCOPE_AGENT);
    if (wc == kh) {
        const unsigned* pf = a.flags + (tile * 2 + (kh ^ 1)) * 32;
        unsigned spins = 0;
        while (__builtin_amdgcn_readfirstlane(__hip_atomic_load(pf, __ATOMIC_RELAXED, __HIP_MEMORY_SCOPE_AGENT)) == 0u) { __builtin_amdgcn_s_sleep(1); if (++spins > (1u << 22)) break; }
        asm volatile("" ::: "memory");
        const float* xr = a.xch + ((size_t)(tile * 2 + (kh ^ 1)) * 2 + wr) * 4096 + lane * 4;
        f32x4 pv[4][4];
#pragma unroll
        for (int m = 0; m < 4; ++m)
#pragma unroll
            for (int n = 0; n < 4; ++n) asm volatile("global_load_dwordx4 %0, %1, off sc1" : "=v"(pv[m][n]) : "v"(xr + (m * 4 + n) * 256) : "memory");
        asm volatile("s_waitcnt vmcnt(0)" : "+v"(pv[0][0]), "+v"(pv[0][1]), "+v"(pv[0][2]), "+v"(pv[0][3]), "+v"(pv[1][0]), "+v"(pv[1][1]), "+v"(pv[1][2]), "+v"(pv[1][3]),
                     "+v"(pv[2][0]), "+v"(pv[2][1]), "+v"(pv[2][2]), "+v"(pv[2][3]), "+v"(pv[3][0]), "+v"(pv[3][1]), "+v"(pv[3][2]), "+v"(pv[3][3]) :: "memory");
        const int cperm = ((fq & 1) << 4) | ((fq >> 1) << 3);
        bf16_t* ob = a.out + (size_t)row0 * D_ + cb * 128 + wc * 64 + cperm;
#pragma unroll
        for (int m = 0; m < 4; ++m)
#pragma unroll
            for (int n = 0; n < 4; n += 2) {
                const f32x4 lo = acc[m][n] + pv[m][n], hi = acc[m][n + 1] + pv[m][n + 1];
                auto r0 = __builtin_amdgcn_permlane16_swap(cvtpk(lo[0], lo[1]), cvtpk(hi[0], hi[1]), false, false);
                auto r1 = __builtin_amdgcn_permlane16_swap(cvtpk(lo[2], lo[3]), cvtpk(hi[2], hi[3]), false, false);
                *(u32x4*)(ob + (size_t)m * 16 * D_ + n * 16) = (u32x4){r0[0], r1[0], r0[1], r1[1]};
            }
    }
    KSTAMP(2, 3);
#undef K3_RD
#undef K3_TILE
}
}

__global__ __launch_bounds__(512, 2) void k1_kernel(k1::Args a, unsigned* flags) {
    extern __shared__ __attribute__((aligned(16))) char smem[];
    if (threadIdx.x == 0) flags[blockIdx.x * 32] = 0u;
    KSTAMP(0, 0);
    k1::body<3, 0>(a, smem);
}
__global__ __launch_bounds__(512, 2) void k2_kernel(k2::Args a) {
    extern __shared__ __attribute__((aligned(16))) char smem[];
    KSTAMP(1, 0);
    k2::body(a, smem);
}
__global__ __launch_bounds__(512, 2) void k3_kernel(k3::Args a) {
    extern __shared__ __attribute__((aligned(16))) char smem[];
    KSTAMP(2, 0);
    k3::body(a, smem);
}
extern "C" void kernel_launch(void* const* d_in, const int* in_sizes, int n_in, void* d_out, int out_size, void* d_ws, size_t ws_size, hipStream_t stream) {
    char* ws = (char*)d_ws;
    bf16_t* qkv = (bf16_t*)ws; bf16_t* h = (bf16_t*)(ws + (16 << 20)); float* g = (float*)(ws + (36 << 20)); bf16_t* o = h + F_;
    float* xch = (float*)(ws + (40 << 20)); unsigned* flags = (unsigned*)(ws + (50 << 20));
    k1::Args a1; a1.x = (const bf16_t*)d_in[0]; a1.wqkv = (const bf16_t*)d_in[1]; a1.wg = (const bf16_t*)d_in[4]; a1.wu = (const bf16_t*)d_in[5]; a1.gw = (const bf16_t*)d_in[7];
    a1.qkv = qkv; a1.h = h; a1.g = g;
    k2::Args a2; a2.qkv = qkv; a2.g = g; a2.o = o; a2.wd = (const bf16_t*)d_in[6]; a2.wo = (const bf16_t*)d_in[2];
    k3::Args a3; a3.ho = h; a3.wd = (const bf16_t*)d_in[6]; a3.wo = (const bf16_t*)d_in[2]; a3.g = g; a3.xch = xch; a3.flags = flags; a3.out = (bf16_t*)d_out;
    (void)hipFuncSetAttribute((const void*)k1_kernel, hipFuncAttributeMaxDynamicSharedMemorySize, k1::LDS_BYTES);
    (void)hipFuncSetAttribute((const void*)k2_kernel, hipFuncAttributeMaxDynamicSharedMemorySize, k2::LDS_BYTES);
    (void)hipFuncSetAttribute((const void*)k3_kernel, hipFuncAttributeMaxDynamicSharedMemorySize, k3::LDS_BYTES);
#define ONLY 7
    if (ONLY & 1) k1_kernel<<<256, 512, k1::LDS_BYTES, stream>>>(a1, flags);
    if (ONLY & 2) k2_kernel<<<256, 512, k2::LDS_BYTES, stream>>>(a2);
    if (ONLY & 4) k3_kernel<<<256, 512, k3::LDS_BYTES, stream>>>(a3);
}
```

```cpp
#include <hip/hip_runtime.h>

#include <hip/hip_runtime.h>
#include <stdint.h>
#define LAS __attribute__((address_space(3)))
#define DI __device__ __forceinline__
typedef unsigned short bf16_t;
typedef short bf16x8 __attribute__((ext_vector_type(8)));
typedef short s16x4 __attribute__((ext_vector_type(4)));
typedef short v4i16_t __attribute__((ext_vector_type(4)));
typedef float f32x4 __attribute__((ext_vector_type(4)));
typedef float f32x16 __attribute__((ext_vector_type(16)));
typedef unsigned u32x4 __attribute__((ext_vector_type(4)));
typedef unsigned u32x2 __attribute__((ext_vector_type(2)));
typedef float f32x2 __attribute__((ext_vector_type(2)));
typedef __bf16 bf16x2_t __attribute__((ext_vector_type(2)));
#define SBAR() __builtin_amdgcn_sched_barrier(0)
#define LP(x) ((const LAS char*)(size_t)(x))

constexpr int S_ = 2048, D_ = 1024, H_ = 32, DH_ = 32, F_ = 3584, HO_LD = F_ + D_;

DI unsigned cvtpk(float lo, float hi) { f32x2 v = {lo, hi}; bf16x2_t b = __builtin_convertvector(v, bf16x2_t); return __builtin_bit_cast(unsigned, b); }
DI void glds16(unsigned voff, const void* sbase, unsigned lds_dst) {
    asm volatile("s_mov_b32 m0, %2\n\ts_nop 0\n\tglobal_load_lds_dwordx4 %0, %1" :: "v"(voff), "s"(sbase), "s"(lds_dst) : "memory");
}
DI void glds16c(unsigned voff, const void* sbase, unsigned lds_dst) {
    asm volatile("s_mov_b32 m0, %2\n\ts_nop 0\n\tglobal_load_lds_dwordx4 %0, %1 sc1" :: "v"(voff), "s"(sbase), "s"(lds_dst) : "memory");
}
DI void st16w(void* p, u32x4 v) { asm volatile("global_store_dwordx4 %0, %1, off sc1\n\ts_nop 1" :: "v"(p), "v"(v) : "memory"); }
DI void st16w(void* p, f32x4 v) { asm volatile("global_store_dwordx4 %0, %1, off sc1\n\ts_nop 1" :: "v"(p), "v"(v) : "memory"); }
DI void st8w(void* p, u32x2 v) { asm volatile("global_store_dwordx2 %0, %1, off sc1\n\ts_nop 1" :: "v"(p), "v"(v) : "memory"); }
DI void st4w(void* p, unsigned v) { asm volatile("global_store_dword %0, %1, off sc1" :: "v"(p), "v"(v) : "memory"); }
DI s16x4 ldtr(const LAS char* p) { return __builtin_bit_cast(s16x4, __builtin_amdgcn_ds_read_tr16_b64_v4i16((LAS v4i16_t*)p)); }
DI bf16x8 ld128(const LAS char* p) { return *(const LAS bf16x8*)p; }
DI bf16x8 cat8(s16x4 lo, s16x4 hi) { return __builtin_shufflevector(lo, hi, 0, 1, 2, 3, 4, 5, 6, 7); }
DI int lane_id() { int l; asm volatile("v_mbcnt_lo_u32_b32 %0, -1, 0\n\tv_mbcnt_hi_u32_b32 %0, -1, %0" : "=v"(l)); return l; }
#define MFMA16(a, b, c) __builtin_amdgcn_mfma_f32_16x16x32_bf16(a, b, c, 0, 0, 0)
#define MFMA16A(acc, a, b) asm volatile("v_mfma_f32_16x16x32_bf16 %0, %1, %2, %0" : "+v"(acc) : "v"(a), "v"(b))
#define MFMA32(a, b, c) __builtin_amdgcn_mfma_f32_32x32x16_bf16(a, b, c, 0, 0, 0)

#define KSTAMP(i)

struct Params {
    const bf16_t* x; const bf16_t* wqkv; const bf16_t* wo; const bf16_t* wg; const bf16_t* wu; const bf16_t* wd; const bf16_t* gw;
    bf16_t* out;
    bf16_t* kv;
    bf16_t* ho;
    float* g;
    float* xch;
    unsigned* flags;
};
constexpr int FLAG_EPOCH = 0, FLAG_Q = 64, FLAG_H = FLAG_Q + 256, FLAG_O = FLAG_H + 256, FLAG_X = FLAG_O + 256;
constexpr unsigned SPIN_MAX = 1u << 21;

DI void flag_set(unsigned* p, unsigned tag) { __hip_atomic_store(p, tag, __ATOMIC_RELAXED, __HIP_MEMORY_SCOPE_AGENT); }
DI unsigned flag_get(const unsigned* p) { return __hip_atomic_load(p, __ATOMIC_RELAXED, __HIP_MEMORY_SCOPE_AGENT); }
DI void wait_flags(const unsigned* p, unsigned tag, bool skip = false) {
    unsigned spins = 0;
    while (__builtin_amdgcn_ballot_w64(!skip && flag_get(p) != tag) != 0ull) { __builtin_amdgcn_s_sleep(2); if (++spins > SPIN_MAX) break; }
    asm volatile("" ::: "memory");
}
DI float ldf_dev(const float* p) { return __hip_atomic_load(p, __ATOMIC_RELAXED, __HIP_MEMORY_SCOPE_AGENT); }

namespace p1 {
constexpr int A_STAGE = 32768, B_STAGE = 49152, B_BASE = 65536;
constexpr int NT = D_ / 64;
constexpr float QSCALE = 0.17677669529663687f * 1.4426950408889634f;
constexpr int PD = 3;

DI void mainloop(const Params& a, const unsigned lds0, const int w, const int rb, const int ct, f32x4 (&acc)[4][10], unsigned& tag) {
    const int lane = lane_id();
    const int wr = w & 3, wc = w >> 2;
    unsigned epoch;
    asm volatile("global_load_dword %0, %1, off sc1" : "=v"(epoch) : "v"(a.flags + FLAG_EPOCH) : "memory");
    const unsigned voffA = (unsigned)((lane >> 3) * 2048 + (((lane & 7) ^ (lane >> 3)) * 16));
    const int sB = (lane >> 4) | (((w >> 1) & 1) << 2);
    int chq = (lane & 15) ^ (sB << 1), chg = chq;
    if (chq >= 12) chq -= 4;
    if (chg >= 14) chg -= 2;
    const unsigned voffQ = (unsigned)((lane >> 4) * 6144 + (chq >> 2) * 2048 + (chq & 3) * 16), voffG = (unsigned)((lane >> 4) * 7168 + chg * 16);
    const char* gA = (const char*)a.x + (size_t)(rb * 256 + w * 32) * 2048;
    const char* gQ = (const char*)a.wqkv + (size_t)(w * 4) * 6144 + ct * 64;
    const char* gG = (const char*)a.wg + (size_t)(w * 4) * 7168 + ct * 224;
    const char* gU = (const char*)a.wu + (size_t)(w * 4) * 7168 + ct * 224;
    const unsigned dA = lds0 + w * 4096, dB = lds0 + B_BASE + w * 1024;
#define P1_DMA(p, t, st) do { \
        if ((p) < 4) glds16(voffA, gA + (size_t)(t) * 128 + (p) * 8 * 2048, dA + (st) * A_STAGE + (p) * 1024); \
        else if ((p) == 4 || (p) == 7) glds16(voffQ, gQ + (size_t)(t) * (64 * 6144) + ((p) == 7 ? 32 * 6144 : 0), dB + (st) * B_STAGE + ((p) == 7 ? 8192 : 0)); \
        else if ((p) == 5 || (p) == 8) glds16(voffG, gG + (size_t)(t) * (64 * 7168) + ((p) == 8 ? 32 * 7168 : 0), dB + (st) * B_STAGE + 16384 + ((p) == 8 ? 8192 : 0)); \
        else glds16(voffG, gU + (size_t)(t) * (64 * 7168) + ((p) == 9 ? 32 * 7168 : 0), dB + (st) * B_STAGE + 32768 + ((p) == 9 ? 8192 : 0)); } while (0)
#pragma unroll
    for (int pc = 0; pc < 10; ++pc) P1_DMA(pc, 0, 0);
#pragma unroll
    for (int pc = 0; pc < 10; ++pc) P1_DMA(pc, 1, 1);
    const int fr = lane & 15, fq = lane >> 4;
    const unsigned rA0 = lds0 + (wr * 64 + fr) * 128 + ((fq ^ (fr & 7)) * 16);
    const unsigned rA1 = lds0 + (wr * 64 + fr) * 128 + (((fq + 4) ^ (fr & 7)) * 16);
    const int q = (lane & 15) >> 2, p = lane & 3, sR = q | ((fq & 1) << 2);
    const int bbase = (8 * fq + q) * 256 + 32 * sR + 8 * p;
    unsigned rB[6];
#pragma unroll
    for (int jj = 0; jj < 4; ++jj) rB[jj] = lds0 + B_BASE + (bbase ^ ((3 * wc + jj) << 5));
    rB[4] = wc ? rB[3] + 16384 : rB[0];
    rB[5] = wc ? rB[3] + 32768 : rB[3];
#pragma unroll
    for (int m = 0; m < 4; ++m)
#pragma unroll
        for (int n = 0; n < 10; ++n) acc[m][n] = (f32x4){0.f, 0.f, 0.f, 0.f};
    bf16x8 Af[2][4];
    bf16x8 Bf[PD + 1];
#define P1_BADDR(n) ((n) < 6 ? LP(rB[(n) >> 1]) + (((n) & 1) ? 32768 : 16384) : (n) == 6 ? LP(rB[1]) : (n) == 7 ? LP(rB[2]) : (n) == 8 ? LP(rB[4]) : LP(rB[5]))
#define P1_LDB(dst, n, ks) do { const LAS char* _p = P1_BADDR(n) + (ks) * 8192; dst = cat8(ldtr(_p), ldtr(_p + 1024)); } while (0)
#define P1_LDA(m, ks, so) Af[ks][m] = ld128(LP((ks) ? rA1 : rA0) + (so) + (m) * 2048)
#define P1_TOGGLE(CUR) do { _Pragma("unroll") for (int jj = 0; jj < 6; ++jj) { rB[jj] += (CUR) ? -B_STAGE : B_STAGE; asm volatile("" : "+v"(rB[jj])); } } while (0)
#define P1_SYNC(N) asm volatile("s_waitcnt vmcnt(" #N ") lgkmcnt(0)\n\ts_barrier" ::: "memory")
    asm volatile("s_waitcnt vmcnt(13)\n\ts_barrier" ::: "memory");
    asm volatile("" : "+v"(epoch));
    tag = (unsigned)__builtin_amdgcn_readfirstlane(epoch) + 1u;
    KSTAMP(1);
#pragma unroll
    for (int m = 0; m < 4; ++m) P1_LDA(m, 0, 0);
#pragma unroll
    for (int s = 0; s < PD; ++s) P1_LDB(Bf[s], s, 0);

    constexpr int SM = 10 - PD, SE = 20 - PD;
#define P1_W1SLOT(r) ((r) == 0 ? 0 : (r) == 1 ? 1 : (r) == 3 ? 2 : (r) == 4 ? 3 : (r) == 6 ? 4 : (r) == 7 ? 5 : (r) == 8 ? 6 : -1)
#define P1_W2SLOT(r) ((r) == 0 ? 7 : (r) == 3 ? 8 : (r) == 6 ? 9 : -1)
#define P1_TILE(t, CUR, VM_M, VM_E, LAST, W1ON, W2ON, W2PREV) do { \
        _Pragma("unroll") for (int s = 0; s < 20; ++s) { \
            const int ks = s / 10, n = s % 10; \
            if (s == SM) P1_SYNC(VM_M); \
            if (s == SE && !(LAST)) { P1_SYNC(VM_E); P1_TOGGLE(CUR); } \
            { const int sp = s + PD; \
              if (sp < 20) P1_LDB(Bf[sp % (PD + 1)], sp % 10, sp / 10); \
              else if (!(LAST)) P1_LDB(Bf[sp % (PD + 1)], sp - 20, 0); } \
            if (s < SM && s >= SM - 7 && ((s - (SM - 7)) & 1) == 0) P1_LDA((s - (SM - 7)) / 2, 1, (CUR) * A_STAGE); \
            if (!(LAST) && s >= SE) { \
                if (s == SE) { P1_LDA(0, 0, (1 - (CUR)) * A_STAGE); P1_LDA(1, 0, (1 - (CUR)) * A_STAGE); } \
                if (s == SE + 1) P1_LDA(2, 0, (1 - (CUR)) * A_STAGE); \
                if (s == SE + 2) P1_LDA(3, 0, (1 - (CUR)) * A_STAGE); } \
            if ((W1ON) && s >= SM && s < SE) { const int pc = P1_W1SLOT(s - SM); if (pc >= 0) P1_DMA(pc, (t) + 2, (CUR)); } \
            if ((W2ON) && s >= SE) { const int pc = P1_W2SLOT(s - SE); if (pc >= 0) P1_DMA(pc, (t) + 2, (CUR)); } \
            if ((W2PREV) && s < SM) { const int pc = P1_W2SLOT(s + 20 - SE); if (pc >= 0) P1_DMA(pc, (t) + 1, 1 - (CUR)); } \
            SBAR(); \
            _Pragma("unroll") for (int m = 0; m < 4; ++m) acc[m][n] = MFMA16(Bf[s % (PD + 1)], Af[ks][m], acc[m][n]); \
            SBAR(); \
        } } while (0)
    static_assert(20 % (PD + 1) == 0, "ring index must be static across tiles");
    P1_TILE(0, 0, 10, 10, false, true, true, false);
    for (int t = 1; t < NT - 3; t += 2) {
        P1_TILE(t, 1, 10, 10, false, true, true, true);
        P1_TILE(t + 1, 0, 10, 10, false, true, true, true);
    }
    P1_TILE(NT - 3, 1, 10, 10, false, true, true, true);
    P1_TILE(NT - 2, 0, 10, 3, false, false, false, true);
    P1_TILE(NT - 1, 1, 0, 0, true, false, false, false);
#undef P1_DMA
#undef P1_BADDR
#undef P1_LDB
#undef P1_LDA
#undef P1_TILE
#undef P1_TOGGLE
#undef P1_SYNC
}

constexpr int LDS_Q = 65536;
DI void epilogue(const Params& a, const unsigned lds0, f32x4 (&acc)[4][10], const int w, const int rb, const int ct, const unsigned tag, u32x4 (&hreg)[4][2], float& gval, unsigned& pollv) {
    const int lane = lane_id();
    const int wr = w & 3, wc = w >> 2, fr = lane & 15, fq = lane >> 4;
    const int row0 = rb * 256 + wr * 64 + fr;
    const int cperm = ((fq & 1) << 4) | ((fq >> 1) << 3);
    asm volatile("s_barrier" ::: "memory");
    {
        auto pack = [&](int m, int nlo, int nhi, float sc) {
            const f32x4 lo = acc[m][nlo] * sc, hi = acc[m][nhi] * sc;
            auto r0 = __builtin_amdgcn_permlane16_swap(cvtpk(lo[0], lo[1]), cvtpk(hi[0], hi[1]), false, false);
            auto r1 = __builtin_amdgcn_permlane16_swap(cvtpk(lo[2], lo[3]), cvtpk(hi[2], hi[3]), false, false);
            return (u32x4){r0[0], r1[0], r0[1], r1[1]}; };
        const int lrow = (wr & 1) * 64 + fr;
        const unsigned slot = lds0 + (wr >> 1) * 16384;
        bf16_t* gdst = a.kv + (size_t)(wc * 32 + ct) * (S_ * 32) + (size_t)row0 * 32 + cperm;
        if (wc) {
#pragma unroll
            for (int m = 0; m < 4; ++m) { const u32x4 v = pack(m, 6, 7, 1.0f); st16w(gdst + m * 16 * 32, v);
                *(LAS u32x4*)(size_t)(slot + 8192 + (lrow + m * 16) * 64 + cperm * 2) = v; }
        } else {
#pragma unroll
            for (int m = 0; m < 4; ++m) { const u32x4 v = pack(m, 7, 9, 1.0f); st16w(gdst + m * 16 * 32, v);
                *(LAS u32x4*)(size_t)(slot + (lrow + m * 16) * 64 + (((cperm >> 3) ^ (((lrow + m * 16) >> 2) & 3)) * 16)) = v; }
#pragma unroll
            for (int m = 0; m < 4; ++m) *(LAS u32x4*)(size_t)(lds0 + LDS_Q + (wr * 64 + m * 16 + fr) * 64 + cperm * 2) = pack(m, 8, 6, QSCALE);
        }
    }
    SBAR();
    KSTAMP(10);
    const int grow = rb * 256 + ct * 8 + w;
    u32x4 gx0, gx1, gg0, gg1;
    { const u32x4* xr = (const u32x4*)(a.x + (size_t)grow * D_) + lane * 2; const u32x4* gr = (const u32x4*)(a.gw) + lane * 2;
      gx0 = xr[0]; gx1 = xr[1]; gg0 = gr[0]; gg1 = gr[1]; }
    SBAR();
    auto swiglu = [&](int m, int ng, unsigned (&o)[2]) {
        float hv[4];
#pragma unroll
        for (int e = 0; e < 4; ++e) { const float gt = acc[m][ng][e], up = acc[m][ng + 1][e]; hv[e] = gt * up * __builtin_amdgcn_rcpf(1.0f + __builtin_amdgcn_exp2f(-1.4426950408889634f * gt)); }
        o[0] = cvtpk(hv[0], hv[1]); o[1] = cvtpk(hv[2], hv[3]);
    };
    auto hrow = [&](int m) {
        unsigned p0[2], p1[2], p2[2], p3[2];
        swiglu(m, 0, p0); swiglu(m, 2, p1); swiglu(m, 4, p2);
        {   auto r0 = __builtin_amdgcn_permlane16_swap(p0[0], p1[0], false, false);
            auto r1 = __builtin_amdgcn_permlane16_swap(p0[1], p1[1], false, false);
            hreg[m][0] = (u32x4){r0[0], r1[0], r0[1], r1[1]}; }
        if (wc) {
            swiglu(m, 8, p3);
            auto r0 = __builtin_amdgcn_permlane16_swap(p2[0], p3[0], false, false);
            auto r1 = __builtin_amdgcn_permlane16_swap(p2[1], p3[1], false, false);
            hreg[m][1] = (u32x4){r0[0], r1[0], r0[1], r1[1]};
        } else {
            hreg[m][1] = (u32x4){p2[0], p2[1], 0u, 0u};
        }
        asm volatile("" : "+v"(hreg[m][0]), "+v"(hreg[m][1]));
    };
    hrow(0);
    SBAR();
    {
        float s = 0.f;
#pragma unroll
        for (int i = 0; i < 4; ++i) {
            s += __uint_as_float(gx0[i] << 16) * __uint_as_float(gg0[i] << 16) + __uint_as_float(gx0[i] & 0xffff0000u) * __uint_as_float(gg0[i] & 0xffff0000u);
            s += __uint_as_float(gx1[i] << 16) * __uint_as_float(gg1[i] << 16) + __uint_as_float(gx1[i] & 0xffff0000u) * __uint_as_float(gg1[i] & 0xffff0000u);
        }
#pragma unroll
        for (int o = 32; o >= 1; o >>= 1) s += __shfl_xor(s, o);
        gval = 1.0f / (1.0f + __expf(-s));
    }
    SBAR();
    hrow(1);
    SBAR();
    KSTAMP(11);
    asm volatile("s_waitcnt vmcnt(0) lgkmcnt(0)\n\ts_barrier" ::: "memory");
    KSTAMP(12);
    if (w == 0 && lane == 0) flag_set(a.flags + FLAG_Q + ct * 8 + rb, tag);
    if (lane == 0) st4w(a.g + grow, __float_as_uint(gval));
    SBAR();
    hrow(2);
    SBAR();
    pollv = 0;
    asm volatile("global_load_dword %0, %1, off sc1" : "+v"(pollv) : "v"(a.flags + FLAG_Q + ct * 8 + (lane & 7)) : "memory");
    SBAR();
    hrow(3);
    KSTAMP(13);
}
DI void store_h(const Params& a, const u32x4 (&hreg)[4][2], const int m, bf16_t* hb, const int wc, const int cperm, const int fq) {
    bf16_t* hp = hb + (size_t)m * 16 * HO_LD;
    st16w(hp + cperm, hreg[m][0]);
    if (wc) st16w(hp + 32 + cperm, hreg[m][1]); else st8w(hp + 32 + 4 * fq, (u32x2){hreg[m][1][0], hreg[m][1][1]});
}
}

namespace p3 { DI void producer_prologue(const Params& a, const unsigned lds0, const int pw, const int lane); }
namespace p2 {
constexpr int SLOT = 16384, NTILE = S_ / 128;

struct Ctx { int h, qb, rb3h; unsigned tag; bf16_t* hb; int wc, cperm, fq; };

DI bool attn_fast(const Params& a, const unsigned lds0, const Ctx& c, const int w, const u32x4 (&hreg)[4][2], unsigned pollkv, f32x16& o, float& lsum, float& gq, bf16x8& q0o, bf16x8& q1o) {
    const int lane = lane_id();
    const int h = c.h;
    const int r = lane & 31, hh = lane >> 5;
    const char* gK = (const char*)(a.kv + (size_t)h * (S_ * 32)) + w * 1024;
    const char* gV = (const char*)(a.kv + (size_t)(32 + h) * (S_ * 32)) + w * 1024;
    const unsigned voffK = (unsigned)((lane >> 2) * 64 + (((lane & 3) ^ ((lane >> 4) & 3)) * 16)), voffV = (unsigned)(lane * 16);
    const unsigned dK = lds0 + w * 1024, dV = lds0 + 8192 + w * 1024;
#define P2_KOFF(T) ((size_t)((((c.qb + ((T) >> 1)) & 7) * 2 + ((T) & 1)) * 8192))
#define P2_DMA(T, SL) do { glds16c(voffK, gK + P2_KOFF(T), dK + (SL) * SLOT); glds16c(voffV, gV + P2_KOFF(T), dV + (SL) * SLOT); } while (0)
    const bf16x8 q0 = ld128(LP(lds0 + p1::LDS_Q + (w * 32 + r) * 64 + hh * 16)), q1 = ld128(LP(lds0 + p1::LDS_Q + (w * 32 + r) * 64 + 32 + hh * 16));
    const unsigned rK0 = lds0 + r * 64 + (((0 + hh) ^ ((r >> 2) & 3)) * 16), rK1 = lds0 + r * 64 + (((2 + hh) ^ ((r >> 2) & 3)) * 16);
    const unsigned rV = lds0 + 8192 + (4 * hh + ((lane & 15) >> 2)) * 64 + ((lane >> 4) & 1) * 32 + (lane & 3) * 8;
    const short one = (short)0x3f80; const bool sel = ((lane & 15) == ((lane >> 4) & 1));
    const short sv = sel ? one : (short)0; const bf16x8 selA = {sv, sv, sv, sv, sv, sv, sv, sv};
    f32x4 ls = {0.f, 0.f, 0.f, 0.f};
#pragma unroll
    for (int i = 0; i < 16; ++i) o[i] = 0.f;
    const char* pfp0; const char* pfp1;
    { const int xg = blockIdx.x & 7, j = blockIdx.x >> 3, kh = xg & 1, cg = (xg >> 1) & 1;
      auto line = [&](int li) { const int row = li >> 3, k = kh * 2304 + row; const char* base = k < F_ ? (const char*)a.wd + (size_t)k * 2048 : (const char*)a.wo + (size_t)(k - F_) * 2048; return base + cg * 1024 + (li & 7) * 128; };
      const int l0 = j * 576 + w * 72; pfp0 = line(l0 + lane); pfp1 = line(l0 + 64 + (lane & 7)); }
    unsigned pfd = 0, pollv = 0; bool okH = false;
    const unsigned* pollp = a.flags + FLAG_H + (lane < 32 ? c.rb3h * 32 + lane : c.qb * 32 + (lane - 32));
#define P2_LDK(k0, k1, off) do { k0 = ld128(LP(rK0) + (off)); k1 = ld128(LP(rK1) + (off)); } while (0)
    KSTAMP(4);
    bf16x8 k0, k1, v0, v1;
    u32x4 p0, p1;
    f32x16 negm, sA, sB;
    P2_LDK(k0, k1, 0);
    {
        f32x16 z;
#pragma unroll
        for (int i = 0; i < 16; ++i) z[i] = 0.f;
        f32x16 s = MFMA32(k0, q0, z); s = MFMA32(k1, q1, s);
        float m = s[0];
#pragma unroll
        for (int i = 1; i < 16; ++i) m = __builtin_fmaxf(m, s[i]);
        auto rr = __builtin_amdgcn_permlane32_swap(__float_as_uint(m), __float_as_uint(m), false, false);
        m = __builtin_fmaxf(__uint_as_float(rr[0]), __uint_as_float(rr[1]));
#pragma unroll
        for (int i = 0; i < 16; ++i) negm[i] = -m;
        asm volatile("" : "+v"(negm));
    }
    sA = MFMA32(k0, q0, negm); sA = MFMA32(k1, q1, sA);
    P2_LDK(k0, k1, 2048);
    v0 = cat8(ldtr(LP(rV)), ldtr(LP(rV) + 512));
    p1 = (u32x4){0u, 0u, 0u, 0u}; v1 = v0;
    SBAR();
#define EX(C, i) C[i] = __builtin_amdgcn_exp2f(C[i])
#define CV(dst, C, i) dst = cvtpk(C[i], C[(i) + 1])
#define P2_STEP(CUR, NXT, voff, koff, vnoff, DOQK, LOADK, LOADV0, SYNC) do { \
        EX(CUR, 0); EX(CUR, 1); SBAR(); \
        o = MFMA32(v1, __builtin_bit_cast(bf16x8, p1), o); SBAR(); \
        EX(CUR, 2); ls = MFMA16(selA, __builtin_bit_cast(bf16x8, p1), ls); SBAR(); \
        v1 = cat8(ldtr(LP(rV) + (voff) + 1024), ldtr(LP(rV) + (voff) + 1536)); \
        EX(CUR, 3); CV(p0[0], CUR, 0); EX(CUR, 4); EX(CUR, 5); SBAR(); \
        if (DOQK) NXT = MFMA32(k0, q0, negm); \
        SBAR(); \
        CV(p0[1], CUR, 2); EX(CUR, 6); EX(CUR, 7); CV(p0[2], CUR, 4); SBAR(); \
        if (DOQK) NXT = MFMA32(k1, q1, NXT); \
        SBAR(); \
        SYNC; \
        if (LOADK) P2_LDK(k0, k1, koff); \
        EX(CUR, 8); CV(p0[3], CUR, 6); EX(CUR, 9); EX(CUR, 10); SBAR(); \
        o = MFMA32(v0, __builtin_bit_cast(bf16x8, p0), o); SBAR(); \
        if (LOADV0) v0 = cat8(ldtr(LP(rV) + (vnoff)), ldtr(LP(rV) + (vnoff) + 512)); \
        EX(CUR, 11); CV(p1[0], CUR, 8); EX(CUR, 12); EX(CUR, 13); SBAR(); \
        ls = MFMA16(selA, __builtin_bit_cast(bf16x8, p0), ls); SBAR(); \
        EX(CUR, 14); EX(CUR, 15); CV(p1[1], CUR, 10); CV(p1[2], CUR, 12); CV(p1[3], CUR, 14); SBAR(); \
    } while (0)
#define VMB(N) asm volatile("s_waitcnt vmcnt(" #N ")\n\ts_barrier" ::: "memory")
#define P2_TILE(T, SL, WAIT, MORE, AFTER) do { \
        P2_STEP(sA, sB, (SL) * SLOT + 0 * 2048, (SL) * SLOT + 2 * 2048, (SL) * SLOT + 1 * 2048, true, true, true, (void)0); \
        P2_STEP(sB, sA, (SL) * SLOT + 1 * 2048, (SL) * SLOT + 3 * 2048, (SL) * SLOT + 2 * 2048, true, true, true, (void)0); \
        P2_STEP(sA, sB, (SL) * SLOT + 2 * 2048, (((SL) + 1) & 3) * SLOT + 0 * 2048, (SL) * SLOT + 3 * 2048, true, MORE, true, \
                do { if (MORE) { WAIT; AFTER; } } while (0)); \
        P2_STEP(sB, sA, (SL) * SLOT + 3 * 2048, (((SL) + 1) & 3) * SLOT + 1 * 2048, (((SL) + 1) & 3) * SLOT + 0 * 2048, MORE, MORE, MORE, (void)0); \
    } while (0)
#define ST_H(m) p1::store_h(a, hreg, m, c.hb, c.wc, c.cperm, c.fq)
    const unsigned* kvp = a.flags + FLAG_Q + h * 8 + (lane & 7);
    P2_TILE(0, 0, { asm volatile("s_waitcnt vmcnt(0)" : "+v"(pollkv) :: "memory"); const bool own = (lane & 7) == c.qb;
                    if (__builtin_amdgcn_ballot_w64(!own && pollkv != c.tag) != 0ull) wait_flags(kvp, c.tag, own); }, true, { P2_DMA(2, 2); P2_DMA(3, 3); ST_H(0); });
    P2_TILE(1, 1, VMB(4), true, { P2_DMA(4, 0); ST_H(1); });
    P2_TILE(2, 2, VMB(6), true, { P2_DMA(5, 1); ST_H(2); });
    P2_TILE(3, 3, VMB(6), true, { P2_DMA(6, 2); ST_H(3); });
    P2_TILE(4, 0, VMB(6), true, { P2_DMA(7, 3); });
    P2_TILE(5, 1, VMB(4), true, { P2_DMA(8, 0); });
    P2_TILE(6, 2, VMB(2), true, { P2_DMA(9, 1); if (w == 0 && lane == 0) flag_set(a.flags + FLAG_H + c.qb * 32 + h, c.tag); });
    P2_TILE(7, 3, VMB(2), true, { P2_DMA(10, 2); });
    P2_TILE(8, 0, VMB(2), true, { P2_DMA(11, 3); });
    P2_TILE(9, 1, VMB(2), true, { P2_DMA(12, 0);
        asm volatile("global_load_dword %0, %1, off" : "+v"(pfd) : "v"(pfp0) : "memory"); asm volatile("global_load_dword %0, %1, off" : "+v"(pfd) : "v"(pfp1) : "memory");
        asm volatile("global_load_dword %0, %1, off sc1" : "+v"(pollv) : "v"(pollp) : "memory"); });
    P2_TILE(10, 2, VMB(5), true, { P2_DMA(13, 1); });
    P2_TILE(11, 3, VMB(2), true, { asm volatile("" : "+v"(pollv)); okH = __builtin_amdgcn_ballot_w64(pollv != c.tag) == 0ull; P2_DMA(14, 2); });
#undef ST_H
    P2_TILE(NTILE - 4, 0, VMB(2), true, { P2_DMA(NTILE - 1, 3); });
    P2_TILE(NTILE - 3, 1, VMB(2), true, (void)0);
    P2_STEP(sA, sB, 2 * SLOT + 0 * 2048, 2 * SLOT + 2 * 2048, 2 * SLOT + 1 * 2048, true, true, true, (void)0);
    P2_STEP(sB, sA, 2 * SLOT + 1 * 2048, 2 * SLOT + 3 * 2048, 2 * SLOT + 2 * 2048, true, true, true, (void)0);
    P2_STEP(sA, sB, 2 * SLOT + 2 * 2048, 3 * SLOT + 0 * 2048, 2 * SLOT + 3 * 2048, true, true, true,
            do { if (!okH) wait_flags(pollp, c.tag);
                 asm volatile("s_waitcnt vmcnt(0)\n\ts_barrier" : "+v"(pfd) :: "memory");
                 if (w >= 4) p3::producer_prologue(a, lds0, w - 4, lane);
                 asm volatile("global_load_dword %0, %1, off sc1" : "=v"(gq) : "v"(a.g + c.qb * 256 + w * 32 + r) : "memory"); } while (0));
    P2_STEP(sB, sA, 2 * SLOT + 3 * 2048, 3 * SLOT + 1 * 2048, 3 * SLOT + 0 * 2048, true, true, true, (void)0);
    P2_TILE(NTILE - 1, 3, (void)0, false, (void)0);
    o = MFMA32(v1, __builtin_bit_cast(bf16x8, p1), o); ls = MFMA16(selA, __builtin_bit_cast(bf16x8, p1), ls);
#undef EX
#undef CV
#undef P2_STEP
#undef P2_TILE
#undef VMB
    asm volatile("s_waitcnt vmcnt(0)" : "+v"(gq) :: "memory");
    const float l0 = __shfl(ls[0], r & 15), l1 = __shfl(ls[1], r & 15);
    lsum = (r < 16) ? l0 : l1;
    bool ok = lsum < 3.0e38f;
#pragma unroll
    for (int i = 0; i < 16; ++i) ok = ok && (__builtin_fabsf(o[i]) < 3.0e38f);
    q0o = q0; q1o = q1;
    return ok;
}

DI void attn_safe(const Params& a, const unsigned lds0, const Ctx& c, const int w, const bf16x8 q0, const bf16x8 q1, f32x16& o, float& lsum) {
    const int lane = lane_id();
    const int h = c.h;
    const int r = lane & 31, hh = lane >> 5;
    const char* gK = (const char*)(a.kv + (size_t)h * (S_ * 32)) + w * 1024;
    const char* gV = (const char*)(a.kv + (size_t)(32 + h) * (S_ * 32)) + w * 1024;
    const unsigned voffK = (unsigned)((lane >> 2) * 64 + (((lane & 3) ^ ((lane >> 4) & 3)) * 16)), voffV = (unsigned)(lane * 16);
    const unsigned dK = lds0 + w * 1024, dV = lds0 + 8192 + w * 1024;
    const unsigned rK0 = lds0 + r * 64 + (((0 + hh) ^ ((r >> 2) & 3)) * 16), rK1 = lds0 + r * 64 + (((2 + hh) ^ ((r >> 2) & 3)) * 16);
    const unsigned rV = lds0 + 8192 + (4 * hh + ((lane & 15) >> 2)) * 64 + ((lane >> 4) & 1) * 32 + (lane & 3) * 8;
#pragma unroll
    for (int i = 0; i < 16; ++i) o[i] = 0.f;
    float m = -INFINITY, l = 0.f;
#pragma unroll 1
    for (int T = 0; T < NTILE; ++T) {
        asm volatile("s_barrier" ::: "memory");
        P2_DMA(T, 0);
        asm volatile("s_waitcnt vmcnt(0)\n\ts_barrier" ::: "memory");
#pragma unroll 1
        for (int u = 0; u < 4; ++u) {
            bf16x8 k0, k1, v0, v1;
            P2_LDK(k0, k1, u * 2048);
            v0 = cat8(ldtr(LP(rV) + u * 2048), ldtr(LP(rV) + u * 2048 + 512)); v1 = cat8(ldtr(LP(rV) + u * 2048 + 1024), ldtr(LP(rV) + u * 2048 + 1536));
            f32x16 z;
#pragma unroll
            for (int i = 0; i < 16; ++i) z[i] = 0.f;
            f32x16 s = MFMA32(k0, q0, z); s = MFMA32(k1, q1, s);
            float mx = s[0];
#pragma unroll
            for (int i = 1; i < 16; ++i) mx = __builtin_fmaxf(mx, s[i]);
            auto rr = __builtin_amdgcn_permlane32_swap(__float_as_uint(mx), __float_as_uint(mx), false, false);
            mx = __builtin_fmaxf(__uint_as_float(rr[0]), __uint_as_float(rr[1]));
            const float mn = __builtin_fmaxf(m, mx);
            const float al = (mn == m) ? 1.0f : __builtin_amdgcn_exp2f(m - mn);
            const float mref = (mn == -INFINITY) ? 0.f : mn;
            m = mn;
            float ps = 0.f; u32x4 p0, p1;
#pragma unroll
            for (int i = 0; i < 16; ++i) s[i] = __builtin_amdgcn_exp2f(s[i] - mref);
#pragma unroll
            for (int i = 0; i < 4; ++i) { p0[i] = cvtpk(s[2 * i], s[2 * i + 1]); p1[i] = cvtpk(s[8 + 2 * i], s[8 + 2 * i + 1]); }
#pragma unroll
            for (int i = 0; i < 4; ++i) ps += __uint_as_float(p0[i] << 16) + __uint_as_float(p0[i] & 0xffff0000u) + __uint_as_float(p1[i] << 16) + __uint_as_float(p1[i] & 0xffff0000u);
            l = l * al + ps;
#pragma unroll
            for (int i = 0; i < 16; ++i) o[i] *= al;
            o = MFMA32(v0, __builtin_bit_cast(bf16x8, p0), o);
            o = MFMA32(v1, __builtin_bit_cast(bf16x8, p1), o);
        }
    }
    auto rr = __builtin_amdgcn_permlane32_swap(__float_as_uint(l), __float_as_uint(l), false, false);
    lsum = __uint_as_float(rr[0]) + __uint_as_float(rr[1]);
#undef P2_DMA
#undef P2_LDK
}

DI void epilogue(const Params& a, const Ctx& c, const int w, const f32x16& o, const float lsum, const float gq) {
    const int lane = lane_id();
    const int r = lane & 31, hh = lane >> 5;
    const int row = c.qb * 256 + w * 32 + r;
    const float sc = gq / lsum;
    unsigned pk[4][2];
#pragma unroll
    for (int G = 0; G < 4; ++G) { pk[G][0] = cvtpk(o[4 * G] * sc, o[4 * G + 1] * sc); pk[G][1] = cvtpk(o[4 * G + 2] * sc, o[4 * G + 3] * sc); }
    bf16_t* op = a.ho + (size_t)row * HO_LD + F_ + c.h * 32 + hh * 8;
#pragma unroll
    for (int G = 0; G < 4; G += 2) {
        auto r0 = __builtin_amdgcn_permlane32_swap(pk[G][0], pk[G + 1][0], false, false);
        auto r1 = __builtin_amdgcn_permlane32_swap(pk[G][1], pk[G + 1][1], false, false);
        st16w(op + G * 8, (u32x4){r0[0], r1[0], r0[1], r1[1]});
    }
}
}

namespace p3 {
constexpr int NS = 5, STAGE = 32768, NKT = 36;
DI int kh_of() { return blockIdx.x & 1; }
DI int rb_of() { return ((blockIdx.x & 7) >> 2) * 8 + ((blockIdx.x >> 3) & 7); }
DI int cb_of() { return (((blockIdx.x & 7) >> 1) & 1) * 4 + (blockIdx.x >> 6); }

struct Prod { unsigned voffA, voffB0, voffB1, dA, dB; const char* gA; const char* gWd; const char* gWo; int nH; };
DI Prod prod_setup(const Params& a, const unsigned lds0, const int pw, const int lane) {
    const int kh = kh_of(), rb = rb_of(), cb = cb_of();
    Prod P;
    P.nH = kh ? 20 : 36;
    P.voffA = (unsigned)((lane >> 3) * (HO_LD * 2) + (((lane & 7) ^ (lane >> 3)) * 16));
    P.voffB0 = (unsigned)((lane >> 4) * 2048 + (((lane & 15) ^ ((lane >> 4) << 1)) * 16));
    P.voffB1 = (unsigned)((lane >> 4) * 2048 + (((lane & 15) ^ (((lane >> 4) | 4) << 1)) * 16));
    P.gA = (const char*)a.ho + (size_t)(rb * 128 + pw * 32) * (HO_LD * 2) + (kh ? 2304 * 2 : 0);
    P.gWd = (const char*)a.wd + (size_t)((kh ? 2304 : 0) + pw * 16) * 2048 + cb * 256;
    P.gWo = (const char*)a.wo + (size_t)(pw * 16) * 2048 + cb * 256 - (size_t)P.nH * (64 * 2048);
    P.dA = lds0 + pw * 4096; P.dB = lds0 + 16384 + pw * 4096;
    return P;
}
DI void prod_issue(const Prod& P, const int t, const unsigned so) {
    const char* sa = P.gA + (size_t)t * 128; const char* sb = (t < P.nH ? P.gWd : P.gWo) + (size_t)t * (64 * 2048);
#pragma unroll
    for (int i = 0; i < 4; ++i) glds16c(P.voffA, sa + (size_t)i * 8 * (HO_LD * 2), P.dA + so + i * 1024);
#pragma unroll
    for (int i = 0; i < 4; ++i) glds16((i & 2) ? P.voffB1 : P.voffB0, sb + i * 4 * 2048, P.dB + so + i * 1024);
}
DI void producer_prologue(const Params& a, const unsigned lds0, const int pw, const int lane) {
    const Prod P = prod_setup(a, lds0, pw, lane);
#pragma unroll
    for (int t = 0; t < 3; ++t) prod_issue(P, t, (t + 2) * STAGE);
}

DI void body(const Params& a, const unsigned lds0, const int w, const unsigned tag) {
    const int lane = lane_id();
    const int kh = kh_of(), rb = rb_of(), cb = cb_of();
    const int tile = rb * 8 + cb;
    const int nH = kh ? 20 : 36;
    if (w >= 4) {
        const Prod P = prod_setup(a, lds0, w - 4, lane);
        prod_issue(P, 3, 0);
        unsigned so = STAGE;
        const unsigned* pollp = a.flags + FLAG_O + (rb >> 1) * 32 + (lane & 31);
#define P3_PSTEP(t) do { asm volatile("s_waitcnt vmcnt(24)\n\ts_barrier" ::: "memory"); \
            if ((t) + NS - 1 < NKT) prod_issue(P, (t) + NS - 1, so); \
            so = (so == (NS - 1) * STAGE) ? 0u : so + STAGE; } while (0)
        for (int t = 0; t <= 10; ++t) P3_PSTEP(t);
        unsigned pollv = 0;
        asm volatile("global_load_dword %0, %1, off sc1" : "+v"(pollv) : "v"(pollp) : "memory");
        P3_PSTEP(11); P3_PSTEP(12);
        asm volatile("s_waitcnt vmcnt(16)" : "+v"(pollv) :: "memory");
        const bool okO = __builtin_amdgcn_ballot_w64(pollv != tag) == 0ull;
        for (int t = 13; t <= 15; ++t) P3_PSTEP(t);
        if (kh && !okO) wait_flags(pollp, tag);
        for (int t = 16; t < NKT - (NS - 2); ++t) P3_PSTEP(t);
#undef P3_PSTEP
        asm volatile("s_waitcnt vmcnt(16)\n\ts_barrier" ::: "memory");
        asm volatile("s_waitcnt vmcnt(8)\n\ts_barrier" ::: "memory");
        asm volatile("s_waitcnt vmcnt(0)\n\ts_barrier" ::: "memory");
        asm volatile("s_barrier" ::: "memory");
        return;
    }
    const int wr = w & 1, wc = w >> 1, fr = lane & 15, fq = lane >> 4;
    const int q = (lane & 15) >> 2, p4 = lane & 3, sR = q | ((fq & 1) << 2);
    unsigned rA[3][2], rB[3][4];
#pragma unroll
    for (int sp = 0; sp < 3; ++sp) {
#pragma unroll
        for (int ks = 0; ks < 2; ++ks) { rA[sp][ks] = lds0 + sp * 2 * STAGE + (wr * 64 + fr) * 128 + (((fq + 4 * ks) ^ (fr & 7)) * 16); asm volatile("" : "+v"(rA[sp][ks])); }
#pragma unroll
        for (int n = 0; n < 4; ++n) { rB[sp][n] = lds0 + sp * 2 * STAGE + 16384 + (((8 * fq + q) * 256 + 32 * sR + 8 * p4) ^ ((wc * 4 + n) << 5)); asm volatile("" : "+v"(rB[sp][n])); }
    }
    f32x4 acc[4][4];
#pragma unroll
    for (int m = 0; m < 4; ++m)
#pragma unroll
        for (int n = 0; n < 4; ++n) acc[m][n] = (f32x4){0.f, 0.f, 0.f, 0.f};
    bf16x8 Af[2][4]; s16x4 Bh[2][4][2];
#define P3_RD(buf, i, SL, ks) do { if ((i) < 4) Af[buf][i] = ld128(LP(rA[(SL) >> 1][ks]) + ((SL) & 1) * STAGE + (i) * 2048); \
        else Bh[buf][((i) - 4) >> 1][((i) - 4) & 1] = ldtr(LP(rB[(SL) >> 1][((i) - 4) >> 1]) + ((SL) & 1) * STAGE + (ks) * 8192 + (((i) - 4) & 1) * 1024); } while (0)
    const int row0 = rb * 128 + wr * 64 + fr;
    float omg[4];
#pragma unroll
    for (int m = 0; m < 4; ++m) omg[m] = 1.0f - ldf_dev(a.g + row0 + m * 16);
    asm volatile("s_barrier" ::: "memory");
    KSTAMP(7);
#pragma unroll
    for (int i = 0; i < 12; ++i) P3_RD(0, i, 2, 0);
#define P3_TILE(t, SL, MORE, OFLAG) do { \
        _Pragma("unroll") for (int i = 0; i < 16; ++i) { \
            const int n = i >> 2, m = i & 3; \
            MFMA16A(acc[m][n], cat8(Bh[0][n][0], Bh[0][n][1]), Af[0][m]); \
            SBAR(); \
            if (i < 12) P3_RD(1, i, SL, 1); \
            SBAR(); } \
        if ((OFLAG) && (t) == 2) {        \
            asm volatile("s_waitcnt vmcnt(0) lgkmcnt(0)\n\ts_barrier" ::: "memory"); \
            if (w == 0 && lane == 0) flag_set(a.flags + FLAG_O + (blockIdx.x >> 3 & 7) * 32 + (blockIdx.x & 7) * 4 + (blockIdx.x >> 6), tag); \
        } else if (MORE) asm volatile("s_waitcnt lgkmcnt(0)\n\ts_barrier" ::: "memory");         \
        SBAR(); \
        _Pragma("unroll") for (int i = 0; i < 16; ++i) { \
            const int n = i >> 2, m = i & 3; \
            MFMA16A(acc[m][n], cat8(Bh[1][n][0], Bh[1][n][1]), Af[1][m]); \
            SBAR(); \
            if (i < 12 && (MORE)) P3_RD(0, i, ((SL) + 1) % NS, 0); \
            SBAR(); } \
        if ((t) + 1 == nH) { asm volatile("s_nop 7\n\ts_nop 7" ::: "memory"); SBAR(); _Pragma("unroll") for (int m = 0; m < 4; ++m) _Pragma("unroll") for (int n = 0; n < 4; ++n) { acc[m][n] *= omg[m]; asm volatile("" : "+v"(acc[m][n])); } \
            asm volatile("s_nop 3" ::: "memory"); SBAR(); } \
    } while (0)
    for (int t = 0; t < 35; t += 5) { P3_TILE(t, 2, true, false); P3_TILE(t + 1, 3, true, false); P3_TILE(t + 2, 4, true, true); P3_TILE(t + 3, 0, true, false); P3_TILE(t + 4, 1, true, false); }
    P3_TILE(35, 2, false, false);
    asm volatile("s_nop 7\n\ts_nop 7" ::: "memory"); SBAR();
    KSTAMP(8);
    if (wc != kh) {
        float* xs = a.xch + ((size_t)(tile * 2 + kh) * 2 + wr) * 4096 + lane * 4;
#pragma unroll
        for (int m = 0; m < 4; ++m)
#pragma unroll
            for (int n = 0; n < 4; ++n) st16w(xs + (m * 4 + n) * 256, acc[m][n]);
        asm volatile("s_waitcnt vmcnt(0)" ::: "memory");
    }
    asm volatile("s_barrier" ::: "memory");
    if (w == 0 && lane == 0) flag_set(a.flags + FLAG_X + (tile * 2 + kh) * 16, tag);
    if (wc == kh) {
        const unsigned* pf = a.flags + FLAG_X + (tile * 2 + (kh ^ 1)) * 16;
        unsigned spins = 0;
        while ((unsigned)__builtin_amdgcn_readfirstlane(flag_get(pf)) != tag) { __builtin_amdgcn_s_sleep(1); if (++spins > SPIN_MAX) break; }
        asm volatile("" ::: "memory");
        const float* xr = a.xch + ((size_t)(tile * 2 + (kh ^ 1)) * 2 + wr) * 4096 + lane * 4;
        f32x4 pv[4][4];
#pragma unroll
        for (int m = 0; m < 4; ++m)
#pragma unroll
            for (int n = 0; n < 4; ++n) asm volatile("global_load_dwordx4 %0, %1, off sc1" : "=v"(pv[m][n]) : "v"(xr + (m * 4 + n) * 256) : "memory");
        asm volatile("s_waitcnt vmcnt(0)" : "+v"(pv[0][0]), "+v"(pv[0][1]), "+v"(pv[0][2]), "+v"(pv[0][3]), "+v"(pv[1][0]), "+v"(pv[1][1]), "+v"(pv[1][2]), "+v"(pv[1][3]),
                     "+v"(pv[2][0]), "+v"(pv[2][1]), "+v"(pv[2][2]), "+v"(pv[2][3]), "+v"(pv[3][0]), "+v"(pv[3][1]), "+v"(pv[3][2]), "+v"(pv[3][3]) :: "memory");
        const int cperm = ((fq & 1) << 4) | ((fq >> 1) << 3);
        bf16_t* ob = a.out + (size_t)row0 * D_ + cb * 128 + wc * 64 + cperm;
#pragma unroll
        for (int m = 0; m < 4; ++m)
#pragma unroll
            for (int n = 0; n < 4; n += 2) {
                const f32x4 lo = acc[m][n] + pv[m][n], hi = acc[m][n + 1] + pv[m][n + 1];
                auto r0 = __builtin_amdgcn_permlane16_swap(cvtpk(lo[0], lo[1]), cvtpk(hi[0], hi[1]), false, false);
                auto r1 = __builtin_amdgcn_permlane16_swap(cvtpk(lo[2], lo[3]), cvtpk(hi[2], hi[3]), false, false);
                *(u32x4*)(ob + (size_t)m * 16 * D_ + n * 16) = (u32x4){r0[0], r1[0], r0[1], r1[1]};
            }
    }
#undef P3_RD
#undef P3_TILE
}
}

constexpr int LDS_BYTES = 163840;

__global__ __launch_bounds__(512, 2) void fused_kernel(Params a) {
    extern __shared__ __attribute__((aligned(16))) char smem[];
    const unsigned lds0 = (unsigned)(uintptr_t)smem;
    const int w = __builtin_amdgcn_readfirstlane(threadIdx.x >> 6);
    const int xg = blockIdx.x & 7, j = blockIdx.x >> 3;
    const int rb = j & 7, ct = xg * 4 + (j >> 3);
    KSTAMP(0);
    unsigned tag;
    u32x4 hreg[4][2]; float gval; unsigned pollkv;
    {
        f32x4 acc[4][10];
        p1::mainloop(a, lds0, w, rb, ct, acc, tag);
        KSTAMP(2);
        p1::epilogue(a, lds0, acc, w, rb, ct, tag, hreg, gval, pollkv);
    }
    const int lane = lane_id();
    KSTAMP(3);
    p2::Ctx c;
    c.h = ct; c.qb = rb; c.rb3h = p3::rb_of() >> 1; c.tag = tag; c.wc = w >> 2; c.fq = lane >> 4; c.cperm = ((c.fq & 1) << 4) | ((c.fq >> 1) << 3);
    c.hb = a.ho + (size_t)(rb * 256 + (w & 3) * 64 + (lane & 15)) * HO_LD + ct * 112 + c.wc * 48;
    f32x16 o; float lsum, gq; bf16x8 q0, q1;
    const bool ok = p2::attn_fast(a, lds0, c, w, hreg, pollkv, o, lsum, gq, q0, q1);
    KSTAMP(5);
    {
        asm volatile("s_barrier" ::: "memory");
        const bool wok = __builtin_amdgcn_ballot_w64(!ok) == 0ull;
        volatile LAS unsigned* fl = (volatile LAS unsigned*)(size_t)lds0;
        if (lane == 0) fl[w] = wok ? 0u : 1u;
        asm volatile("s_waitcnt lgkmcnt(0)\n\ts_barrier" ::: "memory");
        const unsigned bad = fl[lane & 7];
        if (__builtin_amdgcn_ballot_w64(bad != 0u) != 0ull) p2::attn_safe(a, lds0, c, w, q0, q1, o, lsum);
    }
    p2::epilogue(a, c, w, o, lsum, gq);
    asm volatile("s_waitcnt lgkmcnt(0)\n\ts_barrier" ::: "memory");
    KSTAMP(6);
    p3::body(a, lds0, w, tag);
    KSTAMP(9);
    if (blockIdx.x == 0 && w == 0) {
        unsigned spins = 0;
        for (int i = 0; i < 4; ++i)
            while (__builtin_amdgcn_ballot_w64(flag_get(a.flags + FLAG_H + i * 64 + lane) != tag) != 0ull) { __builtin_amdgcn_s_sleep(2); if (++spins > SPIN_MAX) break; }
        if (lane == 0) flag_set(a.flags + FLAG_EPOCH, tag);
    }
}

extern "C" void kernel_launch(void* const* d_in, const int* in_sizes, int n_in, void* d_out, int out_size, void* d_ws, size_t ws_size, hipStream_t stream) {
    char* ws = (char*)d_ws;
    Params p;
    p.x = (const bf16_t*)d_in[0]; p.wqkv = (const bf16_t*)d_in[1]; p.wo = (const bf16_t*)d_in[2]; p.wg = (const bf16_t*)d_in[4]; p.wu = (const bf16_t*)d_in[5];
    p.wd = (const bf16_t*)d_in[6]; p.gw = (const bf16_t*)d_in[7]; p.out = (bf16_t*)d_out;
    p.kv = (bf16_t*)ws; p.ho = (bf16_t*)(ws + (16 << 20)); p.g = (float*)(ws + (36 << 20)); p.xch = (float*)(ws + (40 << 20)); p.flags = (unsigned*)(ws + (50 << 20));
    (void)hipFuncSetAttribute((const void*)fused_kernel, hipFuncAttributeMaxDynamicSharedMemorySize, LDS_BYTES);
    fused_kernel<<<256, 512, LDS_BYTES, stream>>>(p);
}
```

```cpp
#include <hip/hip_runtime.h>

#include <hip/hip_runtime.h>
#include <stdint.h>
#define LAS __attribute__((address_space(3)))
#define DI __device__ __forceinline__
typedef unsigned short bf16_t;
typedef short bf16x8 __attribute__((ext_vector_type(8)));
typedef short s16x4 __attribute__((ext_vector_type(4)));
typedef short v4i16_t __attribute__((ext_vector_type(4)));
typedef float f32x4 __attribute__((ext_vector_type(4)));
typedef float f32x16 __attribute__((ext_vector_type(16)));
typedef unsigned u32x4 __attribute__((ext_vector_type(4)));
typedef unsigned u32x2 __attribute__((ext_vector_type(2)));
typedef float f32x2 __attribute__((ext_vector_type(2)));
typedef __bf16 bf16x2_t __attribute__((ext_vector_type(2)));
#define SBAR() __builtin_amdgcn_sched_barrier(0)
#define LP(x) ((const LAS char*)(size_t)(x))

constexpr int S_ = 2048, D_ = 1024, H_ = 32, DH_ = 32, F_ = 3584, HO_LD = F_ + D_;

DI unsigned cvtpk(float lo, float hi) { f32x2 v = {lo, hi}; bf16x2_t b = __builtin_convertvector(v, bf16x2_t); return __builtin_bit_cast(unsigned, b); }
DI void glds16(unsigned voff, const void* sbase, unsigned lds_dst) {
    asm volatile("s_mov_b32 m0, %2\n\ts_nop 0\n\tglobal_load_lds_dwordx4 %0, %1" :: "v"(voff), "s"(sbase), "s"(lds_dst) : "memory");
}
DI void glds16c(unsigned voff, const void* sbase, unsigned lds_dst) {
    asm volatile("s_mov_b32 m0, %2\n\ts_nop 0\n\tglobal_load_lds_dwordx4 %0, %1 sc1" :: "v"(voff), "s"(sbase), "s"(lds_dst) : "memory");
}
DI void st16w(void* p, u32x4 v) { asm volatile("global_store_dwordx4 %0, %1, off sc1\n\ts_nop 1" :: "v"(p), "v"(v) : "memory"); }
DI void st16w(void* p, f32x4 v) { asm volatile("global_store_dwordx4 %0, %1, off sc1\n\ts_nop 1" :: "v"(p), "v"(v) : "memory"); }
DI void st8w(void* p, u32x2 v) { asm volatile("global_store_dwordx2 %0, %1, off sc1\n\ts_nop 1" :: "v"(p), "v"(v) : "memory"); }
DI void st4w(void* p, unsigned v) { asm volatile("global_store_dword %0, %1, off sc1" :: "v"(p), "v"(v) : "memory"); }
DI s16x4 ldtr(const LAS char* p) { return __builtin_bit_cast(s16x4, __builtin_amdgcn_ds_read_tr16_b64_v4i16((LAS v4i16_t*)p)); }
DI bf16x8 ld128(const LAS char* p) { return *(const LAS bf16x8*)p; }
DI bf16x8 cat8(s16x4 lo, s16x4 hi) { return __builtin_shufflevector(lo, hi, 0, 1, 2, 3, 4, 5, 6, 7); }
DI int lane_id() { int l; asm volatile("v_mbcnt_lo_u32_b32 %0, -1, 0\n\tv_mbcnt_hi_u32_b32 %0, -1, %0" : "=v"(l)); return l; }
#define MFMA16(a, b, c) __builtin_amdgcn_mfma_f32_16x16x32_bf16(a, b, c, 0, 0, 0)
#define MFMA16A(acc, a, b) asm volatile("v_mfma_f32_16x16x32_bf16 %0, %1, %2, %0" : "+v"(acc) : "v"(a), "v"(b))
#define MFMA32(a, b, c) __builtin_amdgcn_mfma_f32_32x32x16_bf16(a, b, c, 0, 0, 0)

#define KSTAMP(i)
#define TSTAMP(v, k)
#define TSTORE(v, row)

struct Params {
    const bf16_t* x; const bf16_t* wqkv; const bf16_t* wo; const bf16_t* wg; const bf16_t* wu; const bf16_t* wd; const bf16_t* gw;
    bf16_t* out;
    bf16_t* kv;
    bf16_t* ho;
    float* g;
    float* xch;
    unsigned* flags;
};
constexpr int FLAG_EPOCH = 0, FLAG_Q = 64, FLAG_H = FLAG_Q + 256, FLAG_O = FLAG_H + 256, FLAG_X = FLAG_O + 256;
constexpr unsigned SPIN_MAX = 1u << 21;

DI void flag_set(unsigned* p, unsigned tag) { __hip_atomic_store(p, tag, __ATOMIC_RELAXED, __HIP_MEMORY_SCOPE_AGENT); }
DI unsigned flag_get(const unsigned* p) { return __hip_atomic_load(p, __ATOMIC_RELAXED, __HIP_MEMORY_SCOPE_AGENT); }
DI void wait_flags(const unsigned* p, unsigned tag, bool skip = false) {
    unsigned spins = 0;
    while (__builtin_amdgcn_ballot_w64(!skip && flag_get(p) != tag) != 0ull) { __builtin_amdgcn_s_sleep(2); if (++spins > SPIN_MAX) break; }
    asm volatile("" ::: "memory");
}
DI float ldf_dev(const float* p) { return __hip_atomic_load(p, __ATOMIC_RELAXED, __HIP_MEMORY_SCOPE_AGENT); }

namespace p1 {
constexpr int A_STAGE = 32768, B_STAGE = 49152, B_BASE = 65536;
constexpr int NT = D_ / 64;
constexpr float QSCALE = 0.17677669529663687f * 1.4426950408889634f;
constexpr int PD = 3;

DI void mainloop(const Params& a, const unsigned lds0, const int w, const int rb, const int ct, f32x4 (&acc)[4][10], unsigned& tag) {
    const int lane = lane_id();
    const int wr = w & 3, wc = w >> 2;
    unsigned epoch;
    asm volatile("global_load_dword %0, %1, off sc1" : "=v"(epoch) : "v"(a.flags + FLAG_EPOCH) : "memory");
    const unsigned voffA = (unsigned)((lane >> 3) * 2048 + (((lane & 7) ^ (lane >> 3)) * 16));
    const int sB = (lane >> 4) | (((w >> 1) & 1) << 2);
    int chq = (lane & 15) ^ (sB << 1), chg = chq;
    if (chq >= 12) chq -= 4;
    if (chg >= 14) chg -= 2;
    const unsigned voffQ = (unsigned)((lane >> 4) * 6144 + (chq >> 2) * 2048 + (chq & 3) * 16), voffG = (unsigned)((lane >> 4) * 7168 + chg * 16);
    const char* gA = (const char*)a.x + (size_t)(rb * 256 + w * 32) * 2048;
    const char* gQ = (const char*)a.wqkv + (size_t)(w * 4) * 6144 + ct * 64;
    const char* gG = (const char*)a.wg + (size_t)(w * 4) * 7168 + ct * 224;
    const char* gU = (const char*)a.wu + (size_t)(w * 4) * 7168 + ct * 224;
    const unsigned dA = lds0 + w * 4096, dB = lds0 + B_BASE + w * 1024;
#define P1_DMA(p, t, st) do { \
        if ((p) < 4) glds16(voffA, gA + (size_t)(t) * 128 + (p) * 8 * 2048, dA + (st) * A_STAGE + (p) * 1024); \
        else if ((p) == 4 || (p) == 7) glds16(voffQ, gQ + (size_t)(t) * (64 * 6144) + ((p) == 7 ? 32 * 6144 : 0), dB + (st) * B_STAGE + ((p) == 7 ? 8192 : 0)); \
        else if ((p) == 5 || (p) == 8) glds16(voffG, gG + (size_t)(t) * (64 * 7168) + ((p) == 8 ? 32 * 7168 : 0), dB + (st) * B_STAGE + 16384 + ((p) == 8 ? 8192 : 0)); \
        else glds16(voffG, gU + (size_t)(t) * (64 * 7168) + ((p) == 9 ? 32 * 7168 : 0), dB + (st) * B_STAGE + 32768 + ((p) == 9 ? 8192 : 0)); } while (0)
#pragma unroll
    for (int pc = 0; pc < 10; ++pc) P1_DMA(pc, 0, 0);
#pragma unroll
    for (int pc = 0; pc < 10; ++pc) P1_DMA(pc, 1, 1);
    const int fr = lane & 15, fq = lane >> 4;
    const unsigned rA0 = lds0 + (wr * 64 + fr) * 128 + ((fq ^ (fr & 7)) * 16);
    const unsigned rA1 = lds0 + (wr * 64 + fr) * 128 + (((fq + 4) ^ (fr & 7)) * 16);
    const int q = (lane & 15) >> 2, p = lane & 3, sR = q | ((fq & 1) << 2);
    const int bbase = (8 * fq + q) * 256 + 32 * sR + 8 * p;
    unsigned rB[6];
#pragma unroll
    for (int jj = 0; jj < 4; ++jj) rB[jj] = lds0 + B_BASE + (bbase ^ ((3 * wc + jj) << 5));
    rB[4] = wc ? rB[3] + 16384 : rB[0];
    rB[5] = wc ? rB[3] + 32768 : rB[3];
#pragma unroll
    for (int m = 0; m < 4; ++m)
#pragma unroll
        for (int n = 0; n < 10; ++n) acc[m][n] = (f32x4){0.f, 0.f, 0.f, 0.f};
    bf16x8 Af[2][4];
    bf16x8 Bf[PD + 1];
#define P1_BADDR(n) ((n) < 6 ? LP(rB[(n) >> 1]) + (((n) & 1) ? 32768 : 16384) : (n) == 6 ? LP(rB[1]) : (n) == 7 ? LP(rB[2]) : (n) == 8 ? LP(rB[4]) : LP(rB[5]))
#define P1_LDB(dst, n, ks) do { const LAS char* _p = P1_BADDR(n) + (ks) * 8192; dst = cat8(ldtr(_p), ldtr(_p + 1024)); } while (0)
#define P1_LDA(m, ks, so) Af[ks][m] = ld128(LP((ks) ? rA1 : rA0) + (so) + (m) * 2048)
#define P1_TOGGLE(CUR) do { _Pragma("unroll") for (int jj = 0; jj < 6; ++jj) { rB[jj] += (CUR) ? -B_STAGE : B_STAGE; asm volatile("" : "+v"(rB[jj])); } } while (0)
#define P1_SYNC(N) asm volatile("s_waitcnt vmcnt(" #N ") lgkmcnt(0)\n\ts_barrier" ::: "memory")
    asm volatile("s_waitcnt vmcnt(13)\n\ts_barrier" ::: "memory");
    asm volatile("" : "+v"(epoch));
    tag = (unsigned)__builtin_amdgcn_readfirstlane(epoch) + 1u;
    KSTAMP(1);
#pragma unroll
    for (int m = 0; m < 4; ++m) P1_LDA(m, 0, 0);
#pragma unroll
    for (int s = 0; s < PD; ++s) P1_LDB(Bf[s], s, 0);

    constexpr int SM = 10 - PD, SE = 20 - PD;
#define P1_W1SLOT(r) ((r) == 0 ? 0 : (r) == 1 ? 1 : (r) == 3 ? 2 : (r) == 4 ? 3 : (r) == 6 ? 4 : (r) == 7 ? 5 : (r) == 8 ? 6 : -1)
#define P1_W2SLOT(r) ((r) == 0 ? 7 : (r) == 3 ? 8 : (r) == 6 ? 9 : -1)
#define P1_TILE(t, CUR, VM_M, VM_E, LAST, W1ON, W2ON, W2PREV) do { \
        _Pragma("unroll") for (int s = 0; s < 20; ++s) { \
            const int ks = s / 10, n = s % 10; \
            if (s == SM) P1_SYNC(VM_M); \
            if (s == SE && !(LAST)) { P1_SYNC(VM_E); P1_TOGGLE(CUR); } \
            { const int sp = s + PD; \
              if (sp < 20) P1_LDB(Bf[sp % (PD + 1)], sp % 10, sp / 10); \
              else if (!(LAST)) P1_LDB(Bf[sp % (PD + 1)], sp - 20, 0); } \
            if (s < SM && s >= SM - 7 && ((s - (SM - 7)) & 1) == 0) P1_LDA((s - (SM - 7)) / 2, 1, (CUR) * A_STAGE); \
            if (!(LAST) && s >= SE) { \
                if (s == SE) { P1_LDA(0, 0, (1 - (CUR)) * A_STAGE); P1_LDA(1, 0, (1 - (CUR)) * A_STAGE); } \
                if (s == SE + 1) P1_LDA(2, 0, (1 - (CUR)) * A_STAGE); \
                if (s == SE + 2) P1_LDA(3, 0, (1 - (CUR)) * A_STAGE); } \
            if ((W1ON) && s >= SM && s < SE) { const int pc = P1_W1SLOT(s - SM); if (pc >= 0) P1_DMA(pc, (t) + 2, (CUR)); } \
            if ((W2ON) && s >= SE) { const int pc = P1_W2SLOT(s - SE); if (pc >= 0) P1_DMA(pc, (t) + 2, (CUR)); } \
            if ((W2PREV) && s < SM) { const int pc = P1_W2SLOT(s + 20 - SE); if (pc >= 0) P1_DMA(pc, (t) + 1, 1 - (CUR)); } \
            SBAR(); \
            _Pragma("unroll") for (int m = 0; m < 4; ++m) acc[m][n] = MFMA16(Bf[s % (PD + 1)], Af[ks][m], acc[m][n]); \
            SBAR(); \
        } } while (0)
    static_assert(20 % (PD + 1) == 0, "ring index must be static across tiles");
    P1_TILE(0, 0, 10, 10, false, true, true, false);
    for (int t = 1; t < NT - 3; t += 2) {
        P1_TILE(t, 1, 10, 10, false, true, true, true);
        P1_TILE(t + 1, 0, 10, 10, false, true, true, true);
    }
    P1_TILE(NT - 3, 1, 10, 10, false, true, true, true);
    P1_TILE(NT - 2, 0, 10, 3, false, false, false, true);
    P1_TILE(NT - 1, 1, 0, 0, true, false, false, false);
#undef P1_DMA
#undef P1_BADDR
#undef P1_LDB
#undef P1_LDA
#undef P1_TILE
#undef P1_TOGGLE
#undef P1_SYNC
}

constexpr int LDS_Q = 65536;
constexpr int LDS_H = 81920;
constexpr int LDS_G = LDS_H + 256 * 224;
constexpr int LDS_VT = 139520;
DI void epilogue(const Params& a, const unsigned lds0, f32x4 (&acc)[4][10], const int w, const int rb, const int ct, const unsigned tag) {
    const int lane = lane_id();
    const int wr = w & 3, wc = w >> 2, fr = lane & 15, fq = lane >> 4;
    const int row0 = rb * 256 + wr * 64 + fr;
    const int cperm = ((fq & 1) << 4) | ((fq >> 1) << 3);
    asm volatile("s_barrier" ::: "memory");
    {
        auto pack = [&](int m, int nlo, int nhi, float sc) {
            const f32x4 lo = acc[m][nlo] * sc, hi = acc[m][nhi] * sc;
            auto r0 = __builtin_amdgcn_permlane16_swap(cvtpk(lo[0], lo[1]), cvtpk(hi[0], hi[1]), false, false);
            auto r1 = __builtin_amdgcn_permlane16_swap(cvtpk(lo[2], lo[3]), cvtpk(hi[2], hi[3]), false, false);
            return (u32x4){r0[0], r1[0], r0[1], r1[1]}; };
        const int lrow = (wr & 1) * 64 + fr;
        const unsigned slot = lds0 + (wr >> 1) * 16384;
        bf16_t* gdst = a.kv + (size_t)ct * (S_ * 32) + (size_t)row0 * 32 + cperm;
        if (wc) {
#pragma unroll
            for (int m = 0; m < 4; ++m) *(LAS u32x4*)(size_t)(lds0 + LDS_VT + (wr * 64 + m * 16 + fr) * 64 + cperm * 2) = pack(m, 6, 7, 1.0f);
        } else {
#pragma unroll
            for (int m = 0; m < 4; ++m) { const u32x4 v = pack(m, 7, 9, 1.0f); st16w(gdst + m * 16 * 32, v);
                *(LAS u32x4*)(size_t)(slot + (lrow + m * 16) * 64 + (((cperm >> 3) ^ (((lrow + m * 16) >> 2) & 3)) * 16)) = v; }
#pragma unroll
            for (int m = 0; m < 4; ++m) *(LAS u32x4*)(size_t)(lds0 + LDS_Q + (wr * 64 + m * 16 + fr) * 64 + cperm * 2) = pack(m, 8, 6, QSCALE);
        }
    }
    asm volatile("s_waitcnt lgkmcnt(0)\n\ts_barrier" ::: "memory");
    {
        const int g = lane >> 4, i16 = lane & 15;
#pragma unroll
        for (int j = 0; j < 2; ++j) {
            const int d = 16 * j + i16;
            const unsigned src = lds0 + LDS_VT + (w * 32 + g * 8 + (i16 >> 2)) * 64 + j * 32 + (i16 & 3) * 8;
            const bf16x8 v = cat8(ldtr(LP(src)), ldtr(LP(src) + 256));
            const int ch = (w & 3) * 4 + g, tl = w >> 2;
            st16w((char*)(a.kv + (size_t)(32 + ct) * (S_ * 32)) + (size_t)(rb * 2 + tl) * 8192 + d * 256 + ch * 16, __builtin_bit_cast(u32x4, v));
            *(LAS bf16x8*)(size_t)(lds0 + tl * 16384 + 8192 + d * 256 + ((ch ^ (d & 15)) * 16)) = v;
        }
    }
    SBAR();
    KSTAMP(10);
    const int grow = rb * 256 + ct * 8 + w;
    u32x4 gx0, gx1, gg0, gg1;
    { const u32x4* xr = (const u32x4*)(a.x + (size_t)grow * D_) + lane * 2; const u32x4* gr = (const u32x4*)(a.gw) + lane * 2;
      gx0 = xr[0]; gx1 = xr[1]; gg0 = gr[0]; gg1 = gr[1]; }
    SBAR();
    auto swiglu = [&](int m, int ng, unsigned (&o)[2]) {
        float hv[4];
#pragma unroll
        for (int e = 0; e < 4; ++e) { const float gt = acc[m][ng][e], up = acc[m][ng + 1][e]; hv[e] = gt * up * __builtin_amdgcn_rcpf(1.0f + __builtin_amdgcn_exp2f(-1.4426950408889634f * gt)); }
        o[0] = cvtpk(hv[0], hv[1]); o[1] = cvtpk(hv[2], hv[3]);
    };
    auto hrow = [&](int m) {
        unsigned p0[2], p1[2], p2[2], p3[2];
        swiglu(m, 0, p0); swiglu(m, 2, p1); swiglu(m, 4, p2);
        const unsigned hp = lds0 + LDS_H + (wr * 64 + m * 16 + fr) * 224 + wc * 96;
        {   auto r0 = __builtin_amdgcn_permlane16_swap(p0[0], p1[0], false, false);
            auto r1 = __builtin_amdgcn_permlane16_swap(p0[1], p1[1], false, false);
            *(LAS u32x4*)(size_t)(hp + cperm * 2) = (u32x4){r0[0], r1[0], r0[1], r1[1]}; }
        if (wc) {
            swiglu(m, 8, p3);
            auto r0 = __builtin_amdgcn_permlane16_swap(p2[0], p3[0], false, false);
            auto r1 = __builtin_amdgcn_permlane16_swap(p2[1], p3[1], false, false);
            *(LAS u32x4*)(size_t)(hp + 64 + cperm * 2) = (u32x4){r0[0], r1[0], r0[1], r1[1]};
        } else {
            *(LAS u32x2*)(size_t)(hp + 64 + fq * 8) = (u32x2){p2[0], p2[1]};
        }
    };
    float gval;
    hrow(0);
    SBAR();
    {
        float s = 0.f;
#pragma unroll
        for (int i = 0; i < 4; ++i) {
            s += __uint_as_float(gx0[i] << 16) * __uint_as_float(gg0[i] << 16) + __uint_as_float(gx0[i] & 0xffff0000u) * __uint_as_float(gg0[i] & 0xffff0000u);
            s += __uint_as_float(gx1[i] << 16) * __uint_as_float(gg1[i] << 16) + __uint_as_float(gx1[i] & 0xffff0000u) * __uint_as_float(gg1[i] & 0xffff0000u);
        }
#pragma unroll
        for (int o = 32; o >= 1; o >>= 1) s += __shfl_xor(s, o);
        gval = 1.0f / (1.0f + __expf(-s));
    }
    SBAR();
    hrow(1);
    SBAR();
    KSTAMP(11);
    asm volatile("s_waitcnt vmcnt(0) lgkmcnt(0)\n\ts_barrier" ::: "memory");
    KSTAMP(12);
    if (w == 0 && lane == 0) flag_set(a.flags + FLAG_Q + ct * 8 + rb, tag);
    if (lane == 0) *(LAS float*)(size_t)(lds0 + LDS_G + w * 4) = gval;
    SBAR();
    hrow(2);
    SBAR();
    hrow(3);
    KSTAMP(13);
    asm volatile("s_waitcnt lgkmcnt(0)\n\ts_barrier" ::: "memory");
}
}

namespace p3 { DI void producer_prologue(const Params& a, const unsigned lds0, const int pw, const int lane); }
namespace p2 {
constexpr int SLOT = 16384, NTILE = S_ / 128;

struct Ctx { int h, qb, rb3h; unsigned tag; };

#define VMB(N) asm volatile("s_waitcnt vmcnt(" #N ")\n\ts_barrier" ::: "memory")
DI void helper(const Params& a, const unsigned lds0, const Ctx& c, const int w) {
    const int lane = lane_id();
    const int hw = w - 4, h = c.h;
    if (hw < 2) {
        const char* gK = (const char*)(a.kv + (size_t)h * (S_ * 32)) + hw * 4096;
        const char* gV = (const char*)(a.kv + (size_t)(32 + h) * (S_ * 32)) + hw * 4096;
        const unsigned voffK = (unsigned)((lane >> 2) * 64 + (((lane & 3) ^ ((lane >> 4) & 3)) * 16));
        unsigned voffV[4];
#pragma unroll
        for (int i = 0; i < 4; ++i) voffV[i] = (unsigned)((lane >> 4) * 256 + (((lane & 15) ^ (4 * i + (lane >> 4))) * 16));
        const unsigned dK = lds0 + hw * 4096, dV = lds0 + 8192 + hw * 4096;
#define P2_KOFF(T) ((size_t)((((c.qb + ((T) >> 1)) & 7) * 2 + ((T) & 1)) * 8192))
#define P2_DMA(T) do { _Pragma("unroll") for (int i = 0; i < 4; ++i) glds16c(voffK, gK + P2_KOFF(T) + i * 1024, dK + ((T) & 3) * SLOT + i * 1024); \
                       _Pragma("unroll") for (int i = 0; i < 4; ++i) glds16c(voffV[i], gV + P2_KOFF(T) + i * 1024, dV + ((T) & 3) * SLOT + i * 1024); } while (0)
        wait_flags(a.flags + FLAG_Q + h * 8 + (lane & 7), c.tag, (lane & 7) == c.qb);
        P2_DMA(2); P2_DMA(3);
#pragma unroll 1
        for (int k = 1; k <= 12; ++k) { VMB(8); P2_DMA(k + 3); }
        VMB(8);
        VMB(0);
#undef P2_DMA
    } else {
        const int sw = hw - 2;
        const char* hb = (const char*)(a.ho + (size_t)(c.qb * 256 + sw * 128) * HO_LD + h * 112);
        unsigned hoff[7];
#pragma unroll
        for (int i = 0; i < 7; ++i) { const int ch = i * 64 + lane, row = (ch * 4682) >> 16, col = ch - row * 14; hoff[i] = (unsigned)(row * (HO_LD * 2) + col * 16); asm volatile("" : "+v"(hoff[i])); }
        const unsigned hsrc = lds0 + p1::LDS_H + sw * (128 * 224) + lane * 16;
        const unsigned pfoff = (unsigned)((lane >> 3) * 2048 + (lane & 7) * 128);
        const unsigned gv = *(const LAS unsigned*)(size_t)(lds0 + p1::LDS_G + (sw * 4 + (lane & 3)) * 4);
#define P2_HST(i) do { const u32x4 v = *(const LAS u32x4*)(LP(hsrc) + (i) * 1024); \
            asm volatile("global_store_dwordx4 %0, %1, %2 sc1\n\ts_nop 1" :: "v"(hoff[(i) % 7]), "v"(v), "s"(hb + (size_t)((i) / 7) * 32 * (HO_LD * 2)) : "memory"); } while (0)
#pragma unroll
        for (int k = 0; k < 9; ++k) { P2_HST(3 * k); P2_HST(3 * k + 1); P2_HST(3 * k + 2); asm volatile("s_barrier" ::: "memory"); }
        P2_HST(27);
#undef P2_HST
        if (lane < 4) st4w(a.g + c.qb * 256 + h * 8 + sw * 4 + lane, gv);
        unsigned pfd = 0;
        {   const int xg = blockIdx.x & 7, j = blockIdx.x >> 3, kh = xg & 1, cg = (xg >> 1) & 1;
#pragma unroll
            for (int i = 0; i < 5; ++i) {
                int r8 = ((j * 72 + sw * 36) & ~7) + i * 8; r8 = r8 > 2296 ? 2296 : r8;
                const int k = kh * 2304 + r8;
                const char* base = (k < F_ ? (const char*)a.wd + (size_t)k * 2048 : (const char*)a.wo + (size_t)(k - F_) * 2048) + cg * 1024;
                asm volatile("global_load_dword %0, %1, %2" : "+v"(pfd) : "v"(pfoff), "s"(base) : "memory");
            } }
        asm volatile("s_barrier" ::: "memory");
        VMB(5);
        if (sw == 0 && lane == 0) flag_set(a.flags + FLAG_H + c.qb * 32 + h, c.tag);
        asm volatile("s_barrier" ::: "memory");
        asm volatile("s_waitcnt vmcnt(0)\n\ts_barrier" : "+v"(pfd) :: "memory");
        wait_flags(a.flags + FLAG_H + (lane < 32 ? c.rb3h * 32 + lane : c.qb * 32 + (lane - 32)), c.tag);
        asm volatile("s_waitcnt vmcnt(0)\n\ts_barrier" ::: "memory");
    }
    p3::producer_prologue(a, lds0, hw, lane);
}

DI unsigned addr_k(const unsigned lds0, const int lane, const int s) { const int r = lane & 31, hh = lane >> 5, pr = (r & 19) | ((r & 4) << 1) | ((r & 8) >> 1);
    return lds0 + pr * 64 + (((2 * s + hh) ^ ((pr >> 2) & 3)) * 16); }
DI unsigned addr_v(const unsigned lds0, const int lane, const int u, const int s) { const int d = lane & 31, hh = lane >> 5;
    return lds0 + d * 256 + (((4 * u + 2 * s + hh) ^ (d & 15)) * 16); }

DI bool attn_fast(const Params& a, const unsigned lds0, const Ctx& c, const int w, f32x16& oa, f32x16& ob, float& la, float& lb, float& ga, float& gb, bf16x8 (&q)[2][2]) {
    const int lane = lane_id();
    const int r = lane & 31, hh = lane >> 5;
#pragma unroll
    for (int b = 0; b < 2; ++b)
#pragma unroll
        for (int s = 0; s < 2; ++s) q[b][s] = ld128(LP(lds0 + p1::LDS_Q + (w * 64 + b * 32 + r) * 64 + s * 32 + hh * 16));
    const unsigned rK0 = addr_k(lds0, lane, 0), rK1 = addr_k(lds0, lane, 1);
    const short one = (short)0x3f80; const bool sel = ((lane & 15) == ((lane >> 4) & 1));
    const short sv = sel ? one : (short)0; bf16x8 selA = {sv, sv, sv, sv, sv, sv, sv, sv};
    f32x4 lsa = {0.f, 0.f, 0.f, 0.f}, lsb = {0.f, 0.f, 0.f, 0.f};
    f32x16 z;
#pragma unroll
    for (int i = 0; i < 16; ++i) z[i] = 0.f;
    oa = z; ob = z;
    KSTAMP(4);
    bf16x8 k0 = ld128(LP(rK0)), k1 = ld128(LP(rK1));
    f32x16 nma, nmb, sa, sb;
    {
        f32x16 s = MFMA32(k0, q[0][0], z); s = MFMA32(k1, q[0][1], s);
        f32x16 t = MFMA32(k0, q[1][0], z); t = MFMA32(k1, q[1][1], t);
        float m = s[0], n = t[0];
#pragma unroll
        for (int i = 1; i < 16; ++i) { m = __builtin_fmaxf(m, s[i]); n = __builtin_fmaxf(n, t[i]); }
        auto rm = __builtin_amdgcn_permlane32_swap(__float_as_uint(m), __float_as_uint(m), false, false);
        auto rn = __builtin_amdgcn_permlane32_swap(__float_as_uint(n), __float_as_uint(n), false, false);
        m = __builtin_fmaxf(__uint_as_float(rm[0]), __uint_as_float(rm[1])); n = __builtin_fmaxf(__uint_as_float(rn[0]), __uint_as_float(rn[1]));
#pragma unroll
        for (int i = 0; i < 16; ++i) { nma[i] = -m; nmb[i] = -n; }
    }
    sa = MFMA32(k0, q[0][0], nma); sa = MFMA32(k1, q[0][1], sa);
    sb = z;
    u32x4 pz = {0u, 0u, 0u, 0u}, pb0 = pz, pb1 = pz;
    bf16x8 vz0 = {0, 0, 0, 0, 0, 0, 0, 0}, vz1 = vz0;
    unsigned rv[8];
#pragma unroll
    for (int i = 0; i < 8; ++i) rv[i] = addr_v(lds0, lane, i >> 1, i & 1);
    const float* gp = a.g + c.qb * 256 + w * 64 + r;
    asm volatile("s_nop 15\n"
    "s_setprio 2\n"
    "s_mov_b32 s40, 0\n"
    "9:\n"
    "v_cvt_pk_bf16_f32 v151, v94, v95\n"
    "v_mfma_f32_32x32x16_bf16 v[80:95], v[96:99], v[128:131], v[48:63]\n"
    "v_exp_f32 v64, v64\n"
    "v_exp_f32 v65, v65\n"
    "v_exp_f32 v66, v66\n"
    "v_mfma_f32_32x32x16_bf16 v[80:95], v[100:103], v[132:135], v[80:95]\n"
    "ds_read_b128 v[96:99], v164 offset:2048\n"
    "ds_read_b128 v[100:103], v165 offset:2048\n"
    "v_exp_f32 v67, v67\n"
    "v_cvt_pk_bf16_f32 v136, v64, v65\n"
    "v_exp_f32 v68, v68\n"
    "v_exp_f32 v69, v69\n"
    "v_mfma_f32_32x32x16_bf16 v[16:31], v[112:115], v[144:147], v[16:31]\n"
    "ds_read_b128 v[104:107], v166 offset:8192\n"
    "ds_read_b128 v[108:111], v167 offset:8192\n"
    "v_cvt_pk_bf16_f32 v137, v66, v67\n"
    "v_exp_f32 v70, v70\n"
    "v_exp_f32 v71, v71\n"
    "v_cvt_pk_bf16_f32 v138, v68, v69\n"
    "v_mfma_f32_16x16x32_bf16 v[160:163], v[152:155], v[144:147], v[160:163]\n"
    "v_exp_f32 v72, v72\n"
    "v_exp_f32 v73, v73\n"
    "v_mfma_f32_32x32x16_bf16 v[16:31], v[116:119], v[148:151], v[16:31]\n"
    "v_cvt_pk_bf16_f32 v139, v70, v71\n"
    "v_exp_f32 v74, v74\n"
    "v_exp_f32 v75, v75\n"
    "v_cvt_pk_bf16_f32 v140, v72, v73\n"
    "v_mfma_f32_16x16x32_bf16 v[160:163], v[152:155], v[148:151], v[160:163]\n"
    "v_exp_f32 v76, v76\n"
    "v_exp_f32 v77, v77\n"
    "v_cvt_pk_bf16_f32 v141, v74, v75\n"
    "v_exp_f32 v78, v78\n"
    "v_exp_f32 v79, v79\n"
    "v_cvt_pk_bf16_f32 v142, v76, v77\n"
    "s_waitcnt lgkmcnt(0)\n"
    "v_cvt_pk_bf16_f32 v143, v78, v79\n"
    "v_mfma_f32_32x32x16_bf16 v[64:79], v[96:99], v[120:123], v[32:47]\n"
    "v_exp_f32 v80, v80\n"
    "v_exp_f32 v81, v81\n"
    "v_exp_f32 v82, v82\n"
    "v_mfma_f32_32x32x16_bf16 v[64:79], v[100:103], v[124:127], v[64:79]\n"
    "v_exp_f32 v83, v83\n"
    "v_cvt_pk_bf16_f32 v144, v80, v81\n"
    "v_exp_f32 v84, v84\n"
    "v_exp_f32 v85, v85\n"
    "v_mfma_f32_32x32x16_bf16 v[0:15], v[104:107], v[136:139], v[0:15]\n"
    "v_cvt_pk_bf16_f32 v145, v82, v83\n"
    "v_exp_f32 v86, v86\n"
    "v_exp_f32 v87, v87\n"
    "v_cvt_pk_bf16_f32 v146, v84, v85\n"
    "v_mfma_f32_16x16x32_bf16 v[156:159], v[152:155], v[136:139], v[156:159]\n"
    "v_exp_f32 v88, v88\n"
    "v_exp_f32 v89, v89\n"
    "v_mfma_f32_32x32x16_bf16 v[0:15], v[108:111], v[140:143], v[0:15]\n"
    "v_cvt_pk_bf16_f32 v147, v86, v87\n"
    "v_exp_f32 v90, v90\n"
    "v_exp_f32 v91, v91\n"
    "v_cvt_pk_bf16_f32 v148, v88, v89\n"
    "v_mfma_f32_16x16x32_bf16 v[156:159], v[152:155], v[140:143], v[156:159]\n"
    "v_exp_f32 v92, v92\n"
    "v_exp_f32 v93, v93\n"
    "v_cvt_pk_bf16_f32 v149, v90, v91\n"
    "v_exp_f32 v94, v94\n"
    "v_exp_f32 v95, v95\n"
    "v_cvt_pk_bf16_f32 v150, v92, v93\n"
    "v_cvt_pk_bf16_f32 v151, v94, v95\n"
    "v_mfma_f32_32x32x16_bf16 v[80:95], v[96:99], v[128:131], v[48:63]\n"
    "v_exp_f32 v64, v64\n"
    "v_exp_f32 v65, v65\n"
    "v_exp_f32 v66, v66\n"
    "v_mfma_f32_32x32x16_bf16 v[80:95], v[100:103], v[132:135], v[80:95]\n"
    "ds_read_b128 v[96:99], v164 offset:4096\n"
    "ds_read_b128 v[100:103], v165 offset:4096\n"
    "v_exp_f32 v67, v67\n"
    "v_cvt_pk_bf16_f32 v136, v64, v65\n"
    "v_exp_f32 v68, v68\n"
    "v_exp_f32 v69, v69\n"
    "v_mfma_f32_32x32x16_bf16 v[16:31], v[104:107], v[144:147], v[16:31]\n"
    "ds_read_b128 v[112:115], v168 offset:8192\n"
    "ds_read_b128 v[116:119], v169 offset:8192\n"
    "v_cvt_pk_bf16_f32 v137, v66, v67\n"
    "v_exp_f32 v70, v70\n"
    "v_exp_f32 v71, v71\n"
    "v_cvt_pk_bf16_f32 v138, v68, v69\n"
    "v_mfma_f32_16x16x32_bf16 v[160:163], v[152:155], v[144:147], v[160:163]\n"
    "v_exp_f32 v72, v72\n"
    "v_exp_f32 v73, v73\n"
    "v_mfma_f32_32x32x16_bf16 v[16:31], v[108:111], v[148:151], v[16:31]\n"
    "v_cvt_pk_bf16_f32 v139, v70, v71\n"
    "v_exp_f32 v74, v74\n"
    "v_exp_f32 v75, v75\n"
    "v_cvt_pk_bf16_f32 v140, v72, v73\n"
    "v_mfma_f32_16x16x32_bf16 v[160:163], v[152:155], v[148:151], v[160:163]\n"
    "v_exp_f32 v76, v76\n"
    "v_exp_f32 v77, v77\n"
    "v_cvt_pk_bf16_f32 v141, v74, v75\n"
    "v_exp_f32 v78, v78\n"
    "v_exp_f32 v79, v79\n"
    "v_cvt_pk_bf16_f32 v142, v76, v77\n"
    "s_waitcnt lgkmcnt(0)\n"
    "v_cvt_pk_bf16_f32 v143, v78, v79\n"
    "v_mfma_f32_32x32x16_bf16 v[64:79], v[96:99], v[120:123], v[32:47]\n"
    "v_exp_f32 v80, v80\n"
    "v_exp_f32 v81, v81\n"
    "v_exp_f32 v82, v82\n"
    "v_mfma_f32_32x32x16_bf16 v[64:79], v[100:103], v[124:127], v[64:79]\n"
    "v_exp_f32 v83, v83\n"
    "v_cvt_pk_bf16_f32 v144, v80, v81\n"
    "v_exp_f32 v84, v84\n"
    "v_exp_f32 v85, v85\n"
    "v_mfma_f32_32x32x16_bf16 v[0:15], v[112:115], v[136:139], v[0:15]\n"
    "v_cvt_pk_bf16_f32 v145, v82, v83\n"
    "v_exp_f32 v86, v86\n"
    "v_exp_f32 v87, v87\n"
    "v_cvt_pk_bf16_f32 v146, v84, v85\n"
    "v_mfma_f32_16x16x32_bf16 v[156:159], v[152:155], v[136:139], v[156:159]\n"
    "v_exp_f32 v88, v88\n"
    "v_exp_f32 v89, v89\n"
    "v_mfma_f32_32x32x16_bf16 v[0:15], v[116:119], v[140:143], v[0:15]\n"
    "v_cvt_pk_bf16_f32 v147, v86, v87\n"
    "v_exp_f32 v90, v90\n"
    "v_exp_f32 v91, v91\n"
    "v_cvt_pk_bf16_f32 v148, v88, v89\n"
    "v_mfma_f32_16x16x32_bf16 v[156:159], v[152:155], v[140:143], v[156:159]\n"
    "v_exp_f32 v92, v92\n"
    "v_exp_f32 v93, v93\n"
    "v_cvt_pk_bf16_f32 v149, v90, v91\n"
    "v_exp_f32 v94, v94\n"
    "v_exp_f32 v95, v95\n"
    "v_cvt_pk_bf16_f32 v150, v92, v93\n"
    "v_cvt_pk_bf16_f32 v151, v94, v95\n"
    "v_mfma_f32_32x32x16_bf16 v[80:95], v[96:99], v[128:131], v[48:63]\n"
    "v_exp_f32 v64, v64\n"
    "v_exp_f32 v65, v65\n"
    "v_exp_f32 v66, v66\n"
    "v_mfma_f32_32x32x16_bf16 v[80:95], v[100:103], v[132:135], v[80:95]\n"
    "ds_read_b128 v[96:99], v164 offset:6144\n"
    "ds_read_b128 v[100:103], v165 offset:6144\n"
    "v_exp_f32 v67, v67\n"
    "v_cvt_pk_bf16_f32 v136, v64, v65\n"
    "v_exp_f32 v68, v68\n"
    "v_exp_f32 v69, v69\n"
    "v_mfma_f32_32x32x16_bf16 v[16:31], v[112:115], v[144:147], v[16:31]\n"
    "ds_read_b128 v[104:107], v170 offset:8192\n"
    "ds_read_b128 v[108:111], v171 offset:8192\n"
    "v_cvt_pk_bf16_f32 v137, v66, v67\n"
    "v_exp_f32 v70, v70\n"
    "v_exp_f32 v71, v71\n"
    "v_cvt_pk_bf16_f32 v138, v68, v69\n"
    "v_mfma_f32_16x16x32_bf16 v[160:163], v[152:155], v[144:147], v[160:163]\n"
    "v_exp_f32 v72, v72\n"
    "v_exp_f32 v73, v73\n"
    "v_mfma_f32_32x32x16_bf16 v[16:31], v[116:119], v[148:151], v[16:31]\n"
    "v_cvt_pk_bf16_f32 v139, v70, v71\n"
    "v_exp_f32 v74, v74\n"
    "v_exp_f32 v75, v75\n"
    "v_cvt_pk_bf16_f32 v140, v72, v73\n"
    "v_mfma_f32_16x16x32_bf16 v[160:163], v[152:155], v[148:151], v[160:163]\n"
    "v_exp_f32 v76, v76\n"
    "v_exp_f32 v77, v77\n"
    "v_cvt_pk_bf16_f32 v141, v74, v75\n"
    "v_exp_f32 v78, v78\n"
    "v_exp_f32 v79, v79\n"
    "v_cvt_pk_bf16_f32 v142, v76, v77\n"
    "s_waitcnt lgkmcnt(0)\n"
    "v_cvt_pk_bf16_f32 v143, v78, v79\n"
    "v_mfma_f32_32x32x16_bf16 v[64:79], v[96:99], v[120:123], v[32:47]\n"
    "v_exp_f32 v80, v80\n"
    "v_exp_f32 v81, v81\n"
    "v_exp_f32 v82, v82\n"
    "v_mfma_f32_32x32x16_bf16 v[64:79], v[100:103], v[124:127], v[64:79]\n"
    "v_exp_f32 v83, v83\n"
    "v_cvt_pk_bf16_f32 v144, v80, v81\n"
    "v_exp_f32 v84, v84\n"
    "v_exp_f32 v85, v85\n"
    "v_mfma_f32_32x32x16_bf16 v[0:15], v[104:107], v[136:139], v[0:15]\n"
    "v_cvt_pk_bf16_f32 v145, v82, v83\n"
    "v_exp_f32 v86, v86\n"
    "v_exp_f32 v87, v87\n"
    "v_cvt_pk_bf16_f32 v146, v84, v85\n"
    "v_mfma_f32_16x16x32_bf16 v[156:159], v[152:155], v[136:139], v[156:159]\n"
    "v_exp_f32 v88, v88\n"
    "v_exp_f32 v89, v89\n"
    "v_mfma_f32_32x32x16_bf16 v[0:15], v[108:111], v[140:143], v[0:15]\n"
    "v_cvt_pk_bf16_f32 v147, v86, v87\n"
    "v_exp_f32 v90, v90\n"
    "v_exp_f32 v91, v91\n"
    "v_cvt_pk_bf16_f32 v148, v88, v89\n"
    "v_mfma_f32_16x16x32_bf16 v[156:159], v[152:155], v[140:143], v[156:159]\n"
    "v_exp_f32 v92, v92\n"
    "v_exp_f32 v93, v93\n"
    "v_cvt_pk_bf16_f32 v149, v90, v91\n"
    "v_exp_f32 v94, v94\n"
    "v_exp_f32 v95, v95\n"
    "v_cvt_pk_bf16_f32 v150, v92, v93\n"
    "s_cmp_eq_u32 s40, 0\n"
    "s_cbranch_scc1 11f\n"
    "s_barrier\n"
    "11:\n"
    "v_cvt_pk_bf16_f32 v151, v94, v95\n"
    "v_mfma_f32_32x32x16_bf16 v[80:95], v[96:99], v[128:131], v[48:63]\n"
    "v_exp_f32 v64, v64\n"
    "v_exp_f32 v65, v65\n"
    "v_exp_f32 v66, v66\n"
    "v_mfma_f32_32x32x16_bf16 v[80:95], v[100:103], v[132:135], v[80:95]\n"
    "ds_read_b128 v[96:99], v164 offset:16384\n"
    "ds_read_b128 v[100:103], v165 offset:16384\n"
    "v_exp_f32 v67, v67\n"
    "v_cvt_pk_bf16_f32 v136, v64, v65\n"
    "v_exp_f32 v68, v68\n"
    "v_exp_f32 v69, v69\n"
    "v_mfma_f32_32x32x16_bf16 v[16:31], v[104:107], v[144:147], v[16:31]\n"
    "ds_read_b128 v[112:115], v172 offset:8192\n"
    "ds_read_b128 v[116:119], v173 offset:8192\n"
    "v_cvt_pk_bf16_f32 v137, v66, v67\n"
    "v_exp_f32 v70, v70\n"
    "v_exp_f32 v71, v71\n"
    "v_cvt_pk_bf16_f32 v138, v68, v69\n"
    "v_mfma_f32_16x16x32_bf16 v[160:163], v[152:155], v[144:147], v[160:163]\n"
    "v_exp_f32 v72, v72\n"
    "v_exp_f32 v73, v73\n"
    "v_mfma_f32_32x32x16_bf16 v[16:31], v[108:111], v[148:151], v[16:31]\n"
    "v_cvt_pk_bf16_f32 v139, v70, v71\n"
    "v_exp_f32 v74, v74\n"
    "v_exp_f32 v75, v75\n"
    "v_cvt_pk_bf16_f32 v140, v72, v73\n"
    "v_mfma_f32_16x16x32_bf16 v[160:163], v[152:155], v[148:151], v[160:163]\n"
    "v_exp_f32 v76, v76\n"
    "v_exp_f32 v77, v77\n"
    "v_cvt_pk_bf16_f32 v141, v74, v75\n"
    "v_exp_f32 v78, v78\n"
    "v_exp_f32 v79, v79\n"
    "v_cvt_pk_bf16_f32 v142, v76, v77\n"
    "s_waitcnt lgkmcnt(0)\n"
    "v_cvt_pk_bf16_f32 v143, v78, v79\n"
    "v_mfma_f32_32x32x16_bf16 v[64:79], v[96:99], v[120:123], v[32:47]\n"
    "v_exp_f32 v80, v80\n"
    "v_exp_f32 v81, v81\n"
    "v_exp_f32 v82, v82\n"
    "v_mfma_f32_32x32x16_bf16 v[64:79], v[100:103], v[124:127], v[64:79]\n"
    "v_exp_f32 v83, v83\n"
    "v_cvt_pk_bf16_f32 v144, v80, v81\n"
    "v_exp_f32 v84, v84\n"
    "v_exp_f32 v85, v85\n"
    "v_mfma_f32_32x32x16_bf16 v[0:15], v[112:115], v[136:139], v[0:15]\n"
    "v_cvt_pk_bf16_f32 v145, v82, v83\n"
    "v_exp_f32 v86, v86\n"
    "v_exp_f32 v87, v87\n"
    "v_cvt_pk_bf16_f32 v146, v84, v85\n"
    "v_mfma_f32_16x16x32_bf16 v[156:159], v[152:155], v[136:139], v[156:159]\n"
    "v_exp_f32 v88, v88\n"
    "v_exp_f32 v89, v89\n"
    "v_mfma_f32_32x32x16_bf16 v[0:15], v[116:119], v[140:143], v[0:15]\n"
    "v_cvt_pk_bf16_f32 v147, v86, v87\n"
    "v_exp_f32 v90, v90\n"
    "v_exp_f32 v91, v91\n"
    "v_cvt_pk_bf16_f32 v148, v88, v89\n"
    "v_mfma_f32_16x16x32_bf16 v[156:159], v[152:155], v[140:143], v[156:159]\n"
    "v_exp_f32 v92, v92\n"
    "v_exp_f32 v93, v93\n"
    "v_cvt_pk_bf16_f32 v149, v90, v91\n"
    "v_exp_f32 v94, v94\n"
    "v_exp_f32 v95, v95\n"
    "v_cvt_pk_bf16_f32 v150, v92, v93\n"
    "v_cvt_pk_bf16_f32 v151, v94, v95\n"
    "v_mfma_f32_32x32x16_bf16 v[80:95], v[96:99], v[128:131], v[48:63]\n"
    "v_exp_f32 v64, v64\n"
    "v_exp_f32 v65, v65\n"
    "v_exp_f32 v66, v66\n"
    "v_mfma_f32_32x32x16_bf16 v[80:95], v[100:103], v[132:135], v[80:95]\n"
    "ds_read_b128 v[96:99], v164 offset:18432\n"
    "ds_read_b128 v[100:103], v165 offset:18432\n"
    "v_exp_f32 v67, v67\n"
    "v_cvt_pk_bf16_f32 v136, v64, v65\n"
    "v_exp_f32 v68, v68\n"
    "v_exp_f32 v69, v69\n"
    "v_mfma_f32_32x32x16_bf16 v[16:31], v[112:115], v[144:147], v[16:31]\n"
    "ds_read_b128 v[104:107], v166 offset:24576\n"
    "ds_read_b128 v[108:111], v167 offset:24576\n"
    "v_cvt_pk_bf16_f32 v137, v66, v67\n"
    "v_exp_f32 v70, v70\n"
    "v_exp_f32 v71, v71\n"
    "v_cvt_pk_bf16_f32 v138, v68, v69\n"
    "v_mfma_f32_16x16x32_bf16 v[160:163], v[152:155], v[144:147], v[160:163]\n"
    "v_exp_f32 v72, v72\n"
    "v_exp_f32 v73, v73\n"
    "v_mfma_f32_32x32x16_bf16 v[16:31], v[116:119], v[148:151], v[16:31]\n"
    "v_cvt_pk_bf16_f32 v139, v70, v71\n"
    "v_exp_f32 v74, v74\n"
    "v_exp_f32 v75, v75\n"
    "v_cvt_pk_bf16_f32 v140, v72, v73\n"
    "v_mfma_f32_16x16x32_bf16 v[160:163], v[152:155], v[148:151], v[160:163]\n"
    "v_exp_f32 v76, v76\n"
    "v_exp_f32 v77, v77\n"
    "v_cvt_pk_bf16_f32 v141, v74, v75\n"
    "v_exp_f32 v78, v78\n"
    "v_exp_f32 v79, v79\n"
    "v_cvt_pk_bf16_f32 v142, v76, v77\n"
    "s_waitcnt lgkmcnt(0)\n"
    "v_cvt_pk_bf16_f32 v143, v78, v79\n"
    "v_mfma_f32_32x32x16_bf16 v[64:79], v[96:99], v[120:123], v[32:47]\n"
    "v_exp_f32 v80, v80\n"
    "v_exp_f32 v81, v81\n"
    "v_exp_f32 v82, v82\n"
    "v_mfma_f32_32x32x16_bf16 v[64:79], v[100:103], v[124:127], v[64:79]\n"
    "v_exp_f32 v83, v83\n"
    "v_cvt_pk_bf16_f32 v144, v80, v81\n"
    "v_exp_f32 v84, v84\n"
    "v_exp_f32 v85, v85\n"
    "v_mfma_f32_32x32x16_bf16 v[0:15], v[104:107], v[136:139], v[0:15]\n"
    "v_cvt_pk_bf16_f32 v145, v82, v83\n"
    "v_exp_f32 v86, v86\n"
    "v_exp_f32 v87, v87\n"
    "v_cvt_pk_bf16_f32 v146, v84, v85\n"
    "v_mfma_f32_16x16x32_bf16 v[156:159], v[152:155], v[136:139], v[156:159]\n"
    "v_exp_f32 v88, v88\n"
    "v_exp_f32 v89, v89\n"
    "v_mfma_f32_32x32x16_bf16 v[0:15], v[108:111], v[140:143], v[0:15]\n"
    "v_cvt_pk_bf16_f32 v147, v86, v87\n"
    "v_exp_f32 v90, v90\n"
    "v_exp_f32 v91, v91\n"
    "v_cvt_pk_bf16_f32 v148, v88, v89\n"
    "v_mfma_f32_16x16x32_bf16 v[156:159], v[152:155], v[140:143], v[156:159]\n"
    "v_exp_f32 v92, v92\n"
    "v_exp_f32 v93, v93\n"
    "v_cvt_pk_bf16_f32 v149, v90, v91\n"
    "v_exp_f32 v94, v94\n"
    "v_exp_f32 v95, v95\n"
    "v_cvt_pk_bf16_f32 v150, v92, v93\n"
    "v_cvt_pk_bf16_f32 v151, v94, v95\n"
    "v_mfma_f32_32x32x16_bf16 v[80:95], v[96:99], v[128:131], v[48:63]\n"
    "v_exp_f32 v64, v64\n"
    "v_exp_f32 v65, v65\n"
    "v_exp_f32 v66, v66\n"
    "v_mfma_f32_32x32x16_bf16 v[80:95], v[100:103], v[132:135], v[80:95]\n"
    "ds_read_b128 v[96:99], v164 offset:20480\n"
    "ds_read_b128 v[100:103], v165 offset:20480\n"
    "v_exp_f32 v67, v67\n"
    "v_cvt_pk_bf16_f32 v136, v64, v65\n"
    "v_exp_f32 v68, v68\n"
    "v_exp_f32 v69, v69\n"
    "v_mfma_f32_32x32x16_bf16 v[16:31], v[104:107], v[144:147], v[16:31]\n"
    "ds_read_b128 v[112:115], v168 offset:24576\n"
    "ds_read_b128 v[116:119], v169 offset:24576\n"
    "v_cvt_pk_bf16_f32 v137, v66, v67\n"
    "v_exp_f32 v70, v70\n"
    "v_exp_f32 v71, v71\n"
    "v_cvt_pk_bf16_f32 v138, v68, v69\n"
    "v_mfma_f32_16x16x32_bf16 v[160:163], v[152:155], v[144:147], v[160:163]\n"
    "v_exp_f32 v72, v72\n"
    "v_exp_f32 v73, v73\n"
    "v_mfma_f32_32x32x16_bf16 v[16:31], v[108:111], v[148:151], v[16:31]\n"
    "v_cvt_pk_bf16_f32 v139, v70, v71\n"
    "v_exp_f32 v74, v74\n"
    "v_exp_f32 v75, v75\n"
    "v_cvt_pk_bf16_f32 v140, v72, v73\n"
    "v_mfma_f32_16x16x32_bf16 v[160:163], v[152:155], v[148:151], v[160:163]\n"
    "v_exp_f32 v76, v76\n"
    "v_exp_f32 v77, v77\n"
    "v_cvt_pk_bf16_f32 v141, v74, v75\n"
    "v_exp_f32 v78, v78\n"
    "v_exp_f32 v79, v79\n"
    "v_cvt_pk_bf16_f32 v142, v76, v77\n"
    "s_waitcnt lgkmcnt(0)\n"
    "v_cvt_pk_bf16_f32 v143, v78, v79\n"
    "v_mfma_f32_32x32x16_bf16 v[64:79], v[96:99], v[120:123], v[32:47]\n"
    "v_exp_f32 v80, v80\n"
    "v_exp_f32 v81, v81\n"
    "v_exp_f32 v82, v82\n"
    "v_mfma_f32_32x32x16_bf16 v[64:79], v[100:103], v[124:127], v[64:79]\n"
    "v_exp_f32 v83, v83\n"
    "v_cvt_pk_bf16_f32 v144, v80, v81\n"
    "v_exp_f32 v84, v84\n"
    "v_exp_f32 v85, v85\n"
    "v_mfma_f32_32x32x16_bf16 v[0:15], v[112:115], v[136:139], v[0:15]\n"
    "v_cvt_pk_bf16_f32 v145, v82, v83\n"
    "v_exp_f32 v86, v86\n"
    "v_exp_f32 v87, v87\n"
    "v_cvt_pk_bf16_f32 v146, v84, v85\n"
    "v_mfma_f32_16x16x32_bf16 v[156:159], v[152:155], v[136:139], v[156:159]\n"
    "v_exp_f32 v88, v88\n"
    "v_exp_f32 v89, v89\n"
    "v_mfma_f32_32x32x16_bf16 v[0:15], v[116:119], v[140:143], v[0:15]\n"
    "v_cvt_pk_bf16_f32 v147, v86, v87\n"
    "v_exp_f32 v90, v90\n"
    "v_exp_f32 v91, v91\n"
    "v_cvt_pk_bf16_f32 v148, v88, v89\n"
    "v_mfma_f32_16x16x32_bf16 v[156:159], v[152:155], v[140:143], v[156:159]\n"
    "v_exp_f32 v92, v92\n"
    "v_exp_f32 v93, v93\n"
    "v_cvt_pk_bf16_f32 v149, v90, v91\n"
    "v_exp_f32 v94, v94\n"
    "v_exp_f32 v95, v95\n"
    "v_cvt_pk_bf16_f32 v150, v92, v93\n"
    "v_cvt_pk_bf16_f32 v151, v94, v95\n"
    "v_mfma_f32_32x32x16_bf16 v[80:95], v[96:99], v[128:131], v[48:63]\n"
    "v_exp_f32 v64, v64\n"
    "v_exp_f32 v65, v65\n"
    "v_exp_f32 v66, v66\n"
    "v_mfma_f32_32x32x16_bf16 v[80:95], v[100:103], v[132:135], v[80:95]\n"
    "ds_read_b128 v[96:99], v164 offset:22528\n"
    "ds_read_b128 v[100:103], v165 offset:22528\n"
    "v_exp_f32 v67, v67\n"
    "v_cvt_pk_bf16_f32 v136, v64, v65\n"
    "v_exp_f32 v68, v68\n"
    "v_exp_f32 v69, v69\n"
    "v_mfma_f32_32x32x16_bf16 v[16:31], v[112:115], v[144:147], v[16:31]\n"
    "ds_read_b128 v[104:107], v170 offset:24576\n"
    "ds_read_b128 v[108:111], v171 offset:24576\n"
    "v_cvt_pk_bf16_f32 v137, v66, v67\n"
    "v_exp_f32 v70, v70\n"
    "v_exp_f32 v71, v71\n"
    "v_cvt_pk_bf16_f32 v138, v68, v69\n"
    "v_mfma_f32_16x16x32_bf16 v[160:163], v[152:155], v[144:147], v[160:163]\n"
    "v_exp_f32 v72, v72\n"
    "v_exp_f32 v73, v73\n"
    "v_mfma_f32_32x32x16_bf16 v[16:31], v[116:119], v[148:151], v[16:31]\n"
    "v_cvt_pk_bf16_f32 v139, v70, v71\n"
    "v_exp_f32 v74, v74\n"
    "v_exp_f32 v75, v75\n"
    "v_cvt_pk_bf16_f32 v140, v72, v73\n"
    "v_mfma_f32_16x16x32_bf16 v[160:163], v[152:155], v[148:151], v[160:163]\n"
    "v_exp_f32 v76, v76\n"
    "v_exp_f32 v77, v77\n"
    "v_cvt_pk_bf16_f32 v141, v74, v75\n"
    "v_exp_f32 v78, v78\n"
    "v_exp_f32 v79, v79\n"
    "v_cvt_pk_bf16_f32 v142, v76, v77\n"
    "s_waitcnt lgkmcnt(0)\n"
    "v_cvt_pk_bf16_f32 v143, v78, v79\n"
    "v_mfma_f32_32x32x16_bf16 v[64:79], v[96:99], v[120:123], v[32:47]\n"
    "v_exp_f32 v80, v80\n"
    "v_exp_f32 v81, v81\n"
    "v_exp_f32 v82, v82\n"
    "v_mfma_f32_32x32x16_bf16 v[64:79], v[100:103], v[124:127], v[64:79]\n"
    "v_exp_f32 v83, v83\n"
    "v_cvt_pk_bf16_f32 v144, v80, v81\n"
    "v_exp_f32 v84, v84\n"
    "v_exp_f32 v85, v85\n"
    "v_mfma_f32_32x32x16_bf16 v[0:15], v[104:107], v[136:139], v[0:15]\n"
    "v_cvt_pk_bf16_f32 v145, v82, v83\n"
    "v_exp_f32 v86, v86\n"
    "v_exp_f32 v87, v87\n"
    "v_cvt_pk_bf16_f32 v146, v84, v85\n"
    "v_mfma_f32_16x16x32_bf16 v[156:159], v[152:155], v[136:139], v[156:159]\n"
    "v_exp_f32 v88, v88\n"
    "v_exp_f32 v89, v89\n"
    "v_mfma_f32_32x32x16_bf16 v[0:15], v[108:111], v[140:143], v[0:15]\n"
    "v_cvt_pk_bf16_f32 v147, v86, v87\n"
    "v_exp_f32 v90, v90\n"
    "v_exp_f32 v91, v91\n"
    "v_cvt_pk_bf16_f32 v148, v88, v89\n"
    "v_mfma_f32_16x16x32_bf16 v[156:159], v[152:155], v[140:143], v[156:159]\n"
    "v_exp_f32 v92, v92\n"
    "v_exp_f32 v93, v93\n"
    "v_cvt_pk_bf16_f32 v149, v90, v91\n"
    "v_exp_f32 v94, v94\n"
    "v_exp_f32 v95, v95\n"
    "v_cvt_pk_bf16_f32 v150, v92, v93\n"
    "s_barrier\n"
    "v_cvt_pk_bf16_f32 v151, v94, v95\n"
    "v_mfma_f32_32x32x16_bf16 v[80:95], v[96:99], v[128:131], v[48:63]\n"
    "v_exp_f32 v64, v64\n"
    "v_exp_f32 v65, v65\n"
    "v_exp_f32 v66, v66\n"
    "v_mfma_f32_32x32x16_bf16 v[80:95], v[100:103], v[132:135], v[80:95]\n"
    "ds_read_b128 v[96:99], v164 offset:32768\n"
    "ds_read_b128 v[100:103], v165 offset:32768\n"
    "v_exp_f32 v67, v67\n"
    "v_cvt_pk_bf16_f32 v136, v64, v65\n"
    "v_exp_f32 v68, v68\n"
    "v_exp_f32 v69, v69\n"
    "v_mfma_f32_32x32x16_bf16 v[16:31], v[104:107], v[144:147], v[16:31]\n"
    "ds_read_b128 v[112:115], v172 offset:24576\n"
    "ds_read_b128 v[116:119], v173 offset:24576\n"
    "v_cvt_pk_bf16_f32 v137, v66, v67\n"
    "v_exp_f32 v70, v70\n"
    "v_exp_f32 v71, v71\n"
    "v_cvt_pk_bf16_f32 v138, v68, v69\n"
    "v_mfma_f32_16x16x32_bf16 v[160:163], v[152:155], v[144:147], v[160:163]\n"
    "v_exp_f32 v72, v72\n"
    "v_exp_f32 v73, v73\n"
    "v_mfma_f32_32x32x16_bf16 v[16:31], v[108:111], v[148:151], v[16:31]\n"
    "v_cvt_pk_bf16_f32 v139, v70, v71\n"
    "v_exp_f32 v74, v74\n"
    "v_exp_f32 v75, v75\n"
    "v_cvt_pk_bf16_f32 v140, v72, v73\n"
    "v_mfma_f32_16x16x32_bf16 v[160:163], v[152:155], v[148:151], v[160:163]\n"
    "v_exp_f32 v76, v76\n"
    "v_exp_f32 v77, v77\n"
    "v_cvt_pk_bf16_f32 v141, v74, v75\n"
    "v_exp_f32 v78, v78\n"
    "v_exp_f32 v79, v79\n"
    "v_cvt_pk_bf16_f32 v142, v76, v77\n"
    "s_waitcnt lgkmcnt(0)\n"
    "v_cvt_pk_bf16_f32 v143, v78, v79\n"
    "v_mfma_f32_32x32x16_bf16 v[64:79], v[96:99], v[120:123], v[32:47]\n"
    "v_exp_f32 v80, v80\n"
    "v_exp_f32 v81, v81\n"
    "v_exp_f32 v82, v82\n"
    "v_mfma_f32_32x32x16_bf16 v[64:79], v[100:103], v[124:127], v[64:79]\n"
    "v_exp_f32 v83, v83\n"
    "v_cvt_pk_bf16_f32 v144, v80, v81\n"
    "v_exp_f32 v84, v84\n"
    "v_exp_f32 v85, v85\n"
    "v_mfma_f32_32x32x16_bf16 v[0:15], v[112:115], v[136:139], v[0:15]\n"
    "v_cvt_pk_bf16_f32 v145, v82, v83\n"
    "v_exp_f32 v86, v86\n"
    "v_exp_f32 v87, v87\n"
    "v_cvt_pk_bf16_f32 v146, v84, v85\n"
    "v_mfma_f32_16x16x32_bf16 v[156:159], v[152:155], v[136:139], v[156:159]\n"
    "v_exp_f32 v88, v88\n"
    "v_exp_f32 v89, v89\n"
    "v_mfma_f32_32x32x16_bf16 v[0:15], v[116:119], v[140:143], v[0:15]\n"
    "v_cvt_pk_bf16_f32 v147, v86, v87\n"
    "v_exp_f32 v90, v90\n"
    "v_exp_f32 v91, v91\n"
    "v_cvt_pk_bf16_f32 v148, v88, v89\n"
    "v_mfma_f32_16x16x32_bf16 v[156:159], v[152:155], v[140:143], v[156:159]\n"
    "v_exp_f32 v92, v92\n"
    "v_exp_f32 v93, v93\n"
    "v_cvt_pk_bf16_f32 v149, v90, v91\n"
    "v_exp_f32 v94, v94\n"
    "v_exp_f32 v95, v95\n"
    "v_cvt_pk_bf16_f32 v150, v92, v93\n"
    "v_cvt_pk_bf16_f32 v151, v94, v95\n"
    "v_mfma_f32_32x32x16_bf16 v[80:95], v[96:99], v[128:131], v[48:63]\n"
    "v_exp_f32 v64, v64\n"
    "v_exp_f32 v65, v65\n"
    "v_exp_f32 v66, v66\n"
    "v_mfma_f32_32x32x16_bf16 v[80:95], v[100:103], v[132:135], v[80:95]\n"
    "ds_read_b128 v[96:99], v164 offset:34816\n"
    "ds_read_b128 v[100:103], v165 offset:34816\n"
    "v_exp_f32 v67, v67\n"
    "v_cvt_pk_bf16_f32 v136, v64, v65\n"
    "v_exp_f32 v68, v68\n"
    "v_exp_f32 v69, v69\n"
    "v_mfma_f32_32x32x16_bf16 v[16:31], v[112:115], v[144:147], v[16:31]\n"
    "ds_read_b128 v[104:107], v166 offset:40960\n"
    "ds_read_b128 v[108:111], v167 offset:40960\n"
    "v_cvt_pk_bf16_f32 v137, v66, v67\n"
    "v_exp_f32 v70, v70\n"
    "v_exp_f32 v71, v71\n"
    "v_cvt_pk_bf16_f32 v138, v68, v69\n"
    "v_mfma_f32_16x16x32_bf16 v[160:163], v[152:155], v[144:147], v[160:163]\n"
    "v_exp_f32 v72, v72\n"
    "v_exp_f32 v73, v73\n"
    "v_mfma_f32_32x32x16_bf16 v[16:31], v[116:119], v[148:151], v[16:31]\n"
    "v_cvt_pk_bf16_f32 v139, v70, v71\n"
    "v_exp_f32 v74, v74\n"
    "v_exp_f32 v75, v75\n"
    "v_cvt_pk_bf16_f32 v140, v72, v73\n"
    "v_mfma_f32_16x16x32_bf16 v[160:163], v[152:155], v[148:151], v[160:163]\n"
    "v_exp_f32 v76, v76\n"
    "v_exp_f32 v77, v77\n"
    "v_cvt_pk_bf16_f32 v141, v74, v75\n"
    "v_exp_f32 v78, v78\n"
    "v_exp_f32 v79, v79\n"
    "v_cvt_pk_bf16_f32 v142, v76, v77\n"
    "s_waitcnt lgkmcnt(0)\n"
    "v_cvt_pk_bf16_f32 v143, v78, v79\n"
    "v_mfma_f32_32x32x16_bf16 v[64:79], v[96:99], v[120:123], v[32:47]\n"
    "v_exp_f32 v80, v80\n"
    "v_exp_f32 v81, v81\n"
    "v_exp_f32 v82, v82\n"
    "v_mfma_f32_32x32x16_bf16 v[64:79], v[100:103], v[124:127], v[64:79]\n"
    "v_exp_f32 v83, v83\n"
    "v_cvt_pk_bf16_f32 v144, v80, v81\n"
    "v_exp_f32 v84, v84\n"
    "v_exp_f32 v85, v85\n"
    "v_mfma_f32_32x32x16_bf16 v[0:15], v[104:107], v[136:139], v[0:15]\n"
    "v_cvt_pk_bf16_f32 v145, v82, v83\n"
    "v_exp_f32 v86, v86\n"
    "v_exp_f32 v87, v87\n"
    "v_cvt_pk_bf16_f32 v146, v84, v85\n"
    "v_mfma_f32_16x16x32_bf16 v[156:159], v[152:155], v[136:139], v[156:159]\n"
    "v_exp_f32 v88, v88\n"
    "v_exp_f32 v89, v89\n"
    "v_mfma_f32_32x32x16_bf16 v[0:15], v[108:111], v[140:143], v[0:15]\n"
    "v_cvt_pk_bf16_f32 v147, v86, v87\n"
    "v_exp_f32 v90, v90\n"
    "v_exp_f32 v91, v91\n"
    "v_cvt_pk_bf16_f32 v148, v88, v89\n"
    "v_mfma_f32_16x16x32_bf16 v[156:159], v[152:155], v[140:143], v[156:159]\n"
    "v_exp_f32 v92, v92\n"
    "v_exp_f32 v93, v93\n"
    "v_cvt_pk_bf16_f32 v149, v90, v91\n"
    "v_exp_f32 v94, v94\n"
    "v_exp_f32 v95, v95\n"
    "v_cvt_pk_bf16_f32 v150, v92, v93\n"
    "v_cvt_pk_bf16_f32 v151, v94, v95\n"
    "v_mfma_f32_32x32x16_bf16 v[80:95], v[96:99], v[128:131], v[48:63]\n"
    "v_exp_f32 v64, v64\n"
    "v_exp_f32 v65, v65\n"
    "v_exp_f32 v66, v66\n"
    "v_mfma_f32_32x32x16_bf16 v[80:95], v[100:103], v[132:135], v[80:95]\n"
    "ds_read_b128 v[96:99], v164 offset:36864\n"
    "ds_read_b128 v[100:103], v165 offset:36864\n"
    "v_exp_f32 v67, v67\n"
    "v_cvt_pk_bf16_f32 v136, v64, v65\n"
    "v_exp_f32 v68, v68\n"
    "v_exp_f32 v69, v69\n"
    "v_mfma_f32_32x32x16_bf16 v[16:31], v[104:107], v[144:147], v[16:31]\n"
    "ds_read_b128 v[112:115], v168 offset:40960\n"
    "ds_read_b128 v[116:119], v169 offset:40960\n"
    "v_cvt_pk_bf16_f32 v137, v66, v67\n"
    "v_exp_f32 v70, v70\n"
    "v_exp_f32 v71, v71\n"
    "v_cvt_pk_bf16_f32 v138, v68, v69\n"
    "v_mfma_f32_16x16x32_bf16 v[160:163], v[152:155], v[144:147], v[160:163]\n"
    "v_exp_f32 v72, v72\n"
    "v_exp_f32 v73, v73\n"
    "v_mfma_f32_32x32x16_bf16 v[16:31], v[108:111], v[148:151], v[16:31]\n"
    "v_cvt_pk_bf16_f32 v139, v70, v71\n"
    "v_exp_f32 v74, v74\n"
    "v_exp_f32 v75, v75\n"
    "v_cvt_pk_bf16_f32 v140, v72, v73\n"
    "v_mfma_f32_16x16x32_bf16 v[160:163], v[152:155], v[148:151], v[160:163]\n"
    "v_exp_f32 v76, v76\n"
    "v_exp_f32 v77, v77\n"
    "v_cvt_pk_bf16_f32 v141, v74, v75\n"
    "v_exp_f32 v78, v78\n"
    "v_exp_f32 v79, v79\n"
    "v_cvt_pk_bf16_f32 v142, v76, v77\n"
    "s_waitcnt lgkmcnt(0)\n"
    "v_cvt_pk_bf16_f32 v143, v78, v79\n"
    "v_mfma_f32_32x32x16_bf16 v[64:79], v[96:99], v[120:123], v[32:47]\n"
    "v_exp_f32 v80, v80\n"
    "v_exp_f32 v81, v81\n"
    "v_exp_f32 v82, v82\n"
    "v_mfma_f32_32x32x16_bf16 v[64:79], v[100:103], v[124:127], v[64:79]\n"
    "v_exp_f32 v83, v83\n"
    "v_cvt_pk_bf16_f32 v144, v80, v81\n"
    "v_exp_f32 v84, v84\n"
    "v_exp_f32 v85, v85\n"
    "v_mfma_f32_32x32x16_bf16 v[0:15], v[112:115], v[136:139], v[0:15]\n"
    "v_cvt_pk_bf16_f32 v145, v82, v83\n"
    "v_exp_f32 v86, v86\n"
    "v_exp_f32 v87, v87\n"
    "v_cvt_pk_bf16_f32 v146, v84, v85\n"
    "v_mfma_f32_16x16x32_bf16 v[156:159], v[152:155], v[136:139], v[156:159]\n"
    "v_exp_f32 v88, v88\n"
    "v_exp_f32 v89, v89\n"
    "v_mfma_f32_32x32x16_bf16 v[0:15], v[116:119], v[140:143], v[0:15]\n"
    "v_cvt_pk_bf16_f32 v147, v86, v87\n"
    "v_exp_f32 v90, v90\n"
    "v_exp_f32 v91, v91\n"
    "v_cvt_pk_bf16_f32 v148, v88, v89\n"
    "v_mfma_f32_16x16x32_bf16 v[156:159], v[152:155], v[140:143], v[156:159]\n"
    "v_exp_f32 v92, v92\n"
    "v_exp_f32 v93, v93\n"
    "v_cvt_pk_bf16_f32 v149, v90, v91\n"
    "v_exp_f32 v94, v94\n"
    "v_exp_f32 v95, v95\n"
    "v_cvt_pk_bf16_f32 v150, v92, v93\n"
    "v_cvt_pk_bf16_f32 v151, v94, v95\n"
    "v_mfma_f32_32x32x16_bf16 v[80:95], v[96:99], v[128:131], v[48:63]\n"
    "v_exp_f32 v64, v64\n"
    "v_exp_f32 v65, v65\n"
    "v_exp_f32 v66, v66\n"
    "v_mfma_f32_32x32x16_bf16 v[80:95], v[100:103], v[132:135], v[80:95]\n"
    "ds_read_b128 v[96:99], v164 offset:38912\n"
    "ds_read_b128 v[100:103], v165 offset:38912\n"
    "v_exp_f32 v67, v67\n"
    "v_cvt_pk_bf16_f32 v136, v64, v65\n"
    "v_exp_f32 v68, v68\n"
    "v_exp_f32 v69, v69\n"
    "v_mfma_f32_32x32x16_bf16 v[16:31], v[112:115], v[144:147], v[16:31]\n"
    "ds_read_b128 v[104:107], v170 offset:40960\n"
    "ds_read_b128 v[108:111], v171 offset:40960\n"
    "v_cvt_pk_bf16_f32 v137, v66, v67\n"
    "v_exp_f32 v70, v70\n"
    "v_exp_f32 v71, v71\n"
    "v_cvt_pk_bf16_f32 v138, v68, v69\n"
    "v_mfma_f32_16x16x32_bf16 v[160:163], v[152:155], v[144:147], v[160:163]\n"
    "v_exp_f32 v72, v72\n"
    "v_exp_f32 v73, v73\n"
    "v_mfma_f32_32x32x16_bf16 v[16:31], v[116:119], v[148:151], v[16:31]\n"
    "v_cvt_pk_bf16_f32 v139, v70, v71\n"
    "v_exp_f32 v74, v74\n"
    "v_exp_f32 v75, v75\n"
    "v_cvt_pk_bf16_f32 v140, v72, v73\n"
    "v_mfma_f32_16x16x32_bf16 v[160:163], v[152:155], v[148:151], v[160:163]\n"
    "v_exp_f32 v76, v76\n"
    "v_exp_f32 v77, v77\n"
    "v_cvt_pk_bf16_f32 v141, v74, v75\n"
    "v_exp_f32 v78, v78\n"
    "v_exp_f32 v79, v79\n"
    "v_cvt_pk_bf16_f32 v142, v76, v77\n"
    "s_waitcnt lgkmcnt(0)\n"
    "v_cvt_pk_bf16_f32 v143, v78, v79\n"
    "v_mfma_f32_32x32x16_bf16 v[64:79], v[96:99], v[120:123], v[32:47]\n"
    "v_exp_f32 v80, v80\n"
    "v_exp_f32 v81, v81\n"
    "v_exp_f32 v82, v82\n"
    "v_mfma_f32_32x32x16_bf16 v[64:79], v[100:103], v[124:127], v[64:79]\n"
    "v_exp_f32 v83, v83\n"
    "v_cvt_pk_bf16_f32 v144, v80, v81\n"
    "v_exp_f32 v84, v84\n"
    "v_exp_f32 v85, v85\n"
    "v_mfma_f32_32x32x16_bf16 v[0:15], v[104:107], v[136:139], v[0:15]\n"
    "v_cvt_pk_bf16_f32 v145, v82, v83\n"
    "v_exp_f32 v86, v86\n"
    "v_exp_f32 v87, v87\n"
    "v_cvt_pk_bf16_f32 v146, v84, v85\n"
    "v_mfma_f32_16x16x32_bf16 v[156:159], v[152:155], v[136:139], v[156:159]\n"
    "v_exp_f32 v88, v88\n"
    "v_exp_f32 v89, v89\n"
    "v_mfma_f32_32x32x16_bf16 v[0:15], v[108:111], v[140:143], v[0:15]\n"
    "v_cvt_pk_bf16_f32 v147, v86, v87\n"
    "v_exp_f32 v90, v90\n"
    "v_exp_f32 v91, v91\n"
    "v_cvt_pk_bf16_f32 v148, v88, v89\n"
    "v_mfma_f32_16x16x32_bf16 v[156:159], v[152:155], v[140:143], v[156:159]\n"
    "v_exp_f32 v92, v92\n"
    "v_exp_f32 v93, v93\n"
    "v_cvt_pk_bf16_f32 v149, v90, v91\n"
    "v_exp_f32 v94, v94\n"
    "v_exp_f32 v95, v95\n"
    "v_cvt_pk_bf16_f32 v150, v92, v93\n"
    "s_barrier\n"
    "s_cmp_lg_u32 s40, 3\n"
    "s_cbranch_scc1 12f\n"
    "global_load_dword v176, v[174:175], off sc1\n"
    "global_load_dword v177, v[174:175], off offset:128 sc1\n"
    "12:\n"
    "v_cvt_pk_bf16_f32 v151, v94, v95\n"
    "v_mfma_f32_32x32x16_bf16 v[80:95], v[96:99], v[128:131], v[48:63]\n"
    "v_exp_f32 v64, v64\n"
    "v_exp_f32 v65, v65\n"
    "v_exp_f32 v66, v66\n"
    "v_mfma_f32_32x32x16_bf16 v[80:95], v[100:103], v[132:135], v[80:95]\n"
    "ds_read_b128 v[96:99], v164 offset:49152\n"
    "ds_read_b128 v[100:103], v165 offset:49152\n"
    "v_exp_f32 v67, v67\n"
    "v_cvt_pk_bf16_f32 v136, v64, v65\n"
    "v_exp_f32 v68, v68\n"
    "v_exp_f32 v69, v69\n"
    "v_mfma_f32_32x32x16_bf16 v[16:31], v[104:107], v[144:147], v[16:31]\n"
    "ds_read_b128 v[112:115], v172 offset:40960\n"
    "ds_read_b128 v[116:119], v173 offset:40960\n"
    "v_cvt_pk_bf16_f32 v137, v66, v67\n"
    "v_exp_f32 v70, v70\n"
    "v_exp_f32 v71, v71\n"
    "v_cvt_pk_bf16_f32 v138, v68, v69\n"
    "v_mfma_f32_16x16x32_bf16 v[160:163], v[152:155], v[144:147], v[160:163]\n"
    "v_exp_f32 v72, v72\n"
    "v_exp_f32 v73, v73\n"
    "v_mfma_f32_32x32x16_bf16 v[16:31], v[108:111], v[148:151], v[16:31]\n"
    "v_cvt_pk_bf16_f32 v139, v70, v71\n"
    "v_exp_f32 v74, v74\n"
    "v_exp_f32 v75, v75\n"
    "v_cvt_pk_bf16_f32 v140, v72, v73\n"
    "v_mfma_f32_16x16x32_bf16 v[160:163], v[152:155], v[148:151], v[160:163]\n"
    "v_exp_f32 v76, v76\n"
    "v_exp_f32 v77, v77\n"
    "v_cvt_pk_bf16_f32 v141, v74, v75\n"
    "v_exp_f32 v78, v78\n"
    "v_exp_f32 v79, v79\n"
    "v_cvt_pk_bf16_f32 v142, v76, v77\n"
    "s_waitcnt lgkmcnt(0)\n"
    "v_cvt_pk_bf16_f32 v143, v78, v79\n"
    "v_mfma_f32_32x32x16_bf16 v[64:79], v[96:99], v[120:123], v[32:47]\n"
    "v_exp_f32 v80, v80\n"
    "v_exp_f32 v81, v81\n"
    "v_exp_f32 v82, v82\n"
    "v_mfma_f32_32x32x16_bf16 v[64:79], v[100:103], v[124:127], v[64:79]\n"
    "v_exp_f32 v83, v83\n"
    "v_cvt_pk_bf16_f32 v144, v80, v81\n"
    "v_exp_f32 v84, v84\n"
    "v_exp_f32 v85, v85\n"
    "v_mfma_f32_32x32x16_bf16 v[0:15], v[112:115], v[136:139], v[0:15]\n"
    "v_cvt_pk_bf16_f32 v145, v82, v83\n"
    "v_exp_f32 v86, v86\n"
    "v_exp_f32 v87, v87\n"
    "v_cvt_pk_bf16_f32 v146, v84, v85\n"
    "v_mfma_f32_16x16x32_bf16 v[156:159], v[152:155], v[136:139], v[156:159]\n"
    "v_exp_f32 v88, v88\n"
    "v_exp_f32 v89, v89\n"
    "v_mfma_f32_32x32x16_bf16 v[0:15], v[116:119], v[140:143], v[0:15]\n"
    "v_cvt_pk_bf16_f32 v147, v86, v87\n"
    "v_exp_f32 v90, v90\n"
    "v_exp_f32 v91, v91\n"
    "v_cvt_pk_bf16_f32 v148, v88, v89\n"
    "v_mfma_f32_16x16x32_bf16 v[156:159], v[152:155], v[140:143], v[156:159]\n"
    "v_exp_f32 v92, v92\n"
    "v_exp_f32 v93, v93\n"
    "v_cvt_pk_bf16_f32 v149, v90, v91\n"
    "v_exp_f32 v94, v94\n"
    "v_exp_f32 v95, v95\n"
    "v_cvt_pk_bf16_f32 v150, v92, v93\n"
    "v_cvt_pk_bf16_f32 v151, v94, v95\n"
    "v_mfma_f32_32x32x16_bf16 v[80:95], v[96:99], v[128:131], v[48:63]\n"
    "v_exp_f32 v64, v64\n"
    "v_exp_f32 v65, v65\n"
    "v_exp_f32 v66, v66\n"
    "v_mfma_f32_32x32x16_bf16 v[80:95], v[100:103], v[132:135], v[80:95]\n"
    "ds_read_b128 v[96:99], v164 offset:51200\n"
    "ds_read_b128 v[100:103], v165 offset:51200\n"
    "v_exp_f32 v67, v67\n"
    "v_cvt_pk_bf16_f32 v136, v64, v65\n"
    "v_exp_f32 v68, v68\n"
    "v_exp_f32 v69, v69\n"
    "v_mfma_f32_32x32x16_bf16 v[16:31], v[112:115], v[144:147], v[16:31]\n"
    "ds_read_b128 v[104:107], v166 offset:57344\n"
    "ds_read_b128 v[108:111], v167 offset:57344\n"
    "v_cvt_pk_bf16_f32 v137, v66, v67\n"
    "v_exp_f32 v70, v70\n"
    "v_exp_f32 v71, v71\n"
    "v_cvt_pk_bf16_f32 v138, v68, v69\n"
    "v_mfma_f32_16x16x32_bf16 v[160:163], v[152:155], v[144:147], v[160:163]\n"
    "v_exp_f32 v72, v72\n"
    "v_exp_f32 v73, v73\n"
    "v_mfma_f32_32x32x16_bf16 v[16:31], v[116:119], v[148:151], v[16:31]\n"
    "v_cvt_pk_bf16_f32 v139, v70, v71\n"
    "v_exp_f32 v74, v74\n"
    "v_exp_f32 v75, v75\n"
    "v_cvt_pk_bf16_f32 v140, v72, v73\n"
    "v_mfma_f32_16x16x32_bf16 v[160:163], v[152:155], v[148:151], v[160:163]\n"
    "v_exp_f32 v76, v76\n"
    "v_exp_f32 v77, v77\n"
    "v_cvt_pk_bf16_f32 v141, v74, v75\n"
    "v_exp_f32 v78, v78\n"
    "v_exp_f32 v79, v79\n"
    "v_cvt_pk_bf16_f32 v142, v76, v77\n"
    "s_waitcnt lgkmcnt(0)\n"
    "v_cvt_pk_bf16_f32 v143, v78, v79\n"
    "v_mfma_f32_32x32x16_bf16 v[64:79], v[96:99], v[120:123], v[32:47]\n"
    "v_exp_f32 v80, v80\n"
    "v_exp_f32 v81, v81\n"
    "v_exp_f32 v82, v82\n"
    "v_mfma_f32_32x32x16_bf16 v[64:79], v[100:103], v[124:127], v[64:79]\n"
    "v_exp_f32 v83, v83\n"
    "v_cvt_pk_bf16_f32 v144, v80, v81\n"
    "v_exp_f32 v84, v84\n"
    "v_exp_f32 v85, v85\n"
    "v_mfma_f32_32x32x16_bf16 v[0:15], v[104:107], v[136:139], v[0:15]\n"
    "v_cvt_pk_bf16_f32 v145, v82, v83\n"
    "v_exp_f32 v86, v86\n"
    "v_exp_f32 v87, v87\n"
    "v_cvt_pk_bf16_f32 v146, v84, v85\n"
    "v_mfma_f32_16x16x32_bf16 v[156:159], v[152:155], v[136:139], v[156:159]\n"
    "v_exp_f32 v88, v88\n"
    "v_exp_f32 v89, v89\n"
    "v_mfma_f32_32x32x16_bf16 v[0:15], v[108:111], v[140:143], v[0:15]\n"
    "v_cvt_pk_bf16_f32 v147, v86, v87\n"
    "v_exp_f32 v90, v90\n"
    "v_exp_f32 v91, v91\n"
    "v_cvt_pk_bf16_f32 v148, v88, v89\n"
    "v_mfma_f32_16x16x32_bf16 v[156:159], v[152:155], v[140:143], v[156:159]\n"
    "v_exp_f32 v92, v92\n"
    "v_exp_f32 v93, v93\n"
    "v_cvt_pk_bf16_f32 v149, v90, v91\n"
    "v_exp_f32 v94, v94\n"
    "v_exp_f32 v95, v95\n"
    "v_cvt_pk_bf16_f32 v150, v92, v93\n"
    "v_cvt_pk_bf16_f32 v151, v94, v95\n"
    "v_mfma_f32_32x32x16_bf16 v[80:95], v[96:99], v[128:131], v[48:63]\n"
    "v_exp_f32 v64, v64\n"
    "v_exp_f32 v65, v65\n"
    "v_exp_f32 v66, v66\n"
    "v_mfma_f32_32x32x16_bf16 v[80:95], v[100:103], v[132:135], v[80:95]\n"
    "ds_read_b128 v[96:99], v164 offset:53248\n"
    "ds_read_b128 v[100:103], v165 offset:53248\n"
    "v_exp_f32 v67, v67\n"
    "v_cvt_pk_bf16_f32 v136, v64, v65\n"
    "v_exp_f32 v68, v68\n"
    "v_exp_f32 v69, v69\n"
    "v_mfma_f32_32x32x16_bf16 v[16:31], v[104:107], v[144:147], v[16:31]\n"
    "ds_read_b128 v[112:115], v168 offset:57344\n"
    "ds_read_b128 v[116:119], v169 offset:57344\n"
    "v_cvt_pk_bf16_f32 v137, v66, v67\n"
    "v_exp_f32 v70, v70\n"
    "v_exp_f32 v71, v71\n"
    "v_cvt_pk_bf16_f32 v138, v68, v69\n"
    "v_mfma_f32_16x16x32_bf16 v[160:163], v[152:155], v[144:147], v[160:163]\n"
    "v_exp_f32 v72, v72\n"
    "v_exp_f32 v73, v73\n"
    "v_mfma_f32_32x32x16_bf16 v[16:31], v[108:111], v[148:151], v[16:31]\n"
    "v_cvt_pk_bf16_f32 v139, v70, v71\n"
    "v_exp_f32 v74, v74\n"
    "v_exp_f32 v75, v75\n"
    "v_cvt_pk_bf16_f32 v140, v72, v73\n"
    "v_mfma_f32_16x16x32_bf16 v[160:163], v[152:155], v[148:151], v[160:163]\n"
    "v_exp_f32 v76, v76\n"
    "v_exp_f32 v77, v77\n"
    "v_cvt_pk_bf16_f32 v141, v74, v75\n"
    "v_exp_f32 v78, v78\n"
    "v_exp_f32 v79, v79\n"
    "v_cvt_pk_bf16_f32 v142, v76, v77\n"
    "s_waitcnt lgkmcnt(0)\n"
    "v_cvt_pk_bf16_f32 v143, v78, v79\n"
    "v_mfma_f32_32x32x16_bf16 v[64:79], v[96:99], v[120:123], v[32:47]\n"
    "v_exp_f32 v80, v80\n"
    "v_exp_f32 v81, v81\n"
    "v_exp_f32 v82, v82\n"
    "v_mfma_f32_32x32x16_bf16 v[64:79], v[100:103], v[124:127], v[64:79]\n"
    "v_exp_f32 v83, v83\n"
    "v_cvt_pk_bf16_f32 v144, v80, v81\n"
    "v_exp_f32 v84, v84\n"
    "v_exp_f32 v85, v85\n"
    "v_mfma_f32_32x32x16_bf16 v[0:15], v[112:115], v[136:139], v[0:15]\n"
    "v_cvt_pk_bf16_f32 v145, v82, v83\n"
    "v_exp_f32 v86, v86\n"
    "v_exp_f32 v87, v87\n"
    "v_cvt_pk_bf16_f32 v146, v84, v85\n"
    "v_mfma_f32_16x16x32_bf16 v[156:159], v[152:155], v[136:139], v[156:159]\n"
    "v_exp_f32 v88, v88\n"
    "v_exp_f32 v89, v89\n"
    "v_mfma_f32_32x32x16_bf16 v[0:15], v[116:119], v[140:143], v[0:15]\n"
    "v_cvt_pk_bf16_f32 v147, v86, v87\n"
    "v_exp_f32 v90, v90\n"
    "v_exp_f32 v91, v91\n"
    "v_cvt_pk_bf16_f32 v148, v88, v89\n"
    "v_mfma_f32_16x16x32_bf16 v[156:159], v[152:155], v[140:143], v[156:159]\n"
    "v_exp_f32 v92, v92\n"
    "v_exp_f32 v93, v93\n"
    "v_cvt_pk_bf16_f32 v149, v90, v91\n"
    "v_exp_f32 v94, v94\n"
    "v_exp_f32 v95, v95\n"
    "v_cvt_pk_bf16_f32 v150, v92, v93\n"
    "v_cvt_pk_bf16_f32 v151, v94, v95\n"
    "v_mfma_f32_32x32x16_bf16 v[80:95], v[96:99], v[128:131], v[48:63]\n"
    "v_exp_f32 v64, v64\n"
    "v_exp_f32 v65, v65\n"
    "v_exp_f32 v66, v66\n"
    "v_mfma_f32_32x32x16_bf16 v[80:95], v[100:103], v[132:135], v[80:95]\n"
    "ds_read_b128 v[96:99], v164 offset:55296\n"
    "ds_read_b128 v[100:103], v165 offset:55296\n"
    "v_exp_f32 v67, v67\n"
    "v_cvt_pk_bf16_f32 v136, v64, v65\n"
    "v_exp_f32 v68, v68\n"
    "v_exp_f32 v69, v69\n"
    "v_mfma_f32_32x32x16_bf16 v[16:31], v[112:115], v[144:147], v[16:31]\n"
    "ds_read_b128 v[104:107], v170 offset:57344\n"
    "ds_read_b128 v[108:111], v171 offset:57344\n"
    "v_cvt_pk_bf16_f32 v137, v66, v67\n"
    "v_exp_f32 v70, v70\n"
    "v_exp_f32 v71, v71\n"
    "v_cvt_pk_bf16_f32 v138, v68, v69\n"
    "v_mfma_f32_16x16x32_bf16 v[160:163], v[152:155], v[144:147], v[160:163]\n"
    "v_exp_f32 v72, v72\n"
    "v_exp_f32 v73, v73\n"
    "v_mfma_f32_32x32x16_bf16 v[16:31], v[116:119], v[148:151], v[16:31]\n"
    "v_cvt_pk_bf16_f32 v139, v70, v71\n"
    "v_exp_f32 v74, v74\n"
    "v_exp_f32 v75, v75\n"
    "v_cvt_pk_bf16_f32 v140, v72, v73\n"
    "v_mfma_f32_16x16x32_bf16 v[160:163], v[152:155], v[148:151], v[160:163]\n"
    "v_exp_f32 v76, v76\n"
    "v_exp_f32 v77, v77\n"
    "v_cvt_pk_bf16_f32 v141, v74, v75\n"
    "v_exp_f32 v78, v78\n"
    "v_exp_f32 v79, v79\n"
    "v_cvt_pk_bf16_f32 v142, v76, v77\n"
    "s_waitcnt lgkmcnt(0)\n"
    "v_cvt_pk_bf16_f32 v143, v78, v79\n"
    "v_mfma_f32_32x32x16_bf16 v[64:79], v[96:99], v[120:123], v[32:47]\n"
    "v_exp_f32 v80, v80\n"
    "v_exp_f32 v81, v81\n"
    "v_exp_f32 v82, v82\n"
    "v_mfma_f32_32x32x16_bf16 v[64:79], v[100:103], v[124:127], v[64:79]\n"
    "v_exp_f32 v83, v83\n"
    "v_cvt_pk_bf16_f32 v144, v80, v81\n"
    "v_exp_f32 v84, v84\n"
    "v_exp_f32 v85, v85\n"
    "v_mfma_f32_32x32x16_bf16 v[0:15], v[104:107], v[136:139], v[0:15]\n"
    "v_cvt_pk_bf16_f32 v145, v82, v83\n"
    "v_exp_f32 v86, v86\n"
    "v_exp_f32 v87, v87\n"
    "v_cvt_pk_bf16_f32 v146, v84, v85\n"
    "v_mfma_f32_16x16x32_bf16 v[156:159], v[152:155], v[136:139], v[156:159]\n"
    "v_exp_f32 v88, v88\n"
    "v_exp_f32 v89, v89\n"
    "v_mfma_f32_32x32x16_bf16 v[0:15], v[108:111], v[140:143], v[0:15]\n"
    "v_cvt_pk_bf16_f32 v147, v86, v87\n"
    "v_exp_f32 v90, v90\n"
    "v_exp_f32 v91, v91\n"
    "v_cvt_pk_bf16_f32 v148, v88, v89\n"
    "v_mfma_f32_16x16x32_bf16 v[156:159], v[152:155], v[140:143], v[156:159]\n"
    "v_exp_f32 v92, v92\n"
    "v_exp_f32 v93, v93\n"
    "v_cvt_pk_bf16_f32 v149, v90, v91\n"
    "v_exp_f32 v94, v94\n"
    "v_exp_f32 v95, v95\n"
    "v_cvt_pk_bf16_f32 v150, v92, v93\n"
    "s_cmp_eq_u32 s40, 3\n"
    "s_cbranch_scc1 13f\n"
    "s_barrier\n"
    "13:\n"
    "v_cvt_pk_bf16_f32 v151, v94, v95\n"
    "v_mfma_f32_32x32x16_bf16 v[80:95], v[96:99], v[128:131], v[48:63]\n"
    "v_exp_f32 v64, v64\n"
    "v_exp_f32 v65, v65\n"
    "v_exp_f32 v66, v66\n"
    "v_mfma_f32_32x32x16_bf16 v[80:95], v[100:103], v[132:135], v[80:95]\n"
    "ds_read_b128 v[96:99], v164 offset:0\n"
    "ds_read_b128 v[100:103], v165 offset:0\n"
    "v_exp_f32 v67, v67\n"
    "v_cvt_pk_bf16_f32 v136, v64, v65\n"
    "v_exp_f32 v68, v68\n"
    "v_exp_f32 v69, v69\n"
    "v_mfma_f32_32x32x16_bf16 v[16:31], v[104:107], v[144:147], v[16:31]\n"
    "ds_read_b128 v[112:115], v172 offset:57344\n"
    "ds_read_b128 v[116:119], v173 offset:57344\n"
    "v_cvt_pk_bf16_f32 v137, v66, v67\n"
    "v_exp_f32 v70, v70\n"
    "v_exp_f32 v71, v71\n"
    "v_cvt_pk_bf16_f32 v138, v68, v69\n"
    "v_mfma_f32_16x16x32_bf16 v[160:163], v[152:155], v[144:147], v[160:163]\n"
    "v_exp_f32 v72, v72\n"
    "v_exp_f32 v73, v73\n"
    "v_mfma_f32_32x32x16_bf16 v[16:31], v[108:111], v[148:151], v[16:31]\n"
    "v_cvt_pk_bf16_f32 v139, v70, v71\n"
    "v_exp_f32 v74, v74\n"
    "v_exp_f32 v75, v75\n"
    "v_cvt_pk_bf16_f32 v140, v72, v73\n"
    "v_mfma_f32_16x16x32_bf16 v[160:163], v[152:155], v[148:151], v[160:163]\n"
    "v_exp_f32 v76, v76\n"
    "v_exp_f32 v77, v77\n"
    "v_cvt_pk_bf16_f32 v141, v74, v75\n"
    "v_exp_f32 v78, v78\n"
    "v_exp_f32 v79, v79\n"
    "v_cvt_pk_bf16_f32 v142, v76, v77\n"
    "s_waitcnt lgkmcnt(0)\n"
    "v_cvt_pk_bf16_f32 v143, v78, v79\n"
    "v_mfma_f32_32x32x16_bf16 v[64:79], v[96:99], v[120:123], v[32:47]\n"
    "v_exp_f32 v80, v80\n"
    "v_exp_f32 v81, v81\n"
    "v_exp_f32 v82, v82\n"
    "v_mfma_f32_32x32x16_bf16 v[64:79], v[100:103], v[124:127], v[64:79]\n"
    "v_exp_f32 v83, v83\n"
    "v_cvt_pk_bf16_f32 v144, v80, v81\n"
    "v_exp_f32 v84, v84\n"
    "v_exp_f32 v85, v85\n"
    "v_mfma_f32_32x32x16_bf16 v[0:15], v[112:115], v[136:139], v[0:15]\n"
    "v_cvt_pk_bf16_f32 v145, v82, v83\n"
    "v_exp_f32 v86, v86\n"
    "v_exp_f32 v87, v87\n"
    "v_cvt_pk_bf16_f32 v146, v84, v85\n"
    "v_mfma_f32_16x16x32_bf16 v[156:159], v[152:155], v[136:139], v[156:159]\n"
    "v_exp_f32 v88, v88\n"
    "v_exp_f32 v89, v89\n"
    "v_mfma_f32_32x32x16_bf16 v[0:15], v[116:119], v[140:143], v[0:15]\n"
    "v_cvt_pk_bf16_f32 v147, v86, v87\n"
    "v_exp_f32 v90, v90\n"
    "v_exp_f32 v91, v91\n"
    "v_cvt_pk_bf16_f32 v148, v88, v89\n"
    "v_mfma_f32_16x16x32_bf16 v[156:159], v[152:155], v[140:143], v[156:159]\n"
    "v_exp_f32 v92, v92\n"
    "v_exp_f32 v93, v93\n"
    "v_cvt_pk_bf16_f32 v149, v90, v91\n"
    "v_exp_f32 v94, v94\n"
    "v_exp_f32 v95, v95\n"
    "v_cvt_pk_bf16_f32 v150, v92, v93\n"
    "s_add_u32 s40, s40, 1\n"
    "s_cmp_lt_u32 s40, 4\n"
    "s_cbranch_scc1 9b\n"
    "v_cvt_pk_bf16_f32 v151, v94, v95\n"
    "s_nop 1\n"
    "v_mfma_f32_32x32x16_bf16 v[16:31], v[112:115], v[144:147], v[16:31]\n"
    "v_mfma_f32_16x16x32_bf16 v[160:163], v[152:155], v[144:147], v[160:163]\n"
    "v_mfma_f32_32x32x16_bf16 v[16:31], v[116:119], v[148:151], v[16:31]\n"
    "v_mfma_f32_16x16x32_bf16 v[160:163], v[152:155], v[148:151], v[160:163]\n"
    "s_setprio 0\n"
    "s_waitcnt vmcnt(0)\n"
    "s_nop 15\n"

        : "+{v[0:15]}"(oa),
          "+{v[16:31]}"(ob), "+{v[64:79]}"(sa), "+{v[80:95]}"(sb), "+{v[96:99]}"(k0), "+{v[100:103]}"(k1), "+{v[112:115]}"(vz0), "+{v[116:119]}"(vz1),
          "+{v[144:147]}"(pb0), "+{v[148:151]}"(pb1), "+{v[156:159]}"(lsa), "+{v[160:163]}"(lsb), "={v176}"(ga), "={v177}"(gb)
        : "{v[32:47]}"(nma), "{v[48:63]}"(nmb), "{v[120:123]}"(q[0][0]), "{v[124:127]}"(q[0][1]), "{v[128:131]}"(q[1][0]), "{v[132:135]}"(q[1][1]), "{v[152:155]}"(selA),
          "{v164}"(rK0), "{v165}"(rK1), "{v166}"(rv[0]), "{v167}"(rv[1]), "{v168}"(rv[2]), "{v169}"(rv[3]), "{v170}"(rv[4]), "{v171}"(rv[5]), "{v172}"(rv[6]), "{v173}"(rv[7]),
          "{v[174:175]}"(gp)
        : "memory", "scc", "s40", "s44", "s45", "s46", "v104", "v105", "v106", "v107", "v108", "v109", "v110", "v111", "v136", "v137", "v138", "v139", "v140", "v141", "v142", "v143");
    { const float l0 = __shfl(lsa[0], r & 15), l1 = __shfl(lsa[1], r & 15); la = (r < 16) ? l0 : l1; }
    { const float l0 = __shfl(lsb[0], r & 15), l1 = __shfl(lsb[1], r & 15); lb = (r < 16) ? l0 : l1; }
    bool ok = la < 3.0e38f && lb < 3.0e38f;
#pragma unroll
    for (int i = 0; i < 16; ++i) ok = ok && (__builtin_fabsf(oa[i]) < 3.0e38f) && (__builtin_fabsf(ob[i]) < 3.0e38f);
    return ok;
}

DI void attn_safe(const Params& a, const unsigned lds0, const Ctx& c, const int w, const bf16x8 (&q)[2][2], f32x16& oa, f32x16& ob, float& la, float& lb) {
    const int lane = lane_id();
    const int h = c.h;
    if (w >= 4) {
        const int hw = w - 4;
        const char* gK = (const char*)(a.kv + (size_t)h * (S_ * 32)) + hw * 2048;
        const char* gV = (const char*)(a.kv + (size_t)(32 + h) * (S_ * 32)) + hw * 2048;
        const unsigned voffK = (unsigned)((lane >> 2) * 64 + (((lane & 3) ^ ((lane >> 4) & 3)) * 16));
        const unsigned voffV0 = (unsigned)((lane >> 4) * 256 + (((lane & 15) ^ ((8 * hw + (lane >> 4)) & 15)) * 16)), voffV1 = (unsigned)((lane >> 4) * 256 + (((lane & 15) ^ ((8 * hw + 4 + (lane >> 4)) & 15)) * 16));
        const unsigned dK = lds0 + hw * 2048, dV = lds0 + 8192 + hw * 2048;
#pragma unroll 1
        for (int T = 0; T < NTILE; ++T) {
            asm volatile("s_barrier" ::: "memory");
            glds16c(voffK, gK + (size_t)T * 8192, dK); glds16c(voffK, gK + (size_t)T * 8192 + 1024, dK + 1024);
            glds16c(voffV0, gV + (size_t)T * 8192, dV); glds16c(voffV1, gV + (size_t)T * 8192 + 1024, dV + 1024);
            asm volatile("s_waitcnt vmcnt(0)\n\ts_barrier" ::: "memory");
        }
        return;
    }
    const unsigned rK0 = addr_k(lds0, lane, 0), rK1 = addr_k(lds0, lane, 1);
#pragma unroll
    for (int i = 0; i < 16; ++i) { oa[i] = 0.f; ob[i] = 0.f; }
    float m[2] = {-INFINITY, -INFINITY}, l[2] = {0.f, 0.f};
#pragma unroll 1
    for (int T = 0; T < NTILE; ++T) {
        asm volatile("s_waitcnt lgkmcnt(0)\n\ts_barrier" ::: "memory");
        asm volatile("s_barrier" ::: "memory");
#pragma unroll 1
        for (int u = 0; u < 4; ++u) {
            const bf16x8 k0 = ld128(LP(rK0) + u * 2048), k1 = ld128(LP(rK1) + u * 2048);
            const bf16x8 v0 = ld128(LP(addr_v(lds0, lane, u, 0) + 8192)), v1 = ld128(LP(addr_v(lds0, lane, u, 1) + 8192));
#pragma unroll
            for (int b = 0; b < 2; ++b) {
                f32x16& o = b ? ob : oa;
                f32x16 z;
#pragma unroll
                for (int i = 0; i < 16; ++i) z[i] = 0.f;
                f32x16 s = MFMA32(k0, q[b][0], z); s = MFMA32(k1, q[b][1], s);
                float mx = s[0];
#pragma unroll
                for (int i = 1; i < 16; ++i) mx = __builtin_fmaxf(mx, s[i]);
                auto rr = __builtin_amdgcn_permlane32_swap(__float_as_uint(mx), __float_as_uint(mx), false, false);
                mx = __builtin_fmaxf(__uint_as_float(rr[0]), __uint_as_float(rr[1]));
                const float mn = __builtin_fmaxf(m[b], mx);
                const float al = (mn == m[b]) ? 1.0f : __builtin_amdgcn_exp2f(m[b] - mn);
                const float mref = (mn == -INFINITY) ? 0.f : mn;
                m[b] = mn;
                float ps = 0.f; u32x4 p0, p1;
#pragma unroll
                for (int i = 0; i < 16; ++i) s[i] = __builtin_amdgcn_exp2f(s[i] - mref);
#pragma unroll
                for (int i = 0; i < 4; ++i) { p0[i] = cvtpk(s[2 * i], s[2 * i + 1]); p1[i] = cvtpk(s[8 + 2 * i], s[8 + 2 * i + 1]); }
#pragma unroll
                for (int i = 0; i < 4; ++i) ps += __uint_as_float(p0[i] << 16) + __uint_as_float(p0[i] & 0xffff0000u) + __uint_as_float(p1[i] << 16) + __uint_as_float(p1[i] & 0xffff0000u);
                l[b] = l[b] * al + ps;
#pragma unroll
                for (int i = 0; i < 16; ++i) o[i] *= al;
                o = MFMA32(v0, __builtin_bit_cast(bf16x8, p0), o);
                o = MFMA32(v1, __builtin_bit_cast(bf16x8, p1), o);
            }
        }
    }
    { auto rr = __builtin_amdgcn_permlane32_swap(__float_as_uint(l[0]), __float_as_uint(l[0]), false, false); la = __uint_as_float(rr[0]) + __uint_as_float(rr[1]); }
    { auto rr = __builtin_amdgcn_permlane32_swap(__float_as_uint(l[1]), __float_as_uint(l[1]), false, false); lb = __uint_as_float(rr[0]) + __uint_as_float(rr[1]); }
}

DI void store_o(const Params& a, const Ctx& c, const int row, const int hh, const f32x16& o, const float sc) {
    unsigned pk[4][2];
#pragma unroll
    for (int G = 0; G < 4; ++G) { pk[G][0] = cvtpk(o[4 * G] * sc, o[4 * G + 1] * sc); pk[G][1] = cvtpk(o[4 * G + 2] * sc, o[4 * G + 3] * sc); }
    bf16_t* op = a.ho + (size_t)row * HO_LD + F_ + c.h * 32 + hh * 8;
#pragma unroll
    for (int G = 0; G < 4; G += 2) {
        auto r0 = __builtin_amdgcn_permlane32_swap(pk[G][0], pk[G + 1][0], false, false);
        auto r1 = __builtin_amdgcn_permlane32_swap(pk[G][1], pk[G + 1][1], false, false);
        st16w(op + G * 8, (u32x4){r0[0], r1[0], r0[1], r1[1]});
    }
}
#undef VMB
#undef P2_KOFF
}

namespace p3 {
constexpr int NS = 5, STAGE = 32768, NKT = 36;
DI int kh_of() { return blockIdx.x & 1; }
DI int rb_of() { return ((blockIdx.x & 7) >> 2) * 8 + ((blockIdx.x >> 3) & 7); }
DI int cb_of() { return (((blockIdx.x & 7) >> 1) & 1) * 4 + (blockIdx.x >> 6); }

struct Prod { unsigned voffA, voffB0, voffB1, dA, dB; const char* gA; const char* gWd; const char* gWo; int nH; };
DI Prod prod_setup(const Params& a, const unsigned lds0, const int pw, const int lane) {
    const int kh = kh_of(), rb = rb_of(), cb = cb_of();
    Prod P;
    P.nH = kh ? 20 : 36;
    P.voffA = (unsigned)((lane >> 3) * (HO_LD * 2) + (((lane & 7) ^ (lane >> 3)) * 16));
    P.voffB0 = (unsigned)((lane >> 4) * 2048 + (((lane & 15) ^ ((lane >> 4) << 1)) * 16));
    P.voffB1 = (unsigned)((lane >> 4) * 2048 + (((lane & 15) ^ (((lane >> 4) | 4) << 1)) * 16));
    P.gA = (const char*)a.ho + (size_t)(rb * 128 + pw * 32) * (HO_LD * 2) + (kh ? 2304 * 2 : 0);
    P.gWd = (const char*)a.wd + (size_t)((kh ? 2304 : 0) + pw * 16) * 2048 + cb * 256;
    P.gWo = (const char*)a.wo + (size_t)(pw * 16) * 2048 + cb * 256 - (size_t)P.nH * (64 * 2048);
    P.dA = lds0 + pw * 4096; P.dB = lds0 + 16384 + pw * 4096;
    return P;
}
DI void prod_issue(const Prod& P, const int t, const unsigned so) {
    const char* sa = P.gA + (size_t)t * 128; const char* sb = (t < P.nH ? P.gWd : P.gWo) + (size_t)t * (64 * 2048);
#pragma unroll
    for (int i = 0; i < 4; ++i) glds16c(P.voffA, sa + (size_t)i * 8 * (HO_LD * 2), P.dA + so + i * 1024);
#pragma unroll
    for (int i = 0; i < 4; ++i) glds16((i & 2) ? P.voffB1 : P.voffB0, sb + i * 4 * 2048, P.dB + so + i * 1024);
}
DI void producer_prologue(const Params& a, const unsigned lds0, const int pw, const int lane) {
    const Prod P = prod_setup(a, lds0, pw, lane);
#pragma unroll
    for (int t = 0; t < 3; ++t) prod_issue(P, t, (t + 2) * STAGE);
}

DI void body(const Params& a, const unsigned lds0, const int w, const unsigned tag) {
    const int lane = lane_id();
    const int kh = kh_of(), rb = rb_of(), cb = cb_of();
    const int tile = rb * 8 + cb;
    const int nH = kh ? 20 : 36;
    if (w >= 4) {
        const Prod P = prod_setup(a, lds0, w - 4, lane);
        prod_issue(P, 3, 0);
        unsigned so = STAGE;
        const unsigned* pollp = a.flags + FLAG_O + (rb >> 1) * 32 + (lane & 31);
#define P3_PSTEP(t) do { asm volatile("s_waitcnt vmcnt(24)\n\ts_barrier" ::: "memory"); \
            if ((t) + NS - 1 < NKT) prod_issue(P, (t) + NS - 1, so); \
            so = (so == (NS - 1) * STAGE) ? 0u : so + STAGE; } while (0)
        for (int t = 0; t <= 10; ++t) P3_PSTEP(t);
        unsigned pollv = 0;
        asm volatile("global_load_dword %0, %1, off sc1" : "+v"(pollv) : "v"(pollp) : "memory");
        P3_PSTEP(11); P3_PSTEP(12);
        asm volatile("s_waitcnt vmcnt(16)" : "+v"(pollv) :: "memory");
        const bool okO = __builtin_amdgcn_ballot_w64(pollv != tag) == 0ull;
        for (int t = 13; t <= 15; ++t) P3_PSTEP(t);
        if (kh && !okO) wait_flags(pollp, tag);
        for (int t = 16; t < NKT - (NS - 2); ++t) P3_PSTEP(t);
#undef P3_PSTEP
        asm volatile("s_waitcnt vmcnt(16)\n\ts_barrier" ::: "memory");
        asm volatile("s_waitcnt vmcnt(8)\n\ts_barrier" ::: "memory");
        asm volatile("s_waitcnt vmcnt(0)\n\ts_barrier" ::: "memory");
        asm volatile("s_barrier" ::: "memory");
        return;
    }
    const int wr = w & 1, wc = w >> 1, fr = lane & 15, fq = lane >> 4;
    const int q = (lane & 15) >> 2, p4 = lane & 3, sR = q | ((fq & 1) << 2);
    unsigned rA[3][2], rB[3][4];
#pragma unroll
    for (int sp = 0; sp < 3; ++sp) {
#pragma unroll
        for (int ks = 0; ks < 2; ++ks) { rA[sp][ks] = lds0 + sp * 2 * STAGE + (wr * 64 + fr) * 128 + (((fq + 4 * ks) ^ (fr & 7)) * 16); asm volatile("" : "+v"(rA[sp][ks])); }
#pragma unroll
        for (int n = 0; n < 4; ++n) { rB[sp][n] = lds0 + sp * 2 * STAGE + 16384 + (((8 * fq + q) * 256 + 32 * sR + 8 * p4) ^ ((wc * 4 + n) << 5)); asm volatile("" : "+v"(rB[sp][n])); }
    }
    f32x4 acc[4][4];
#pragma unroll
    for (int m = 0; m < 4; ++m)
#pragma unroll
        for (int n = 0; n < 4; ++n) acc[m][n] = (f32x4){0.f, 0.f, 0.f, 0.f};
    bf16x8 Af[2][4]; s16x4 Bh[2][4][2];
#define P3_RD(buf, i, SL, ks) do { if ((i) < 4) Af[buf][i] = ld128(LP(rA[(SL) >> 1][ks]) + ((SL) & 1) * STAGE + (i) * 2048); \
        else Bh[buf][((i) - 4) >> 1][((i) - 4) & 1] = ldtr(LP(rB[(SL) >> 1][((i) - 4) >> 1]) + ((SL) & 1) * STAGE + (ks) * 8192 + (((i) - 4) & 1) * 1024); } while (0)
    const int row0 = rb * 128 + wr * 64 + fr;
    float omg[4];
#pragma unroll
    for (int m = 0; m < 4; ++m) omg[m] = 1.0f - ldf_dev(a.g + row0 + m * 16);
    asm volatile("s_barrier" ::: "memory");
    KSTAMP(7);
#pragma unroll
    for (int i = 0; i < 12; ++i) P3_RD(0, i, 2, 0);
#define P3_TILE(t, SL, MORE, OFLAG) do { \
        _Pragma("unroll") for (int i = 0; i < 16; ++i) { \
            const int n = i >> 2, m = i & 3; \
            MFMA16A(acc[m][n], cat8(Bh[0][n][0], Bh[0][n][1]), Af[0][m]); \
            SBAR(); \
            if (i < 12) P3_RD(1, i, SL, 1); \
            SBAR(); } \
        if ((OFLAG) && (t) == 2) {        \
            asm volatile("s_waitcnt vmcnt(0) lgkmcnt(0)\n\ts_barrier" ::: "memory"); \
            if (w == 0 && lane == 0) flag_set(a.flags + FLAG_O + (blockIdx.x >> 3 & 7) * 32 + (blockIdx.x & 7) * 4 + (blockIdx.x >> 6), tag); \
        } else if (MORE) asm volatile("s_waitcnt lgkmcnt(0)\n\ts_barrier" ::: "memory");         \
        SBAR(); \
        _Pragma("unroll") for (int i = 0; i < 16; ++i) { \
            const int n = i >> 2, m = i & 3; \
            MFMA16A(acc[m][n], cat8(Bh[1][n][0], Bh[1][n][1]), Af[1][m]); \
            SBAR(); \
            if (i < 12 && (MORE)) P3_RD(0, i, ((SL) + 1) % NS, 0); \
            SBAR(); } \
        if ((t) + 1 == nH) { asm volatile("s_nop 7\n\ts_nop 7" ::: "memory"); SBAR(); _Pragma("unroll") for (int m = 0; m < 4; ++m) _Pragma("unroll") for (int n = 0; n < 4; ++n) { acc[m][n] *= omg[m]; asm volatile("" : "+v"(acc[m][n])); } \
            asm volatile("s_nop 3" ::: "memory"); SBAR(); } \
    } while (0)
    for (int t = 0; t < 35; t += 5) { P3_TILE(t, 2, true, false); P3_TILE(t + 1, 3, true, false); P3_TILE(t + 2, 4, true, true); P3_TILE(t + 3, 0, true, false); P3_TILE(t + 4, 1, true, false); }
    P3_TILE(35, 2, false, false);
    asm volatile("s_nop 7\n\ts_nop 7" ::: "memory"); SBAR();
    KSTAMP(8);
    if (wc != kh) {
        float* xs = a.xch + ((size_t)(tile * 2 + kh) * 2 + wr) * 4096 + lane * 4;
#pragma unroll
        for (int m = 0; m < 4; ++m)
#pragma unroll
            for (int n = 0; n < 4; ++n) st16w(xs + (m * 4 + n) * 256, acc[m][n]);
        asm volatile("s_waitcnt vmcnt(0)" ::: "memory");
    }
    asm volatile("s_barrier" ::: "memory");
    if (w == 0 && lane == 0) flag_set(a.flags + FLAG_X + (tile * 2 + kh) * 16, tag);
    if (wc == kh) {
        const unsigned* pf = a.flags + FLAG_X + (tile * 2 + (kh ^ 1)) * 16;
        unsigned spins = 0;
        while ((unsigned)__builtin_amdgcn_readfirstlane(flag_get(pf)) != tag) { __builtin_amdgcn_s_sleep(1); if (++spins > SPIN_MAX) break; }
        asm volatile("" ::: "memory");
        const float* xr = a.xch + ((size_t)(tile * 2 + (kh ^ 1)) * 2 + wr) * 4096 + lane * 4;
        f32x4 pv[4][4];
#pragma unroll
        for (int m = 0; m < 4; ++m)
#pragma unroll
            for (int n = 0; n < 4; ++n) asm volatile("global_load_dwordx4 %0, %1, off sc1" : "=v"(pv[m][n]) : "v"(xr + (m * 4 + n) * 256) : "memory");
        asm volatile("s_waitcnt vmcnt(0)" : "+v"(pv[0][0]), "+v"(pv[0][1]), "+v"(pv[0][2]), "+v"(pv[0][3]), "+v"(pv[1][0]), "+v"(pv[1][1]), "+v"(pv[1][2]), "+v"(pv[1][3]),
                     "+v"(pv[2][0]), "+v"(pv[2][1]), "+v"(pv[2][2]), "+v"(pv[2][3]), "+v"(pv[3][0]), "+v"(pv[3][1]), "+v"(pv[3][2]), "+v"(pv[3][3]) :: "memory");
        const int cperm = ((fq & 1) << 4) | ((fq >> 1) << 3);
        bf16_t* ob = a.out + (size_t)row0 * D_ + cb * 128 + wc * 64 + cperm;
#pragma unroll
        for (int m = 0; m < 4; ++m)
#pragma unroll
            for (int n = 0; n < 4; n += 2) {
                const f32x4 lo = acc[m][n] + pv[m][n], hi = acc[m][n + 1] + pv[m][n + 1];
                auto r0 = __builtin_amdgcn_permlane16_swap(cvtpk(lo[0], lo[1]), cvtpk(hi[0], hi[1]), false, false);
                auto r1 = __builtin_amdgcn_permlane16_swap(cvtpk(lo[2], lo[3]), cvtpk(hi[2], hi[3]), false, false);
                *(u32x4*)(ob + (size_t)m * 16 * D_ + n * 16) = (u32x4){r0[0], r1[0], r0[1], r1[1]};
            }
    }
#undef P3_RD
#undef P3_TILE
}
}

constexpr int LDS_BYTES = 163840;

__global__ __launch_bounds__(512, 2) void fused_kernel(Params a) {
    extern __shared__ __attribute__((aligned(16))) char smem[];
    const unsigned lds0 = (unsigned)(uintptr_t)smem;
    const int w = __builtin_amdgcn_readfirstlane(threadIdx.x >> 6);
    const int xg = blockIdx.x & 7, j = blockIdx.x >> 3;
    const int rb = j & 7, ct = xg * 4 + (j >> 3);
    KSTAMP(0);
    unsigned tag;
    {
        f32x4 acc[4][10];
        p1::mainloop(a, lds0, w, rb, ct, acc, tag);
        KSTAMP(2);
        p1::epilogue(a, lds0, acc, w, rb, ct, tag);
    }
    const int lane = lane_id();
    KSTAMP(3);
    p2::Ctx c;
    c.h = ct; c.qb = rb; c.rb3h = p3::rb_of() >> 1; c.tag = tag;
    volatile LAS unsigned* fl = (volatile LAS unsigned*)(size_t)lds0;
    if (w >= 4) {
        p2::helper(a, lds0, c, w);
        asm volatile("s_barrier" ::: "memory");
        asm volatile("s_barrier" ::: "memory");
        const unsigned bad = fl[lane & 3];
        if (__builtin_amdgcn_ballot_w64(bad != 0u) != 0ull) { bf16x8 qd[2][2]; f32x16 od0, od1; float ld0, ld1; p2::attn_safe(a, lds0, c, w, qd, od0, od1, ld0, ld1); }
    } else {
        f32x16 oa, ob; float la, lb, ga, gb; bf16x8 q[2][2];
        const bool ok = p2::attn_fast(a, lds0, c, w, oa, ob, la, lb, ga, gb, q);
        KSTAMP(5);
        asm volatile("s_barrier" ::: "memory");
        const bool wok = __builtin_amdgcn_ballot_w64(!ok) == 0ull;
        if (lane == 0) fl[w] = wok ? 0u : 1u;
        asm volatile("s_waitcnt lgkmcnt(0)\n\ts_barrier" ::: "memory");
        const unsigned bad = fl[lane & 3];
        if (__builtin_amdgcn_ballot_w64(bad != 0u) != 0ull) p2::attn_safe(a, lds0, c, w, q, oa, ob, la, lb);
        const int row = rb * 256 + w * 64 + (lane & 31);
        p2::store_o(a, c, row, lane >> 5, oa, ga / la);
        p2::store_o(a, c, row + 32, lane >> 5, ob, gb / lb);
    }
    asm volatile("s_waitcnt lgkmcnt(0)\n\ts_barrier" ::: "memory");
    KSTAMP(6);
    p3::body(a, lds0, w, tag);
    KSTAMP(9);
    if (blockIdx.x == 0 && w == 0) {
        unsigned spins = 0;
        for (int i = 0; i < 4; ++i)
            while (__builtin_amdgcn_ballot_w64(flag_get(a.flags + FLAG_H + i * 64 + lane) != tag) != 0ull) { __builtin_amdgcn_s_sleep(2); if (++spins > SPIN_MAX) break; }
        if (lane == 0) flag_set(a.flags + FLAG_EPOCH, tag);
    }
}

extern "C" void kernel_launch(void* const* d_in, const int* in_sizes, int n_in, void* d_out, int out_size, void* d_ws, size_t ws_size, hipStream_t stream) {
    char* ws = (char*)d_ws;
    Params p;
    p.x = (const bf16_t*)d_in[0]; p.wqkv = (const bf16_t*)d_in[1]; p.wo = (const bf16_t*)d_in[2]; p.wg = (const bf16_t*)d_in[4]; p.wu = (const bf16_t*)d_in[5];
    p.wd = (const bf16_t*)d_in[6]; p.gw = (const bf16_t*)d_in[7]; p.out = (bf16_t*)d_out;
    p.kv = (bf16_t*)ws; p.ho = (bf16_t*)(ws + (16 << 20)); p.g = (float*)(ws + (36 << 20)); p.xch = (float*)(ws + (40 << 20)); p.flags = (unsigned*)(ws + (50 << 20));
    (void)hipFuncSetAttribute((const void*)fused_kernel, hipFuncAttributeMaxDynamicSharedMemorySize, LDS_BYTES);
    fused_kernel<<<256, 512, LDS_BYTES, stream>>>(p);
}
```

```cpp
#include <hip/hip_runtime.h>

#include <hip/hip_runtime.h>
#include <stdint.h>
#define LAS __attribute__((address_space(3)))
#define DI __device__ __forceinline__
typedef unsigned short bf16_t;
typedef short bf16x8 __attribute__((ext_vector_type(8)));
typedef short s16x4 __attribute__((ext_vector_type(4)));
typedef short v4i16_t __attribute__((ext_vector_type(4)));
typedef float f32x4 __attribute__((ext_vector_type(4)));
typedef float f32x16 __attribute__((ext_vector_type(16)));
typedef unsigned u32x4 __attribute__((ext_vector_type(4)));
typedef unsigned u32x2 __attribute__((ext_vector_type(2)));
typedef float f32x2 __attribute__((ext_vector_type(2)));
typedef __bf16 bf16x2_t __attribute__((ext_vector_type(2)));
#define SBAR() __builtin_amdgcn_sched_barrier(0)
#define LP(x) ((const LAS char*)(size_t)(x))

constexpr int S_ = 2048, D_ = 1024, H_ = 32, DH_ = 32, F_ = 3584, HO_LD = F_ + D_;

DI unsigned cvtpk(float lo, float hi) { f32x2 v = {lo, hi}; bf16x2_t b = __builtin_convertvector(v, bf16x2_t); return __builtin_bit_cast(unsigned, b); }
DI void glds16(unsigned voff, const void* sbase, unsigned lds_dst) {
    asm volatile("s_mov_b32 m0, %2\n\ts_nop 0\n\tglobal_load_lds_dwordx4 %0, %1" :: "v"(voff), "s"(sbase), "s"(lds_dst) : "memory");
}
DI void glds16c(unsigned voff, const void* sbase, unsigned lds_dst) {
    asm volatile("s_mov_b32 m0, %2\n\ts_nop 0\n\tglobal_load_lds_dwordx4 %0, %1 sc1" :: "v"(voff), "s"(sbase), "s"(lds_dst) : "memory");
}
DI void st16w(void* p, u32x4 v) { asm volatile("global_store_dwordx4 %0, %1, off sc1\n\ts_nop 1" :: "v"(p), "v"(v) : "memory"); }
DI void st16w(void* p, f32x4 v) { asm volatile("global_store_dwordx4 %0, %1, off sc1\n\ts_nop 1" :: "v"(p), "v"(v) : "memory"); }
DI void st8w(void* p, u32x2 v) { asm volatile("global_store_dwordx2 %0, %1, off sc1\n\ts_nop 1" :: "v"(p), "v"(v) : "memory"); }
DI void st4w(void* p, unsigned v) { asm volatile("global_store_dword %0, %1, off sc1" :: "v"(p), "v"(v) : "memory"); }
DI s16x4 ldtr(const LAS char* p) { return __builtin_bit_cast(s16x4, __builtin_amdgcn_ds_read_tr16_b64_v4i16((LAS v4i16_t*)p)); }
DI bf16x8 ld128(const LAS char* p) { return *(const LAS bf16x8*)p; }
DI bf16x8 cat8(s16x4 lo, s16x4 hi) { return __builtin_shufflevector(lo, hi, 0, 1, 2, 3, 4, 5, 6, 7); }
DI int lane_id() { int l; asm volatile("v_mbcnt_lo_u32_b32 %0, -1, 0\n\tv_mbcnt_hi_u32_b32 %0, -1, %0" : "=v"(l)); return l; }
#define MFMA16(a, b, c) __builtin_amdgcn_mfma_f32_16x16x32_bf16(a, b, c, 0, 0, 0)
#define MFMA16A(acc, a, b) asm volatile("v_mfma_f32_16x16x32_bf16 %0, %1, %2, %0" : "+v"(acc) : "v"(a), "v"(b))
#define MFMA32(a, b, c) __builtin_amdgcn_mfma_f32_32x32x16_bf16(a, b, c, 0, 0, 0)

struct Params {
    const bf16_t* x; const bf16_t* wqkv; const bf16_t* wo; const bf16_t* wg; const bf16_t* wu; const bf16_t* wd; const bf16_t* gw;
    bf16_t* out;
    bf16_t* kv;
    bf16_t* ho;
    float* g;
    float* xch;
    unsigned* flags;
};
constexpr int FLAG_EPOCH = 0, FLAG_Q = 64, FLAG_H = FLAG_Q + 256, FLAG_O = FLAG_H + 256, FLAG_X = FLAG_O + 256;
constexpr unsigned SPIN_MAX = 1u << 17;

DI void flag_set(unsigned* p, unsigned tag) { __hip_atomic_store(p, tag, __ATOMIC_RELAXED, __HIP_MEMORY_SCOPE_AGENT); }
DI unsigned flag_get(const unsigned* p) { return __hip_atomic_load(p, __ATOMIC_RELAXED, __HIP_MEMORY_SCOPE_AGENT); }
DI void wait_flags(const unsigned* p, unsigned tag, bool skip = false) {
    unsigned spins = 0;
    while (__builtin_amdgcn_ballot_w64(!skip && flag_get(p) != tag) != 0ull) { __builtin_amdgcn_s_sleep(2); if (++spins > SPIN_MAX) break; }
    asm volatile("" ::: "memory");
}
DI float ldf_dev(const float* p) { return __hip_atomic_load(p, __ATOMIC_RELAXED, __HIP_MEMORY_SCOPE_AGENT); }

DI void store_tile32(bf16_t* base, const int ld, const f32x16& v, const int lane) {
    unsigned pk[4][2];
#pragma unroll
    for (int G = 0; G < 4; ++G) { pk[G][0] = cvtpk(v[4 * G], v[4 * G + 1]); pk[G][1] = cvtpk(v[4 * G + 2], v[4 * G + 3]); }
    u32x4 X, Y;
    { auto r0 = __builtin_amdgcn_permlane32_swap(pk[0][0], pk[1][0], false, false); auto r1 = __builtin_amdgcn_permlane32_swap(pk[0][1], pk[1][1], false, false); X = (u32x4){r0[0], r1[0], r0[1], r1[1]}; }
    { auto r0 = __builtin_amdgcn_permlane32_swap(pk[2][0], pk[3][0], false, false); auto r1 = __builtin_amdgcn_permlane32_swap(pk[2][1], pk[3][1], false, false); Y = (u32x4){r0[0], r1[0], r0[1], r1[1]}; }
#pragma unroll
    for (int i = 0; i < 4; ++i) { auto r = __builtin_amdgcn_permlane16_swap(X[i], Y[i], false, false); X[i] = r[0]; Y[i] = r[1]; }
    const int g = lane >> 4, col = ((g & 1) << 4) | ((g >> 1) << 3);
    bf16_t* p = base + (size_t)(lane & 15) * ld + col;
    st16w(p, X); st16w(p + (size_t)16 * ld, Y);
}

namespace p2 { DI void kv_dma(const Params& a, const unsigned lds0, const int h, const int qb, const int hw, const int lane, const int T); }
namespace p1 {
constexpr int A_STAGE = 32768, B_STAGE = 49152, B_BASE = 65536;
constexpr int NT = D_ / 64;
constexpr float QSCALE = 0.17677669529663687f * 1.4426950408889634f;
constexpr int PD = 3;

DI void mainloop(const Params& a, const unsigned lds0, const int w, const int rb, const int ct, f32x16 (&acc)[10], unsigned& tag, float& gval) {
    typedef unsigned u32x16 __attribute__((ext_vector_type(16)));
    typedef unsigned u32x8 __attribute__((ext_vector_type(8)));
    const int lane = lane_id();
    const int wr = w & 3, wc = w >> 2;
    const unsigned voffA = (unsigned)((lane >> 3) * 2048 + (((lane & 7) ^ (lane >> 3)) * 16));
    const int sB = (lane >> 4) | (((w >> 1) & 1) << 2);
    const int chq = (lane & 15) ^ (sB << 1);
    const int chg = chq >= 14 ? chq - 2 : chq;
    const unsigned long long qmask = __builtin_amdgcn_ballot_w64(chq < 12);
    const unsigned voffQ = (unsigned)((lane >> 4) * 6144 + (chq >> 2) * 2048 + (chq & 3) * 16), voffG = (unsigned)((lane >> 4) * 7168 + chg * 16);
    const char* gA = (const char*)a.x + (size_t)(rb * 256 + w * 32) * 2048;
    const char* gQ = (const char*)a.wqkv + (size_t)(w * 4) * 6144 + ct * 64;
    const char* gG = (const char*)a.wg + (size_t)(w * 4) * 7168 + ct * 224;
    const char* gU = (const char*)a.wu + (size_t)(w * 4) * 7168 + ct * 224;
    const unsigned dA = lds0 + w * 4096, dB = lds0 + B_BASE + w * 1024;
    auto glds16m = [&](unsigned voff, const void* sbase, unsigned lds_dst) {
        asm volatile("s_mov_b32 m0, %2\n\ts_mov_b64 exec, %3\n\tglobal_load_lds_dwordx4 %0, %1\n\ts_mov_b64 exec, -1" :: "v"(voff), "s"(sbase), "s"(lds_dst), "s"(qmask) : "memory"); };
#define P1_DMA(p, t, st) do { \
        if ((p) < 4) glds16(voffA, gA + (size_t)(t) * 128 + (p) * 8 * 2048, dA + (st) * A_STAGE + (p) * 1024); \
        else if ((p) == 4 || (p) == 7) glds16m(voffQ, gQ + (size_t)(t) * (64 * 6144) + ((p) == 7 ? 32 * 6144 : 0), dB + (st) * B_STAGE + ((p) == 7 ? 8192 : 0)); \
        else if ((p) == 5 || (p) == 8) glds16(voffG, gG + (size_t)(t) * (64 * 7168) + ((p) == 8 ? 32 * 7168 : 0), dB + (st) * B_STAGE + 16384 + ((p) == 8 ? 8192 : 0)); \
        else glds16(voffG, gU + (size_t)(t) * (64 * 7168) + ((p) == 9 ? 32 * 7168 : 0), dB + (st) * B_STAGE + 32768 + ((p) == 9 ? 8192 : 0)); } while (0)
#pragma unroll
    for (int pc = 0; pc < 7; ++pc) P1_DMA(pc, 0, 0);
    unsigned epoch;
    asm volatile("global_load_dword %0, %1, off sc1" : "=v"(epoch) : "v"(a.flags + FLAG_EPOCH) : "memory");
    u32x4 gx0, gx1, gg0, gg1;
    { const u32x4* xr = (const u32x4*)(a.x + (size_t)(rb * 256 + ct * 8 + w) * D_) + lane * 2; const u32x4* gr = (const u32x4*)(a.gw) + lane * 2;
      asm volatile("global_load_dwordx4 %0, %4, off\n\tglobal_load_dwordx4 %1, %4, off offset:16\n\tglobal_load_dwordx4 %2, %5, off\n\tglobal_load_dwordx4 %3, %5, off offset:16"
                   : "=&v"(gx0), "=&v"(gx1), "=&v"(gg0), "=&v"(gg1) : "v"(xr), "v"(gr) : "memory"); }
#pragma unroll
    for (int pc = 7; pc < 10; ++pc) P1_DMA(pc, 0, 0);
#pragma unroll
    for (int pc = 0; pc < 10; ++pc) P1_DMA(pc, 1, 1);
#undef P1_DMA
    {
        asm volatile("s_waitcnt vmcnt(13)" : "+v"(gx0), "+v"(gx1), "+v"(gg0), "+v"(gg1) :: "memory");
        float s = 0.f;
#pragma unroll
        for (int i = 0; i < 4; ++i) {
            s += __uint_as_float(gx0[i] << 16) * __uint_as_float(gg0[i] << 16) + __uint_as_float(gx0[i] & 0xffff0000u) * __uint_as_float(gg0[i] & 0xffff0000u);
            s += __uint_as_float(gx1[i] << 16) * __uint_as_float(gg1[i] << 16) + __uint_as_float(gx1[i] & 0xffff0000u) * __uint_as_float(gg1[i] & 0xffff0000u);
        }
        gval = s; asm volatile("" : "+v"(gval));
    }
    const unsigned flg = lds0 + B_BASE + 192;
    if (lane == 0) *(volatile LAS unsigned*)(size_t)(flg + w * 4) = 0u;
    const int fr = lane & 15, fq = lane >> 4;
    const int q = (lane & 15) >> 2, p = lane & 3, sR = q | ((fq & 1) << 2);
    const int bbase = (8 * fq + q) * 256 + 32 * sR + 8 * p;
    u32x16 v0; u32x16 v1;
    v0[0] = lds0 + (wr * 64 + fr) * 128 + ((fq ^ (fr & 7)) * 16);
    v0[1] = lds0 + (wr * 64 + fr) * 128 + (((fq + 4) ^ (fr & 7)) * 16);
#pragma unroll
    for (int jj = 0; jj < 4; ++jj) v0[2 + jj] = lds0 + B_BASE + (bbase ^ ((3 * wc + jj) << 5));
    v0[6] = wc ? v0[5] + 16384 : v0[2];
    v0[7] = wc ? v0[5] + 32768 : v0[5];
#pragma unroll
    for (int jj = 0; jj < 6; ++jj) v0[8 + jj] = v0[2 + jj] + B_STAGE;
    v0[14] = voffA; v0[15] = voffA + 8 * 2048;
    v1[0] = voffA + 16 * 2048; v1[1] = voffA + 24 * 2048; v1[2] = voffQ; v1[3] = voffQ + 32 * 6144; v1[4] = voffG; v1[5] = voffG + 32 * 7168;
    v1[6] = flg + w * 4; v1[7] = flg + (lane & 7) * 4;
#pragma unroll
    for (int i = 8; i < 16; ++i) v1[i] = 0u;
    u32x8 s0; u32x4 s1;
    { const uint64_t pa = (uint64_t)(gA + 128), pq = (uint64_t)(gQ + 64 * 6144), pg = (uint64_t)(gG + 64 * 7168), pu = (uint64_t)(gU + 64 * 7168);
      s0[0] = (unsigned)pa; s0[1] = (unsigned)(pa >> 32); s0[2] = (unsigned)pq; s0[3] = (unsigned)(pq >> 32); s0[4] = (unsigned)pg; s0[5] = (unsigned)(pg >> 32); s0[6] = (unsigned)pu; s0[7] = (unsigned)(pu >> 32);
      s1[0] = dA; s1[1] = dB; s1[2] = (unsigned)qmask; s1[3] = (unsigned)(qmask >> 32); }
    asm volatile("s_waitcnt vmcnt(13) lgkmcnt(0)\n\ts_barrier" : "+v"(epoch) :: "memory");
    tag = (unsigned)__builtin_amdgcn_readfirstlane(epoch) + 1u;

    asm volatile(
    "s_mov_b32 s52, 0\n"
    "ds_read_b128 v[160:163], v208 offset:0\n"
    "ds_read_b128 v[164:167], v208 offset:2048\n"
    "ds_read_b128 v[168:171], v208 offset:4096\n"
    "ds_read_b128 v[172:175], v208 offset:6144\n"
    "ds_read_b64_tr_b16 v[192:193], v210 offset:16384\n"
    "ds_read_b64_tr_b16 v[194:195], v210 offset:17408\n"
    "ds_read_b64_tr_b16 v[196:197], v210 offset:32768\n"
    "ds_read_b64_tr_b16 v[198:199], v210 offset:33792\n"
    "ds_read_b64_tr_b16 v[200:201], v211 offset:16384\n"
    "ds_read_b64_tr_b16 v[202:203], v211 offset:17408\n"
    "s_waitcnt lgkmcnt(4)\n"
    "v_mfma_f32_16x16x32_bf16 v[0:3], v[192:195], v[160:163], 0\n"
    "ds_read_b64_tr_b16 v[204:205], v211 offset:32768\n"
    "ds_read_b64_tr_b16 v[206:207], v211 offset:33792\n"
    "v_mfma_f32_16x16x32_bf16 v[4:7], v[192:195], v[164:167], 0\n"
    "ds_read_b128 v[176:179], v209 offset:0\n"
    "v_mfma_f32_16x16x32_bf16 v[8:11], v[192:195], v[168:171], 0\n"
    "v_mfma_f32_16x16x32_bf16 v[12:15], v[192:195], v[172:175], 0\n"
    "s_waitcnt lgkmcnt(5)\n"
    "s_waitcnt vmcnt(10)\n"
    "s_add_u32 s52, s52, 1\n"
    "v_mov_b32 v233, s52\n"
    "s_mov_b32 s54, s52\n"
    "s_mov_b64 exec, 1\n"
    "ds_write_b32 v230, v233\n"
    "s_mov_b64 exec, -1\n"
    "v_mfma_f32_16x16x32_bf16 v[16:19], v[196:199], v[160:163], 0\n"
    "ds_read_b64_tr_b16 v[192:193], v212 offset:16384\n"
    "ds_read_b64_tr_b16 v[194:195], v212 offset:17408\n"
    "v_mfma_f32_16x16x32_bf16 v[20:23], v[196:199], v[164:167], 0\n"
    "v_mfma_f32_16x16x32_bf16 v[24:27], v[196:199], v[168:171], 0\n"
    "v_mfma_f32_16x16x32_bf16 v[28:31], v[196:199], v[172:175], 0\n"
    "s_waitcnt lgkmcnt(6)\n"
    "v_mfma_f32_16x16x32_bf16 v[32:35], v[200:203], v[160:163], 0\n"
    "ds_read_b64_tr_b16 v[196:197], v212 offset:32768\n"
    "ds_read_b64_tr_b16 v[198:199], v212 offset:33792\n"
    "v_mfma_f32_16x16x32_bf16 v[36:39], v[200:203], v[164:167], 0\n"
    "ds_read_b128 v[180:183], v209 offset:2048\n"
    "v_mfma_f32_16x16x32_bf16 v[40:43], v[200:203], v[168:171], 0\n"
    "v_mfma_f32_16x16x32_bf16 v[44:47], v[200:203], v[172:175], 0\n"
    "s_waitcnt lgkmcnt(7)\n"
    "v_mfma_f32_16x16x32_bf16 v[48:51], v[204:207], v[160:163], 0\n"
    "ds_read_b32 v232, v231\n"
    "v_mfma_f32_16x16x32_bf16 v[52:55], v[204:207], v[164:167], 0\n"
    "ds_read_b64_tr_b16 v[200:201], v211 offset:0\n"
    "ds_read_b64_tr_b16 v[202:203], v211 offset:1024\n"
    "v_mfma_f32_16x16x32_bf16 v[56:59], v[204:207], v[168:171], 0\n"
    "v_mfma_f32_16x16x32_bf16 v[60:63], v[204:207], v[172:175], 0\n"
    "s_waitcnt lgkmcnt(6)\n"
    "s_waitcnt lgkmcnt(2)\n"
    "v_cmp_gt_u32 vcc, s54, v232\n"
    "s_cbranch_vccz 103f\n"
    "102:\n"
    "s_sleep 1\n"
    "ds_read_b32 v232, v231\n"
    "s_waitcnt lgkmcnt(0)\n"
    "v_cmp_gt_u32 vcc, s54, v232\n"
    "s_cbranch_vccnz 102b\n"
    "103:\n"
    "v_mfma_f32_16x16x32_bf16 v[64:67], v[192:195], v[160:163], 0\n"
    "ds_read_b64_tr_b16 v[204:205], v212 offset:0\n"
    "ds_read_b64_tr_b16 v[206:207], v212 offset:1024\n"
    "v_mfma_f32_16x16x32_bf16 v[68:71], v[192:195], v[164:167], 0\n"
    "ds_read_b128 v[184:187], v209 offset:4096\n"
    "v_mfma_f32_16x16x32_bf16 v[72:75], v[192:195], v[168:171], 0\n"
    "v_mfma_f32_16x16x32_bf16 v[76:79], v[192:195], v[172:175], 0\n"
    "v_mfma_f32_16x16x32_bf16 v[80:83], v[196:199], v[160:163], 0\n"
    "ds_read_b64_tr_b16 v[192:193], v214 offset:0\n"
    "ds_read_b64_tr_b16 v[194:195], v214 offset:1024\n"
    "v_mfma_f32_16x16x32_bf16 v[84:87], v[196:199], v[164:167], 0\n"
    "v_mfma_f32_16x16x32_bf16 v[88:91], v[196:199], v[168:171], 0\n"
    "v_mfma_f32_16x16x32_bf16 v[92:95], v[196:199], v[172:175], 0\n"
    "s_waitcnt lgkmcnt(5)\n"
    "v_mfma_f32_16x16x32_bf16 v[96:99], v[200:203], v[160:163], 0\n"
    "ds_read_b64_tr_b16 v[196:197], v215 offset:0\n"
    "ds_read_b64_tr_b16 v[198:199], v215 offset:1024\n"
    "v_mfma_f32_16x16x32_bf16 v[100:103], v[200:203], v[164:167], 0\n"
    "ds_read_b128 v[188:191], v209 offset:6144\n"
    "v_mfma_f32_16x16x32_bf16 v[104:107], v[200:203], v[168:171], 0\n"
    "v_mfma_f32_16x16x32_bf16 v[108:111], v[200:203], v[172:175], 0\n"
    "s_waitcnt lgkmcnt(6)\n"
    "v_mfma_f32_16x16x32_bf16 v[112:115], v[204:207], v[160:163], 0\n"
    "ds_read_b64_tr_b16 v[200:201], v210 offset:24576\n"
    "ds_read_b64_tr_b16 v[202:203], v210 offset:25600\n"
    "v_mfma_f32_16x16x32_bf16 v[116:119], v[204:207], v[164:167], 0\n"
    "v_mfma_f32_16x16x32_bf16 v[120:123], v[204:207], v[168:171], 0\n"
    "v_mfma_f32_16x16x32_bf16 v[124:127], v[204:207], v[172:175], 0\n"
    "s_waitcnt lgkmcnt(5)\n"
    "v_mfma_f32_16x16x32_bf16 v[128:131], v[192:195], v[160:163], 0\n"
    "ds_read_b64_tr_b16 v[204:205], v210 offset:40960\n"
    "ds_read_b64_tr_b16 v[206:207], v210 offset:41984\n"
    "v_mfma_f32_16x16x32_bf16 v[132:135], v[192:195], v[164:167], 0\n"
    "s_add_u32 s40, s40, 128\n"
    "s_addc_u32 s41, s41, 0\n"
    "s_add_u32 s42, s42, 393216\n"
    "s_addc_u32 s43, s43, 0\n"
    "s_add_u32 s44, s44, 458752\n"
    "s_addc_u32 s45, s45, 0\n"
    "s_add_u32 s46, s46, 458752\n"
    "s_addc_u32 s47, s47, 0\n"
    "v_mfma_f32_16x16x32_bf16 v[136:139], v[192:195], v[168:171], 0\n"
    "v_mfma_f32_16x16x32_bf16 v[140:143], v[192:195], v[172:175], 0\n"
    "s_waitcnt lgkmcnt(5)\n"
    "s_waitcnt lgkmcnt(4)\n"
    "s_add_u32 s52, s52, 1\n"
    "v_mov_b32 v233, s52\n"
    "s_mov_b32 s54, s52\n"
    "s_mov_b64 exec, 1\n"
    "ds_write_b32 v230, v233\n"
    "s_mov_b64 exec, -1\n"
    "v_mfma_f32_16x16x32_bf16 v[144:147], v[196:199], v[160:163], 0\n"
    "ds_read_b64_tr_b16 v[192:193], v211 offset:24576\n"
    "ds_read_b64_tr_b16 v[194:195], v211 offset:25600\n"
    "v_mfma_f32_16x16x32_bf16 v[148:151], v[196:199], v[164:167], 0\n"
    "v_mfma_f32_16x16x32_bf16 v[152:155], v[196:199], v[168:171], 0\n"
    "v_mfma_f32_16x16x32_bf16 v[156:159], v[196:199], v[172:175], 0\n"
    "s_waitcnt lgkmcnt(5)\n"
    "v_mfma_f32_16x16x32_bf16 v[0:3], v[200:203], v[176:179], v[0:3]\n"
    "ds_read_b32 v232, v231\n"
    "v_mfma_f32_16x16x32_bf16 v[4:7], v[200:203], v[180:183], v[4:7]\n"
    "ds_read_b64_tr_b16 v[196:197], v211 offset:40960\n"
    "ds_read_b64_tr_b16 v[198:199], v211 offset:41984\n"
    "v_mfma_f32_16x16x32_bf16 v[8:11], v[200:203], v[184:187], v[8:11]\n"
    "v_mfma_f32_16x16x32_bf16 v[12:15], v[200:203], v[188:191], v[12:15]\n"
    "s_waitcnt lgkmcnt(6)\n"
    "s_waitcnt lgkmcnt(2)\n"
    "v_cmp_gt_u32 vcc, s54, v232\n"
    "s_cbranch_vccz 105f\n"
    "104:\n"
    "s_sleep 1\n"
    "ds_read_b32 v232, v231\n"
    "s_waitcnt lgkmcnt(0)\n"
    "v_cmp_gt_u32 vcc, s54, v232\n"
    "s_cbranch_vccnz 104b\n"
    "105:\n"
    "v_mfma_f32_16x16x32_bf16 v[16:19], v[204:207], v[176:179], v[16:19]\n"
    "s_add_u32 m0, s48, 0\n"
    "ds_read_b64_tr_b16 v[200:201], v212 offset:24576\n"
    "ds_read_b64_tr_b16 v[202:203], v212 offset:25600\n"
    "v_mfma_f32_16x16x32_bf16 v[20:23], v[204:207], v[180:183], v[20:23]\n"
    "global_load_lds_dwordx4 v222, s[40:41]\n"
    "v_mfma_f32_16x16x32_bf16 v[24:27], v[204:207], v[184:187], v[24:27]\n"
    "v_mfma_f32_16x16x32_bf16 v[28:31], v[204:207], v[188:191], v[28:31]\n"
    "v_mfma_f32_16x16x32_bf16 v[32:35], v[192:195], v[176:179], v[32:35]\n"
    "s_add_u32 m0, s48, 1024\n"
    "ds_read_b64_tr_b16 v[204:205], v212 offset:40960\n"
    "ds_read_b64_tr_b16 v[206:207], v212 offset:41984\n"
    "v_mfma_f32_16x16x32_bf16 v[36:39], v[192:195], v[180:183], v[36:39]\n"
    "global_load_lds_dwordx4 v223, s[40:41]\n"
    "v_mfma_f32_16x16x32_bf16 v[40:43], v[192:195], v[184:187], v[40:43]\n"
    "v_mfma_f32_16x16x32_bf16 v[44:47], v[192:195], v[188:191], v[44:47]\n"
    "s_waitcnt lgkmcnt(4)\n"
    "v_mfma_f32_16x16x32_bf16 v[48:51], v[196:199], v[176:179], v[48:51]\n"
    "s_add_u32 m0, s48, 2048\n"
    "ds_read_b64_tr_b16 v[192:193], v211 offset:8192\n"
    "ds_read_b64_tr_b16 v[194:195], v211 offset:9216\n"
    "v_mfma_f32_16x16x32_bf16 v[52:55], v[196:199], v[180:183], v[52:55]\n"
    "global_load_lds_dwordx4 v224, s[40:41]\n"
    "v_mfma_f32_16x16x32_bf16 v[56:59], v[196:199], v[184:187], v[56:59]\n"
    "v_mfma_f32_16x16x32_bf16 v[60:63], v[196:199], v[188:191], v[60:63]\n"
    "s_waitcnt lgkmcnt(4)\n"
    "s_waitcnt vmcnt(6)\n"
    "s_add_u32 s52, s52, 1\n"
    "v_mov_b32 v233, s52\n"
    "s_mov_b32 s54, s52\n"
    "s_mov_b64 exec, 1\n"
    "ds_write_b32 v230, v233\n"
    "s_mov_b64 exec, -1\n"
    "v_mfma_f32_16x16x32_bf16 v[64:67], v[200:203], v[176:179], v[64:67]\n"
    "ds_read_b64_tr_b16 v[196:197], v212 offset:8192\n"
    "ds_read_b64_tr_b16 v[198:199], v212 offset:9216\n"
    "v_mfma_f32_16x16x32_bf16 v[68:71], v[200:203], v[180:183], v[68:71]\n"
    "v_mfma_f32_16x16x32_bf16 v[72:75], v[200:203], v[184:187], v[72:75]\n"
    "v_mfma_f32_16x16x32_bf16 v[76:79], v[200:203], v[188:191], v[76:79]\n"
    "s_waitcnt lgkmcnt(5)\n"
    "v_mfma_f32_16x16x32_bf16 v[80:83], v[204:207], v[176:179], v[80:83]\n"
    "s_add_u32 m0, s48, 3072\n"
    "ds_read_b64_tr_b16 v[200:201], v214 offset:8192\n"
    "ds_read_b64_tr_b16 v[202:203], v214 offset:9216\n"
    "v_mfma_f32_16x16x32_bf16 v[84:87], v[204:207], v[180:183], v[84:87]\n"
    "global_load_lds_dwordx4 v225, s[40:41]\n"
    "v_mfma_f32_16x16x32_bf16 v[88:91], v[204:207], v[184:187], v[88:91]\n"
    "v_mfma_f32_16x16x32_bf16 v[92:95], v[204:207], v[188:191], v[92:95]\n"
    "s_waitcnt lgkmcnt(5)\n"
    "v_mfma_f32_16x16x32_bf16 v[96:99], v[192:195], v[176:179], v[96:99]\n"
    "s_add_u32 m0, s49, 0\n"
    "ds_read_b32 v232, v231\n"
    "v_mfma_f32_16x16x32_bf16 v[100:103], v[192:195], v[180:183], v[100:103]\n"
    "s_mov_b64 exec, s[50:51]\n"
    "global_load_lds_dwordx4 v226, s[42:43]\n"
    "s_mov_b64 exec, -1\n"
    "ds_read_b64_tr_b16 v[204:205], v215 offset:8192\n"
    "ds_read_b64_tr_b16 v[206:207], v215 offset:9216\n"
    "v_mfma_f32_16x16x32_bf16 v[104:107], v[192:195], v[184:187], v[104:107]\n"
    "v_mfma_f32_16x16x32_bf16 v[108:111], v[192:195], v[188:191], v[108:111]\n"
    "s_waitcnt lgkmcnt(5)\n"
    "s_waitcnt lgkmcnt(2)\n"
    "v_cmp_gt_u32 vcc, s54, v232\n"
    "s_cbranch_vccz 107f\n"
    "106:\n"
    "s_sleep 1\n"
    "ds_read_b32 v232, v231\n"
    "s_waitcnt lgkmcnt(0)\n"
    "v_cmp_gt_u32 vcc, s54, v232\n"
    "s_cbranch_vccnz 106b\n"
    "107:\n"
    "v_mfma_f32_16x16x32_bf16 v[112:115], v[196:199], v[176:179], v[112:115]\n"
    "ds_read_b64_tr_b16 v[192:193], v216 offset:16384\n"
    "ds_read_b64_tr_b16 v[194:195], v216 offset:17408\n"
    "v_mfma_f32_16x16x32_bf16 v[116:119], v[196:199], v[180:183], v[116:119]\n"
    "ds_read_b128 v[160:163], v208 offset:32768\n"
    "ds_read_b128 v[164:167], v208 offset:34816\n"
    "v_mfma_f32_16x16x32_bf16 v[120:123], v[196:199], v[184:187], v[120:123]\n"
    "v_mfma_f32_16x16x32_bf16 v[124:127], v[196:199], v[188:191], v[124:127]\n"
    "v_mfma_f32_16x16x32_bf16 v[128:131], v[200:203], v[176:179], v[128:131]\n"
    "s_add_u32 m0, s49, 16384\n"
    "ds_read_b64_tr_b16 v[196:197], v216 offset:32768\n"
    "ds_read_b64_tr_b16 v[198:199], v216 offset:33792\n"
    "v_mfma_f32_16x16x32_bf16 v[132:135], v[200:203], v[180:183], v[132:135]\n"
    "global_load_lds_dwordx4 v228, s[44:45]\n"
    "ds_read_b128 v[168:171], v208 offset:36864\n"
    "v_mfma_f32_16x16x32_bf16 v[136:139], v[200:203], v[184:187], v[136:139]\n"
    "v_mfma_f32_16x16x32_bf16 v[140:143], v[200:203], v[188:191], v[140:143]\n"
    "s_waitcnt lgkmcnt(7)\n"
    "v_mfma_f32_16x16x32_bf16 v[144:147], v[204:207], v[176:179], v[144:147]\n"
    "s_add_u32 m0, s49, 32768\n"
    "ds_read_b64_tr_b16 v[200:201], v217 offset:16384\n"
    "ds_read_b64_tr_b16 v[202:203], v217 offset:17408\n"
    "v_mfma_f32_16x16x32_bf16 v[148:151], v[204:207], v[180:183], v[148:151]\n"
    "global_load_lds_dwordx4 v228, s[46:47]\n"
    "ds_read_b128 v[172:175], v208 offset:38912\n"
    "v_mfma_f32_16x16x32_bf16 v[152:155], v[204:207], v[184:187], v[152:155]\n"
    "v_mfma_f32_16x16x32_bf16 v[156:159], v[204:207], v[188:191], v[156:159]\n"
    "s_mov_b32 s53, 0\n"
    "9:\n"
    "s_waitcnt lgkmcnt(0)\n"
    "v_mfma_f32_16x16x32_bf16 v[0:3], v[192:195], v[160:163], v[0:3]\n"
    "ds_read_b64_tr_b16 v[204:205], v217 offset:32768\n"
    "ds_read_b64_tr_b16 v[206:207], v217 offset:33792\n"
    "v_mfma_f32_16x16x32_bf16 v[4:7], v[192:195], v[164:167], v[4:7]\n"
    "ds_read_b128 v[176:179], v209 offset:32768\n"
    "v_mfma_f32_16x16x32_bf16 v[8:11], v[192:195], v[168:171], v[8:11]\n"
    "v_mfma_f32_16x16x32_bf16 v[12:15], v[192:195], v[172:175], v[12:15]\n"
    "s_waitcnt vmcnt(7)\n"
    "s_add_u32 s52, s52, 1\n"
    "v_mov_b32 v233, s52\n"
    "s_mov_b32 s54, s52\n"
    "s_mov_b64 exec, 1\n"
    "ds_write_b32 v230, v233\n"
    "s_mov_b64 exec, -1\n"
    "v_mfma_f32_16x16x32_bf16 v[16:19], v[196:199], v[160:163], v[16:19]\n"
    "ds_read_b64_tr_b16 v[192:193], v218 offset:16384\n"
    "ds_read_b64_tr_b16 v[194:195], v218 offset:17408\n"
    "v_mfma_f32_16x16x32_bf16 v[20:23], v[196:199], v[164:167], v[20:23]\n"
    "v_mfma_f32_16x16x32_bf16 v[24:27], v[196:199], v[168:171], v[24:27]\n"
    "v_mfma_f32_16x16x32_bf16 v[28:31], v[196:199], v[172:175], v[28:31]\n"
    "v_mfma_f32_16x16x32_bf16 v[32:35], v[200:203], v[160:163], v[32:35]\n"
    "ds_read_b64_tr_b16 v[196:197], v218 offset:32768\n"
    "ds_read_b64_tr_b16 v[198:199], v218 offset:33792\n"
    "v_mfma_f32_16x16x32_bf16 v[36:39], v[200:203], v[164:167], v[36:39]\n"
    "ds_read_b128 v[180:183], v209 offset:34816\n"
    "v_mfma_f32_16x16x32_bf16 v[40:43], v[200:203], v[168:171], v[40:43]\n"
    "v_mfma_f32_16x16x32_bf16 v[44:47], v[200:203], v[172:175], v[44:47]\n"
    "s_waitcnt lgkmcnt(7)\n"
    "v_mfma_f32_16x16x32_bf16 v[48:51], v[204:207], v[160:163], v[48:51]\n"
    "ds_read_b32 v232, v231\n"
    "v_mfma_f32_16x16x32_bf16 v[52:55], v[204:207], v[164:167], v[52:55]\n"
    "ds_read_b64_tr_b16 v[200:201], v217 offset:0\n"
    "ds_read_b64_tr_b16 v[202:203], v217 offset:1024\n"
    "v_mfma_f32_16x16x32_bf16 v[56:59], v[204:207], v[168:171], v[56:59]\n"
    "v_mfma_f32_16x16x32_bf16 v[60:63], v[204:207], v[172:175], v[60:63]\n"
    "s_waitcnt lgkmcnt(6)\n"
    "s_waitcnt lgkmcnt(2)\n"
    "v_cmp_gt_u32 vcc, s54, v232\n"
    "s_cbranch_vccz 109f\n"
    "108:\n"
    "s_sleep 1\n"
    "ds_read_b32 v232, v231\n"
    "s_waitcnt lgkmcnt(0)\n"
    "v_cmp_gt_u32 vcc, s54, v232\n"
    "s_cbranch_vccnz 108b\n"
    "109:\n"
    "v_mfma_f32_16x16x32_bf16 v[64:67], v[192:195], v[160:163], v[64:67]\n"
    "s_add_u32 m0, s49, 8192\n"
    "ds_read_b64_tr_b16 v[204:205], v218 offset:0\n"
    "ds_read_b64_tr_b16 v[206:207], v218 offset:1024\n"
    "v_mfma_f32_16x16x32_bf16 v[68:71], v[192:195], v[164:167], v[68:71]\n"
    "s_mov_b64 exec, s[50:51]\n"
    "global_load_lds_dwordx4 v227, s[42:43]\n"
    "s_mov_b64 exec, -1\n"
    "ds_read_b128 v[184:187], v209 offset:36864\n"
    "v_mfma_f32_16x16x32_bf16 v[72:75], v[192:195], v[168:171], v[72:75]\n"
    "v_mfma_f32_16x16x32_bf16 v[76:79], v[192:195], v[172:175], v[76:79]\n"
    "v_mfma_f32_16x16x32_bf16 v[80:83], v[196:199], v[160:163], v[80:83]\n"
    "s_add_u32 m0, s49, 24576\n"
    "ds_read_b64_tr_b16 v[192:193], v220 offset:0\n"
    "ds_read_b64_tr_b16 v[194:195], v220 offset:1024\n"
    "v_mfma_f32_16x16x32_bf16 v[84:87], v[196:199], v[164:167], v[84:87]\n"
    "global_load_lds_dwordx4 v229, s[44:45]\n"
    "v_mfma_f32_16x16x32_bf16 v[88:91], v[196:199], v[168:171], v[88:91]\n"
    "v_mfma_f32_16x16x32_bf16 v[92:95], v[196:199], v[172:175], v[92:95]\n"
    "s_waitcnt lgkmcnt(5)\n"
    "v_mfma_f32_16x16x32_bf16 v[96:99], v[200:203], v[160:163], v[96:99]\n"
    "s_add_u32 m0, s49, 40960\n"
    "ds_read_b64_tr_b16 v[196:197], v221 offset:0\n"
    "ds_read_b64_tr_b16 v[198:199], v221 offset:1024\n"
    "v_mfma_f32_16x16x32_bf16 v[100:103], v[200:203], v[164:167], v[100:103]\n"
    "global_load_lds_dwordx4 v229, s[46:47]\n"
    "ds_read_b128 v[188:191], v209 offset:38912\n"
    "v_mfma_f32_16x16x32_bf16 v[104:107], v[200:203], v[168:171], v[104:107]\n"
    "v_mfma_f32_16x16x32_bf16 v[108:111], v[200:203], v[172:175], v[108:111]\n"
    "s_waitcnt lgkmcnt(6)\n"
    "v_mfma_f32_16x16x32_bf16 v[112:115], v[204:207], v[160:163], v[112:115]\n"
    "ds_read_b64_tr_b16 v[200:201], v216 offset:24576\n"
    "ds_read_b64_tr_b16 v[202:203], v216 offset:25600\n"
    "v_mfma_f32_16x16x32_bf16 v[116:119], v[204:207], v[164:167], v[116:119]\n"
    "v_mfma_f32_16x16x32_bf16 v[120:123], v[204:207], v[168:171], v[120:123]\n"
    "v_mfma_f32_16x16x32_bf16 v[124:127], v[204:207], v[172:175], v[124:127]\n"
    "s_waitcnt lgkmcnt(5)\n"
    "v_mfma_f32_16x16x32_bf16 v[128:131], v[192:195], v[160:163], v[128:131]\n"
    "ds_read_b64_tr_b16 v[204:205], v216 offset:40960\n"
    "ds_read_b64_tr_b16 v[206:207], v216 offset:41984\n"
    "v_mfma_f32_16x16x32_bf16 v[132:135], v[192:195], v[164:167], v[132:135]\n"
    "s_add_u32 s40, s40, 128\n"
    "s_addc_u32 s41, s41, 0\n"
    "s_add_u32 s42, s42, 393216\n"
    "s_addc_u32 s43, s43, 0\n"
    "s_add_u32 s44, s44, 458752\n"
    "s_addc_u32 s45, s45, 0\n"
    "s_add_u32 s46, s46, 458752\n"
    "s_addc_u32 s47, s47, 0\n"
    "v_mfma_f32_16x16x32_bf16 v[136:139], v[192:195], v[168:171], v[136:139]\n"
    "v_mfma_f32_16x16x32_bf16 v[140:143], v[192:195], v[172:175], v[140:143]\n"
    "s_waitcnt lgkmcnt(5)\n"
    "s_waitcnt lgkmcnt(4)\n"
    "s_add_u32 s52, s52, 1\n"
    "v_mov_b32 v233, s52\n"
    "s_mov_b32 s54, s52\n"
    "s_mov_b64 exec, 1\n"
    "ds_write_b32 v230, v233\n"
    "s_mov_b64 exec, -1\n"
    "v_mfma_f32_16x16x32_bf16 v[144:147], v[196:199], v[160:163], v[144:147]\n"
    "ds_read_b64_tr_b16 v[192:193], v217 offset:24576\n"
    "ds_read_b64_tr_b16 v[194:195], v217 offset:25600\n"
    "v_mfma_f32_16x16x32_bf16 v[148:151], v[196:199], v[164:167], v[148:151]\n"
    "v_mfma_f32_16x16x32_bf16 v[152:155], v[196:199], v[168:171], v[152:155]\n"
    "v_mfma_f32_16x16x32_bf16 v[156:159], v[196:199], v[172:175], v[156:159]\n"
    "s_waitcnt lgkmcnt(5)\n"
    "v_mfma_f32_16x16x32_bf16 v[0:3], v[200:203], v[176:179], v[0:3]\n"
    "ds_read_b32 v232, v231\n"
    "v_mfma_f32_16x16x32_bf16 v[4:7], v[200:203], v[180:183], v[4:7]\n"
    "ds_read_b64_tr_b16 v[196:197], v217 offset:40960\n"
    "ds_read_b64_tr_b16 v[198:199], v217 offset:41984\n"
    "v_mfma_f32_16x16x32_bf16 v[8:11], v[200:203], v[184:187], v[8:11]\n"
    "v_mfma_f32_16x16x32_bf16 v[12:15], v[200:203], v[188:191], v[12:15]\n"
    "s_waitcnt lgkmcnt(6)\n"
    "s_waitcnt lgkmcnt(2)\n"
    "v_cmp_gt_u32 vcc, s54, v232\n"
    "s_cbranch_vccz 111f\n"
    "110:\n"
    "s_sleep 1\n"
    "ds_read_b32 v232, v231\n"
    "s_waitcnt lgkmcnt(0)\n"
    "v_cmp_gt_u32 vcc, s54, v232\n"
    "s_cbranch_vccnz 110b\n"
    "111:\n"
    "v_mfma_f32_16x16x32_bf16 v[16:19], v[204:207], v[176:179], v[16:19]\n"
    "s_add_u32 m0, s48, 32768\n"
    "ds_read_b64_tr_b16 v[200:201], v218 offset:24576\n"
    "ds_read_b64_tr_b16 v[202:203], v218 offset:25600\n"
    "v_mfma_f32_16x16x32_bf16 v[20:23], v[204:207], v[180:183], v[20:23]\n"
    "global_load_lds_dwordx4 v222, s[40:41]\n"
    "v_mfma_f32_16x16x32_bf16 v[24:27], v[204:207], v[184:187], v[24:27]\n"
    "v_mfma_f32_16x16x32_bf16 v[28:31], v[204:207], v[188:191], v[28:31]\n"
    "v_mfma_f32_16x16x32_bf16 v[32:35], v[192:195], v[176:179], v[32:35]\n"
    "s_add_u32 m0, s48, 33792\n"
    "ds_read_b64_tr_b16 v[204:205], v218 offset:40960\n"
    "ds_read_b64_tr_b16 v[206:207], v218 offset:41984\n"
    "v_mfma_f32_16x16x32_bf16 v[36:39], v[192:195], v[180:183], v[36:39]\n"
    "global_load_lds_dwordx4 v223, s[40:41]\n"
    "v_mfma_f32_16x16x32_bf16 v[40:43], v[192:195], v[184:187], v[40:43]\n"
    "v_mfma_f32_16x16x32_bf16 v[44:47], v[192:195], v[188:191], v[44:47]\n"
    "s_waitcnt lgkmcnt(4)\n"
    "v_mfma_f32_16x16x32_bf16 v[48:51], v[196:199], v[176:179], v[48:51]\n"
    "s_add_u32 m0, s48, 34816\n"
    "ds_read_b64_tr_b16 v[192:193], v217 offset:8192\n"
    "ds_read_b64_tr_b16 v[194:195], v217 offset:9216\n"
    "v_mfma_f32_16x16x32_bf16 v[52:55], v[196:199], v[180:183], v[52:55]\n"
    "global_load_lds_dwordx4 v224, s[40:41]\n"
    "v_mfma_f32_16x16x32_bf16 v[56:59], v[196:199], v[184:187], v[56:59]\n"
    "v_mfma_f32_16x16x32_bf16 v[60:63], v[196:199], v[188:191], v[60:63]\n"
    "s_waitcnt lgkmcnt(4)\n"
    "s_waitcnt vmcnt(6)\n"
    "s_add_u32 s52, s52, 1\n"
    "v_mov_b32 v233, s52\n"
    "s_mov_b32 s54, s52\n"
    "s_mov_b64 exec, 1\n"
    "ds_write_b32 v230, v233\n"
    "s_mov_b64 exec, -1\n"
    "v_mfma_f32_16x16x32_bf16 v[64:67], v[200:203], v[176:179], v[64:67]\n"
    "ds_read_b64_tr_b16 v[196:197], v218 offset:8192\n"
    "ds_read_b64_tr_b16 v[198:199], v218 offset:9216\n"
    "v_mfma_f32_16x16x32_bf16 v[68:71], v[200:203], v[180:183], v[68:71]\n"
    "v_mfma_f32_16x16x32_bf16 v[72:75], v[200:203], v[184:187], v[72:75]\n"
    "v_mfma_f32_16x16x32_bf16 v[76:79], v[200:203], v[188:191], v[76:79]\n"
    "s_waitcnt lgkmcnt(5)\n"
    "v_mfma_f32_16x16x32_bf16 v[80:83], v[204:207], v[176:179], v[80:83]\n"
    "s_add_u32 m0, s48, 35840\n"
    "ds_read_b64_tr_b16 v[200:201], v220 offset:8192\n"
    "ds_read_b64_tr_b16 v[202:203], v220 offset:9216\n"
    "v_mfma_f32_16x16x32_bf16 v[84:87], v[204:207], v[180:183], v[84:87]\n"
    "global_load_lds_dwordx4 v225, s[40:41]\n"
    "v_mfma_f32_16x16x32_bf16 v[88:91], v[204:207], v[184:187], v[88:91]\n"
    "v_mfma_f32_16x16x32_bf16 v[92:95], v[204:207], v[188:191], v[92:95]\n"
    "s_waitcnt lgkmcnt(5)\n"
    "v_mfma_f32_16x16x32_bf16 v[96:99], v[192:195], v[176:179], v[96:99]\n"
    "s_add_u32 m0, s49, 49152\n"
    "ds_read_b32 v232, v231\n"
    "v_mfma_f32_16x16x32_bf16 v[100:103], v[192:195], v[180:183], v[100:103]\n"
    "s_mov_b64 exec, s[50:51]\n"
    "global_load_lds_dwordx4 v226, s[42:43]\n"
    "s_mov_b64 exec, -1\n"
    "ds_read_b64_tr_b16 v[204:205], v221 offset:8192\n"
    "ds_read_b64_tr_b16 v[206:207], v221 offset:9216\n"
    "v_mfma_f32_16x16x32_bf16 v[104:107], v[192:195], v[184:187], v[104:107]\n"
    "v_mfma_f32_16x16x32_bf16 v[108:111], v[192:195], v[188:191], v[108:111]\n"
    "s_waitcnt lgkmcnt(5)\n"
    "s_waitcnt lgkmcnt(2)\n"
    "v_cmp_gt_u32 vcc, s54, v232\n"
    "s_cbranch_vccz 113f\n"
    "112:\n"
    "s_sleep 1\n"
    "ds_read_b32 v232, v231\n"
    "s_waitcnt lgkmcnt(0)\n"
    "v_cmp_gt_u32 vcc, s54, v232\n"
    "s_cbranch_vccnz 112b\n"
    "113:\n"
    "v_mfma_f32_16x16x32_bf16 v[112:115], v[196:199], v[176:179], v[112:115]\n"
    "ds_read_b64_tr_b16 v[192:193], v210 offset:16384\n"
    "ds_read_b64_tr_b16 v[194:195], v210 offset:17408\n"
    "v_mfma_f32_16x16x32_bf16 v[116:119], v[196:199], v[180:183], v[116:119]\n"
    "ds_read_b128 v[160:163], v208 offset:0\n"
    "ds_read_b128 v[164:167], v208 offset:2048\n"
    "v_mfma_f32_16x16x32_bf16 v[120:123], v[196:199], v[184:187], v[120:123]\n"
    "v_mfma_f32_16x16x32_bf16 v[124:127], v[196:199], v[188:191], v[124:127]\n"
    "v_mfma_f32_16x16x32_bf16 v[128:131], v[200:203], v[176:179], v[128:131]\n"
    "s_add_u32 m0, s49, 65536\n"
    "ds_read_b64_tr_b16 v[196:197], v210 offset:32768\n"
    "ds_read_b64_tr_b16 v[198:199], v210 offset:33792\n"
    "v_mfma_f32_16x16x32_bf16 v[132:135], v[200:203], v[180:183], v[132:135]\n"
    "global_load_lds_dwordx4 v228, s[44:45]\n"
    "ds_read_b128 v[168:171], v208 offset:4096\n"
    "v_mfma_f32_16x16x32_bf16 v[136:139], v[200:203], v[184:187], v[136:139]\n"
    "v_mfma_f32_16x16x32_bf16 v[140:143], v[200:203], v[188:191], v[140:143]\n"
    "s_waitcnt lgkmcnt(7)\n"
    "v_mfma_f32_16x16x32_bf16 v[144:147], v[204:207], v[176:179], v[144:147]\n"
    "s_add_u32 m0, s49, 81920\n"
    "ds_read_b64_tr_b16 v[200:201], v211 offset:16384\n"
    "ds_read_b64_tr_b16 v[202:203], v211 offset:17408\n"
    "v_mfma_f32_16x16x32_bf16 v[148:151], v[204:207], v[180:183], v[148:151]\n"
    "global_load_lds_dwordx4 v228, s[46:47]\n"
    "ds_read_b128 v[172:175], v208 offset:6144\n"
    "v_mfma_f32_16x16x32_bf16 v[152:155], v[204:207], v[184:187], v[152:155]\n"
    "v_mfma_f32_16x16x32_bf16 v[156:159], v[204:207], v[188:191], v[156:159]\n"
    "s_waitcnt lgkmcnt(0)\n"
    "v_mfma_f32_16x16x32_bf16 v[0:3], v[192:195], v[160:163], v[0:3]\n"
    "ds_read_b64_tr_b16 v[204:205], v211 offset:32768\n"
    "ds_read_b64_tr_b16 v[206:207], v211 offset:33792\n"
    "v_mfma_f32_16x16x32_bf16 v[4:7], v[192:195], v[164:167], v[4:7]\n"
    "ds_read_b128 v[176:179], v209 offset:0\n"
    "v_mfma_f32_16x16x32_bf16 v[8:11], v[192:195], v[168:171], v[8:11]\n"
    "v_mfma_f32_16x16x32_bf16 v[12:15], v[192:195], v[172:175], v[12:15]\n"
    "s_waitcnt vmcnt(7)\n"
    "s_add_u32 s52, s52, 1\n"
    "v_mov_b32 v233, s52\n"
    "s_mov_b32 s54, s52\n"
    "s_mov_b64 exec, 1\n"
    "ds_write_b32 v230, v233\n"
    "s_mov_b64 exec, -1\n"
    "v_mfma_f32_16x16x32_bf16 v[16:19], v[196:199], v[160:163], v[16:19]\n"
    "ds_read_b64_tr_b16 v[192:193], v212 offset:16384\n"
    "ds_read_b64_tr_b16 v[194:195], v212 offset:17408\n"
    "v_mfma_f32_16x16x32_bf16 v[20:23], v[196:199], v[164:167], v[20:23]\n"
    "v_mfma_f32_16x16x32_bf16 v[24:27], v[196:199], v[168:171], v[24:27]\n"
    "v_mfma_f32_16x16x32_bf16 v[28:31], v[196:199], v[172:175], v[28:31]\n"
    "v_mfma_f32_16x16x32_bf16 v[32:35], v[200:203], v[160:163], v[32:35]\n"
    "ds_read_b64_tr_b16 v[196:197], v212 offset:32768\n"
    "ds_read_b64_tr_b16 v[198:199], v212 offset:33792\n"
    "v_mfma_f32_16x16x32_bf16 v[36:39], v[200:203], v[164:167], v[36:39]\n"
    "ds_read_b128 v[180:183], v209 offset:2048\n"
    "v_mfma_f32_16x16x32_bf16 v[40:43], v[200:203], v[168:171], v[40:43]\n"
    "v_mfma_f32_16x16x32_bf16 v[44:47], v[200:203], v[172:175], v[44:47]\n"
    "s_waitcnt lgkmcnt(7)\n"
    "v_mfma_f32_16x16x32_bf16 v[48:51], v[204:207], v[160:163], v[48:51]\n"
    "ds_read_b32 v232, v231\n"
    "v_mfma_f32_16x16x32_bf16 v[52:55], v[204:207], v[164:167], v[52:55]\n"
    "ds_read_b64_tr_b16 v[200:201], v211 offset:0\n"
    "ds_read_b64_tr_b16 v[202:203], v211 offset:1024\n"
    "v_mfma_f32_16x16x32_bf16 v[56:59], v[204:207], v[168:171], v[56:59]\n"
    "v_mfma_f32_16x16x32_bf16 v[60:63], v[204:207], v[172:175], v[60:63]\n"
    "s_waitcnt lgkmcnt(6)\n"
    "s_waitcnt lgkmcnt(2)\n"
    "v_cmp_gt_u32 vcc, s54, v232\n"
    "s_cbranch_vccz 115f\n"
    "114:\n"
    "s_sleep 1\n"
    "ds_read_b32 v232, v231\n"
    "s_waitcnt lgkmcnt(0)\n"
    "v_cmp_gt_u32 vcc, s54, v232\n"
    "s_cbranch_vccnz 114b\n"
    "115:\n"
    "v_mfma_f32_16x16x32_bf16 v[64:67], v[192:195], v[160:163], v[64:67]\n"
    "s_add_u32 m0, s49, 57344\n"
    "ds_read_b64_tr_b16 v[204:205], v212 offset:0\n"
    "ds_read_b64_tr_b16 v[206:207], v212 offset:1024\n"
    "v_mfma_f32_16x16x32_bf16 v[68:71], v[192:195], v[164:167], v[68:71]\n"
    "s_mov_b64 exec, s[50:51]\n"
    "global_load_lds_dwordx4 v227, s[42:43]\n"
    "s_mov_b64 exec, -1\n"
    "ds_read_b128 v[184:187], v209 offset:4096\n"
    "v_mfma_f32_16x16x32_bf16 v[72:75], v[192:195], v[168:171], v[72:75]\n"
    "v_mfma_f32_16x16x32_bf16 v[76:79], v[192:195], v[172:175], v[76:79]\n"
    "v_mfma_f32_16x16x32_bf16 v[80:83], v[196:199], v[160:163], v[80:83]\n"
    "s_add_u32 m0, s49, 73728\n"
    "ds_read_b64_tr_b16 v[192:193], v214 offset:0\n"
    "ds_read_b64_tr_b16 v[194:195], v214 offset:1024\n"
    "v_mfma_f32_16x16x32_bf16 v[84:87], v[196:199], v[164:167], v[84:87]\n"
    "global_load_lds_dwordx4 v229, s[44:45]\n"
    "v_mfma_f32_16x16x32_bf16 v[88:91], v[196:199], v[168:171], v[88:91]\n"
    "v_mfma_f32_16x16x32_bf16 v[92:95], v[196:199], v[172:175], v[92:95]\n"
    "s_waitcnt lgkmcnt(5)\n"
    "v_mfma_f32_16x16x32_bf16 v[96:99], v[200:203], v[160:163], v[96:99]\n"
    "s_add_u32 m0, s49, 90112\n"
    "ds_read_b64_tr_b16 v[196:197], v215 offset:0\n"
    "ds_read_b64_tr_b16 v[198:199], v215 offset:1024\n"
    "v_mfma_f32_16x16x32_bf16 v[100:103], v[200:203], v[164:167], v[100:103]\n"
    "global_load_lds_dwordx4 v229, s[46:47]\n"
    "ds_read_b128 v[188:191], v209 offset:6144\n"
    "v_mfma_f32_16x16x32_bf16 v[104:107], v[200:203], v[168:171], v[104:107]\n"
    "v_mfma_f32_16x16x32_bf16 v[108:111], v[200:203], v[172:175], v[108:111]\n"
    "s_waitcnt lgkmcnt(6)\n"
    "v_mfma_f32_16x16x32_bf16 v[112:115], v[204:207], v[160:163], v[112:115]\n"
    "ds_read_b64_tr_b16 v[200:201], v210 offset:24576\n"
    "ds_read_b64_tr_b16 v[202:203], v210 offset:25600\n"
    "v_mfma_f32_16x16x32_bf16 v[116:119], v[204:207], v[164:167], v[116:119]\n"
    "v_mfma_f32_16x16x32_bf16 v[120:123], v[204:207], v[168:171], v[120:123]\n"
    "v_mfma_f32_16x16x32_bf16 v[124:127], v[204:207], v[172:175], v[124:127]\n"
    "s_waitcnt lgkmcnt(5)\n"
    "v_mfma_f32_16x16x32_bf16 v[128:131], v[192:195], v[160:163], v[128:131]\n"
    "ds_read_b64_tr_b16 v[204:205], v210 offset:40960\n"
    "ds_read_b64_tr_b16 v[206:207], v210 offset:41984\n"
    "v_mfma_f32_16x16x32_bf16 v[132:135], v[192:195], v[164:167], v[132:135]\n"
    "s_add_u32 s40, s40, 128\n"
    "s_addc_u32 s41, s41, 0\n"
    "s_add_u32 s42, s42, 393216\n"
    "s_addc_u32 s43, s43, 0\n"
    "s_add_u32 s44, s44, 458752\n"
    "s_addc_u32 s45, s45, 0\n"
    "s_add_u32 s46, s46, 458752\n"
    "s_addc_u32 s47, s47, 0\n"
    "v_mfma_f32_16x16x32_bf16 v[136:139], v[192:195], v[168:171], v[136:139]\n"
    "v_mfma_f32_16x16x32_bf16 v[140:143], v[192:195], v[172:175], v[140:143]\n"
    "s_waitcnt lgkmcnt(5)\n"
    "s_waitcnt lgkmcnt(4)\n"
    "s_add_u32 s52, s52, 1\n"
    "v_mov_b32 v233, s52\n"
    "s_mov_b32 s54, s52\n"
    "s_mov_b64 exec, 1\n"
    "ds_write_b32 v230, v233\n"
    "s_mov_b64 exec, -1\n"
    "v_mfma_f32_16x16x32_bf16 v[144:147], v[196:199], v[160:163], v[144:147]\n"
    "ds_read_b64_tr_b16 v[192:193], v211 offset:24576\n"
    "ds_read_b64_tr_b16 v[194:195], v211 offset:25600\n"
    "v_mfma_f32_16x16x32_bf16 v[148:151], v[196:199], v[164:167], v[148:151]\n"
    "v_mfma_f32_16x16x32_bf16 v[152:155], v[196:199], v[168:171], v[152:155]\n"
    "v_mfma_f32_16x16x32_bf16 v[156:159], v[196:199], v[172:175], v[156:159]\n"
    "s_waitcnt lgkmcnt(5)\n"
    "v_mfma_f32_16x16x32_bf16 v[0:3], v[200:203], v[176:179], v[0:3]\n"
    "ds_read_b32 v232, v231\n"
    "v_mfma_f32_16x16x32_bf16 v[4:7], v[200:203], v[180:183], v[4:7]\n"
    "ds_read_b64_tr_b16 v[196:197], v211 offset:40960\n"
    "ds_read_b64_tr_b16 v[198:199], v211 offset:41984\n"
    "v_mfma_f32_16x16x32_bf16 v[8:11], v[200:203], v[184:187], v[8:11]\n"
    "v_mfma_f32_16x16x32_bf16 v[12:15], v[200:203], v[188:191], v[12:15]\n"
    "s_waitcnt lgkmcnt(6)\n"
    "s_waitcnt lgkmcnt(2)\n"
    "v_cmp_gt_u32 vcc, s54, v232\n"
    "s_cbranch_vccz 117f\n"
    "116:\n"
    "s_sleep 1\n"
    "ds_read_b32 v232, v231\n"
    "s_waitcnt lgkmcnt(0)\n"
    "v_cmp_gt_u32 vcc, s54, v232\n"
    "s_cbranch_vccnz 116b\n"
    "117:\n"
    "v_mfma_f32_16x16x32_bf16 v[16:19], v[204:207], v[176:179], v[16:19]\n"
    "s_add_u32 m0, s48, 0\n"
    "ds_read_b64_tr_b16 v[200:201], v212 offset:24576\n"
    "ds_read_b64_tr_b16 v[202:203], v212 offset:25600\n"
    "v_mfma_f32_16x16x32_bf16 v[20:23], v[204:207], v[180:183], v[20:23]\n"
    "global_load_lds_dwordx4 v222, s[40:41]\n"
    "v_mfma_f32_16x16x32_bf16 v[24:27], v[204:207], v[184:187], v[24:27]\n"
    "v_mfma_f32_16x16x32_bf16 v[28:31], v[204:207], v[188:191], v[28:31]\n"
    "v_mfma_f32_16x16x32_bf16 v[32:35], v[192:195], v[176:179], v[32:35]\n"
    "s_add_u32 m0, s48, 1024\n"
    "ds_read_b64_tr_b16 v[204:205], v212 offset:40960\n"
    "ds_read_b64_tr_b16 v[206:207], v212 offset:41984\n"
    "v_mfma_f32_16x16x32_bf16 v[36:39], v[192:195], v[180:183], v[36:39]\n"
    "global_load_lds_dwordx4 v223, s[40:41]\n"
    "v_mfma_f32_16x16x32_bf16 v[40:43], v[192:195], v[184:187], v[40:43]\n"
    "v_mfma_f32_16x16x32_bf16 v[44:47], v[192:195], v[188:191], v[44:47]\n"
    "s_waitcnt lgkmcnt(4)\n"
    "v_mfma_f32_16x16x32_bf16 v[48:51], v[196:199], v[176:179], v[48:51]\n"
    "s_add_u32 m0, s48, 2048\n"
    "ds_read_b64_tr_b16 v[192:193], v211 offset:8192\n"
    "ds_read_b64_tr_b16 v[194:195], v211 offset:9216\n"
    "v_mfma_f32_16x16x32_bf16 v[52:55], v[196:199], v[180:183], v[52:55]\n"
    "global_load_lds_dwordx4 v224, s[40:41]\n"
    "v_mfma_f32_16x16x32_bf16 v[56:59], v[196:199], v[184:187], v[56:59]\n"
    "v_mfma_f32_16x16x32_bf16 v[60:63], v[196:199], v[188:191], v[60:63]\n"
    "s_waitcnt lgkmcnt(4)\n"
    "s_waitcnt vmcnt(6)\n"
    "s_add_u32 s52, s52, 1\n"
    "v_mov_b32 v233, s52\n"
    "s_mov_b32 s54, s52\n"
    "s_mov_b64 exec, 1\n"
    "ds_write_b32 v230, v233\n"
    "s_mov_b64 exec, -1\n"
    "v_mfma_f32_16x16x32_bf16 v[64:67], v[200:203], v[176:179], v[64:67]\n"
    "ds_read_b64_tr_b16 v[196:197], v212 offset:8192\n"
    "ds_read_b64_tr_b16 v[198:199], v212 offset:9216\n"
    "v_mfma_f32_16x16x32_bf16 v[68:71], v[200:203], v[180:183], v[68:71]\n"
    "v_mfma_f32_16x16x32_bf16 v[72:75], v[200:203], v[184:187], v[72:75]\n"
    "v_mfma_f32_16x16x32_bf16 v[76:79], v[200:203], v[188:191], v[76:79]\n"
    "s_waitcnt lgkmcnt(5)\n"
    "v_mfma_f32_16x16x32_bf16 v[80:83], v[204:207], v[176:179], v[80:83]\n"
    "s_add_u32 m0, s48, 3072\n"
    "ds_read_b64_tr_b16 v[200:201], v214 offset:8192\n"
    "ds_read_b64_tr_b16 v[202:203], v214 offset:9216\n"
    "v_mfma_f32_16x16x32_bf16 v[84:87], v[204:207], v[180:183], v[84:87]\n"
    "global_load_lds_dwordx4 v225, s[40:41]\n"
    "v_mfma_f32_16x16x32_bf16 v[88:91], v[204:207], v[184:187], v[88:91]\n"
    "v_mfma_f32_16x16x32_bf16 v[92:95], v[204:207], v[188:191], v[92:95]\n"
    "s_waitcnt lgkmcnt(5)\n"
    "v_mfma_f32_16x16x32_bf16 v[96:99], v[192:195], v[176:179], v[96:99]\n"
    "s_add_u32 m0, s49, 0\n"
    "ds_read_b32 v232, v231\n"
    "v_mfma_f32_16x16x32_bf16 v[100:103], v[192:195], v[180:183], v[100:103]\n"
    "s_mov_b64 exec, s[50:51]\n"
    "global_load_lds_dwordx4 v226, s[42:43]\n"
    "s_mov_b64 exec, -1\n"
    "ds_read_b64_tr_b16 v[204:205], v215 offset:8192\n"
    "ds_read_b64_tr_b16 v[206:207], v215 offset:9216\n"
    "v_mfma_f32_16x16x32_bf16 v[104:107], v[192:195], v[184:187], v[104:107]\n"
    "v_mfma_f32_16x16x32_bf16 v[108:111], v[192:195], v[188:191], v[108:111]\n"
    "s_waitcnt lgkmcnt(5)\n"
    "s_waitcnt lgkmcnt(2)\n"
    "v_cmp_gt_u32 vcc, s54, v232\n"
    "s_cbranch_vccz 119f\n"
    "118:\n"
    "s_sleep 1\n"
    "ds_read_b32 v232, v231\n"
    "s_waitcnt lgkmcnt(0)\n"
    "v_cmp_gt_u32 vcc, s54, v232\n"
    "s_cbranch_vccnz 118b\n"
    "119:\n"
    "v_mfma_f32_16x16x32_bf16 v[112:115], v[196:199], v[176:179], v[112:115]\n"
    "ds_read_b64_tr_b16 v[192:193], v216 offset:16384\n"
    "ds_read_b64_tr_b16 v[194:195], v216 offset:17408\n"
    "v_mfma_f32_16x16x32_bf16 v[116:119], v[196:199], v[180:183], v[116:119]\n"
    "ds_read_b128 v[160:163], v208 offset:32768\n"
    "ds_read_b128 v[164:167], v208 offset:34816\n"
    "v_mfma_f32_16x16x32_bf16 v[120:123], v[196:199], v[184:187], v[120:123]\n"
    "v_mfma_f32_16x16x32_bf16 v[124:127], v[196:199], v[188:191], v[124:127]\n"
    "v_mfma_f32_16x16x32_bf16 v[128:131], v[200:203], v[176:179], v[128:131]\n"
    "s_add_u32 m0, s49, 16384\n"
    "ds_read_b64_tr_b16 v[196:197], v216 offset:32768\n"
    "ds_read_b64_tr_b16 v[198:199], v216 offset:33792\n"
    "v_mfma_f32_16x16x32_bf16 v[132:135], v[200:203], v[180:183], v[132:135]\n"
    "global_load_lds_dwordx4 v228, s[44:45]\n"
    "ds_read_b128 v[168:171], v208 offset:36864\n"
    "v_mfma_f32_16x16x32_bf16 v[136:139], v[200:203], v[184:187], v[136:139]\n"
    "v_mfma_f32_16x16x32_bf16 v[140:143], v[200:203], v[188:191], v[140:143]\n"
    "s_waitcnt lgkmcnt(7)\n"
    "v_mfma_f32_16x16x32_bf16 v[144:147], v[204:207], v[176:179], v[144:147]\n"
    "s_add_u32 m0, s49, 32768\n"
    "ds_read_b64_tr_b16 v[200:201], v217 offset:16384\n"
    "ds_read_b64_tr_b16 v[202:203], v217 offset:17408\n"
    "v_mfma_f32_16x16x32_bf16 v[148:151], v[204:207], v[180:183], v[148:151]\n"
    "global_load_lds_dwordx4 v228, s[46:47]\n"
    "ds_read_b128 v[172:175], v208 offset:38912\n"
    "v_mfma_f32_16x16x32_bf16 v[152:155], v[204:207], v[184:187], v[152:155]\n"
    "v_mfma_f32_16x16x32_bf16 v[156:159], v[204:207], v[188:191], v[156:159]\n"
    "s_add_u32 s53, s53, 1\n"
    "s_cmp_lt_u32 s53, 6\n"
    "s_cbranch_scc1 9b\n"
    "s_waitcnt lgkmcnt(0)\n"
    "v_mfma_f32_16x16x32_bf16 v[0:3], v[192:195], v[160:163], v[0:3]\n"
    "ds_read_b64_tr_b16 v[204:205], v217 offset:32768\n"
    "ds_read_b64_tr_b16 v[206:207], v217 offset:33792\n"
    "v_mfma_f32_16x16x32_bf16 v[4:7], v[192:195], v[164:167], v[4:7]\n"
    "ds_read_b128 v[176:179], v209 offset:32768\n"
    "v_mfma_f32_16x16x32_bf16 v[8:11], v[192:195], v[168:171], v[8:11]\n"
    "v_mfma_f32_16x16x32_bf16 v[12:15], v[192:195], v[172:175], v[12:15]\n"
    "s_waitcnt vmcnt(7)\n"
    "s_add_u32 s52, s52, 1\n"
    "v_mov_b32 v233, s52\n"
    "s_mov_b32 s54, s52\n"
    "s_mov_b64 exec, 1\n"
    "ds_write_b32 v230, v233\n"
    "s_mov_b64 exec, -1\n"
    "v_mfma_f32_16x16x32_bf16 v[16:19], v[196:199], v[160:163], v[16:19]\n"
    "ds_read_b64_tr_b16 v[192:193], v218 offset:16384\n"
    "ds_read_b64_tr_b16 v[194:195], v218 offset:17408\n"
    "v_mfma_f32_16x16x32_bf16 v[20:23], v[196:199], v[164:167], v[20:23]\n"
    "v_mfma_f32_16x16x32_bf16 v[24:27], v[196:199], v[168:171], v[24:27]\n"
    "v_mfma_f32_16x16x32_bf16 v[28:31], v[196:199], v[172:175], v[28:31]\n"
    "v_mfma_f32_16x16x32_bf16 v[32:35], v[200:203], v[160:163], v[32:35]\n"
    "ds_read_b64_tr_b16 v[196:197], v218 offset:32768\n"
    "ds_read_b64_tr_b16 v[198:199], v218 offset:33792\n"
    "v_mfma_f32_16x16x32_bf16 v[36:39], v[200:203], v[164:167], v[36:39]\n"
    "ds_read_b128 v[180:183], v209 offset:34816\n"
    "v_mfma_f32_16x16x32_bf16 v[40:43], v[200:203], v[168:171], v[40:43]\n"
    "v_mfma_f32_16x16x32_bf16 v[44:47], v[200:203], v[172:175], v[44:47]\n"
    "s_waitcnt lgkmcnt(7)\n"
    "v_mfma_f32_16x16x32_bf16 v[48:51], v[204:207], v[160:163], v[48:51]\n"
    "ds_read_b32 v232, v231\n"
    "v_mfma_f32_16x16x32_bf16 v[52:55], v[204:207], v[164:167], v[52:55]\n"
    "ds_read_b64_tr_b16 v[200:201], v217 offset:0\n"
    "ds_read_b64_tr_b16 v[202:203], v217 offset:1024\n"
    "v_mfma_f32_16x16x32_bf16 v[56:59], v[204:207], v[168:171], v[56:59]\n"
    "v_mfma_f32_16x16x32_bf16 v[60:63], v[204:207], v[172:175], v[60:63]\n"
    "s_waitcnt lgkmcnt(6)\n"
    "s_waitcnt lgkmcnt(2)\n"
    "v_cmp_gt_u32 vcc, s54, v232\n"
    "s_cbranch_vccz 121f\n"
    "120:\n"
    "s_sleep 1\n"
    "ds_read_b32 v232, v231\n"
    "s_waitcnt lgkmcnt(0)\n"
    "v_cmp_gt_u32 vcc, s54, v232\n"
    "s_cbranch_vccnz 120b\n"
    "121:\n"
    "v_mfma_f32_16x16x32_bf16 v[64:67], v[192:195], v[160:163], v[64:67]\n"
    "s_add_u32 m0, s49, 8192\n"
    "ds_read_b64_tr_b16 v[204:205], v218 offset:0\n"
    "ds_read_b64_tr_b16 v[206:207], v218 offset:1024\n"
    "v_mfma_f32_16x16x32_bf16 v[68:71], v[192:195], v[164:167], v[68:71]\n"
    "s_mov_b64 exec, s[50:51]\n"
    "global_load_lds_dwordx4 v227, s[42:43]\n"
    "s_mov_b64 exec, -1\n"
    "ds_read_b128 v[184:187], v209 offset:36864\n"
    "v_mfma_f32_16x16x32_bf16 v[72:75], v[192:195], v[168:171], v[72:75]\n"
    "v_mfma_f32_16x16x32_bf16 v[76:79], v[192:195], v[172:175], v[76:79]\n"
    "v_mfma_f32_16x16x32_bf16 v[80:83], v[196:199], v[160:163], v[80:83]\n"
    "s_add_u32 m0, s49, 24576\n"
    "ds_read_b64_tr_b16 v[192:193], v220 offset:0\n"
    "ds_read_b64_tr_b16 v[194:195], v220 offset:1024\n"
    "v_mfma_f32_16x16x32_bf16 v[84:87], v[196:199], v[164:167], v[84:87]\n"
    "global_load_lds_dwordx4 v229, s[44:45]\n"
    "v_mfma_f32_16x16x32_bf16 v[88:91], v[196:199], v[168:171], v[88:91]\n"
    "v_mfma_f32_16x16x32_bf16 v[92:95], v[196:199], v[172:175], v[92:95]\n"
    "s_waitcnt lgkmcnt(5)\n"
    "v_mfma_f32_16x16x32_bf16 v[96:99], v[200:203], v[160:163], v[96:99]\n"
    "s_add_u32 m0, s49, 40960\n"
    "ds_read_b64_tr_b16 v[196:197], v221 offset:0\n"
    "ds_read_b64_tr_b16 v[198:199], v221 offset:1024\n"
    "v_mfma_f32_16x16x32_bf16 v[100:103], v[200:203], v[164:167], v[100:103]\n"
    "global_load_lds_dwordx4 v229, s[46:47]\n"
    "ds_read_b128 v[188:191], v209 offset:38912\n"
    "v_mfma_f32_16x16x32_bf16 v[104:107], v[200:203], v[168:171], v[104:107]\n"
    "v_mfma_f32_16x16x32_bf16 v[108:111], v[200:203], v[172:175], v[108:111]\n"
    "s_waitcnt lgkmcnt(6)\n"
    "v_mfma_f32_16x16x32_bf16 v[112:115], v[204:207], v[160:163], v[112:115]\n"
    "ds_read_b64_tr_b16 v[200:201], v216 offset:24576\n"
    "ds_read_b64_tr_b16 v[202:203], v216 offset:25600\n"
    "v_mfma_f32_16x16x32_bf16 v[116:119], v[204:207], v[164:167], v[116:119]\n"
    "v_mfma_f32_16x16x32_bf16 v[120:123], v[204:207], v[168:171], v[120:123]\n"
    "v_mfma_f32_16x16x32_bf16 v[124:127], v[204:207], v[172:175], v[124:127]\n"
    "s_waitcnt lgkmcnt(5)\n"
    "v_mfma_f32_16x16x32_bf16 v[128:131], v[192:195], v[160:163], v[128:131]\n"
    "ds_read_b64_tr_b16 v[204:205], v216 offset:40960\n"
    "ds_read_b64_tr_b16 v[206:207], v216 offset:41984\n"
    "v_mfma_f32_16x16x32_bf16 v[132:135], v[192:195], v[164:167], v[132:135]\n"
    "s_add_u32 s40, s40, 128\n"
    "s_addc_u32 s41, s41, 0\n"
    "s_add_u32 s42, s42, 393216\n"
    "s_addc_u32 s43, s43, 0\n"
    "s_add_u32 s44, s44, 458752\n"
    "s_addc_u32 s45, s45, 0\n"
    "s_add_u32 s46, s46, 458752\n"
    "s_addc_u32 s47, s47, 0\n"
    "v_mfma_f32_16x16x32_bf16 v[136:139], v[192:195], v[168:171], v[136:139]\n"
    "v_mfma_f32_16x16x32_bf16 v[140:143], v[192:195], v[172:175], v[140:143]\n"
    "s_waitcnt lgkmcnt(5)\n"
    "s_waitcnt lgkmcnt(4)\n"
    "s_add_u32 s52, s52, 1\n"
    "v_mov_b32 v233, s52\n"
    "s_mov_b32 s54, s52\n"
    "s_mov_b64 exec, 1\n"
    "ds_write_b32 v230, v233\n"
    "s_mov_b64 exec, -1\n"
    "v_mfma_f32_16x16x32_bf16 v[144:147], v[196:199], v[160:163], v[144:147]\n"
    "ds_read_b64_tr_b16 v[192:193], v217 offset:24576\n"
    "ds_read_b64_tr_b16 v[194:195], v217 offset:25600\n"
    "v_mfma_f32_16x16x32_bf16 v[148:151], v[196:199], v[164:167], v[148:151]\n"
    "v_mfma_f32_16x16x32_bf16 v[152:155], v[196:199], v[168:171], v[152:155]\n"
    "v_mfma_f32_16x16x32_bf16 v[156:159], v[196:199], v[172:175], v[156:159]\n"
    "s_waitcnt lgkmcnt(5)\n"
    "v_mfma_f32_16x16x32_bf16 v[0:3], v[200:203], v[176:179], v[0:3]\n"
    "ds_read_b32 v232, v231\n"
    "v_mfma_f32_16x16x32_bf16 v[4:7], v[200:203], v[180:183], v[4:7]\n"
    "ds_read_b64_tr_b16 v[196:197], v217 offset:40960\n"
    "ds_read_b64_tr_b16 v[198:199], v217 offset:41984\n"
    "v_mfma_f32_16x16x32_bf16 v[8:11], v[200:203], v[184:187], v[8:11]\n"
    "v_mfma_f32_16x16x32_bf16 v[12:15], v[200:203], v[188:191], v[12:15]\n"
    "s_waitcnt lgkmcnt(6)\n"
    "s_waitcnt lgkmcnt(2)\n"
    "v_cmp_gt_u32 vcc, s54, v232\n"
    "s_cbranch_vccz 123f\n"
    "122:\n"
    "s_sleep 1\n"
    "ds_read_b32 v232, v231\n"
    "s_waitcnt lgkmcnt(0)\n"
    "v_cmp_gt_u32 vcc, s54, v232\n"
    "s_cbranch_vccnz 122b\n"
    "123:\n"
    "v_mfma_f32_16x16x32_bf16 v[16:19], v[204:207], v[176:179], v[16:19]\n"
    "s_add_u32 m0, s48, 32768\n"
    "ds_read_b64_tr_b16 v[200:201], v218 offset:24576\n"
    "ds_read_b64_tr_b16 v[202:203], v218 offset:25600\n"
    "v_mfma_f32_16x16x32_bf16 v[20:23], v[204:207], v[180:183], v[20:23]\n"
    "global_load_lds_dwordx4 v222, s[40:41]\n"
    "v_mfma_f32_16x16x32_bf16 v[24:27], v[204:207], v[184:187], v[24:27]\n"
    "v_mfma_f32_16x16x32_bf16 v[28:31], v[204:207], v[188:191], v[28:31]\n"
    "v_mfma_f32_16x16x32_bf16 v[32:35], v[192:195], v[176:179], v[32:35]\n"
    "s_add_u32 m0, s48, 33792\n"
    "ds_read_b64_tr_b16 v[204:205], v218 offset:40960\n"
    "ds_read_b64_tr_b16 v[206:207], v218 offset:41984\n"
    "v_mfma_f32_16x16x32_bf16 v[36:39], v[192:195], v[180:183], v[36:39]\n"
    "global_load_lds_dwordx4 v223, s[40:41]\n"
    "v_mfma_f32_16x16x32_bf16 v[40:43], v[192:195], v[184:187], v[40:43]\n"
    "v_mfma_f32_16x16x32_bf16 v[44:47], v[192:195], v[188:191], v[44:47]\n"
    "s_waitcnt lgkmcnt(4)\n"
    "v_mfma_f32_16x16x32_bf16 v[48:51], v[196:199], v[176:179], v[48:51]\n"
    "s_add_u32 m0, s48, 34816\n"
    "ds_read_b64_tr_b16 v[192:193], v217 offset:8192\n"
    "ds_read_b64_tr_b16 v[194:195], v217 offset:9216\n"
    "v_mfma_f32_16x16x32_bf16 v[52:55], v[196:199], v[180:183], v[52:55]\n"
    "global_load_lds_dwordx4 v224, s[40:41]\n"
    "v_mfma_f32_16x16x32_bf16 v[56:59], v[196:199], v[184:187], v[56:59]\n"
    "v_mfma_f32_16x16x32_bf16 v[60:63], v[196:199], v[188:191], v[60:63]\n"
    "s_waitcnt lgkmcnt(4)\n"
    "s_waitcnt vmcnt(6)\n"
    "s_add_u32 s52, s52, 1\n"
    "v_mov_b32 v233, s52\n"
    "s_mov_b32 s54, s52\n"
    "s_mov_b64 exec, 1\n"
    "ds_write_b32 v230, v233\n"
    "s_mov_b64 exec, -1\n"
    "v_mfma_f32_16x16x32_bf16 v[64:67], v[200:203], v[176:179], v[64:67]\n"
    "ds_read_b64_tr_b16 v[196:197], v218 offset:8192\n"
    "ds_read_b64_tr_b16 v[198:199], v218 offset:9216\n"
    "v_mfma_f32_16x16x32_bf16 v[68:71], v[200:203], v[180:183], v[68:71]\n"
    "v_mfma_f32_16x16x32_bf16 v[72:75], v[200:203], v[184:187], v[72:75]\n"
    "v_mfma_f32_16x16x32_bf16 v[76:79], v[200:203], v[188:191], v[76:79]\n"
    "s_waitcnt lgkmcnt(5)\n"
    "v_mfma_f32_16x16x32_bf16 v[80:83], v[204:207], v[176:179], v[80:83]\n"
    "s_add_u32 m0, s48, 35840\n"
    "ds_read_b64_tr_b16 v[200:201], v220 offset:8192\n"
    "ds_read_b64_tr_b16 v[202:203], v220 offset:9216\n"
    "v_mfma_f32_16x16x32_bf16 v[84:87], v[204:207], v[180:183], v[84:87]\n"
    "global_load_lds_dwordx4 v225, s[40:41]\n"
    "v_mfma_f32_16x16x32_bf16 v[88:91], v[204:207], v[184:187], v[88:91]\n"
    "v_mfma_f32_16x16x32_bf16 v[92:95], v[204:207], v[188:191], v[92:95]\n"
    "s_waitcnt lgkmcnt(5)\n"
    "v_mfma_f32_16x16x32_bf16 v[96:99], v[192:195], v[176:179], v[96:99]\n"
    "s_add_u32 m0, s49, 49152\n"
    "ds_read_b32 v232, v231\n"
    "v_mfma_f32_16x16x32_bf16 v[100:103], v[192:195], v[180:183], v[100:103]\n"
    "s_mov_b64 exec, s[50:51]\n"
    "global_load_lds_dwordx4 v226, s[42:43]\n"
    "s_mov_b64 exec, -1\n"
    "ds_read_b64_tr_b16 v[204:205], v221 offset:8192\n"
    "ds_read_b64_tr_b16 v[206:207], v221 offset:9216\n"
    "v_mfma_f32_16x16x32_bf16 v[104:107], v[192:195], v[184:187], v[104:107]\n"
    "v_mfma_f32_16x16x32_bf16 v[108:111], v[192:195], v[188:191], v[108:111]\n"
    "s_waitcnt lgkmcnt(5)\n"
    "s_waitcnt lgkmcnt(2)\n"
    "v_cmp_gt_u32 vcc, s54, v232\n"
    "s_cbranch_vccz 125f\n"
    "124:\n"
    "s_sleep 1\n"
    "ds_read_b32 v232, v231\n"
    "s_waitcnt lgkmcnt(0)\n"
    "v_cmp_gt_u32 vcc, s54, v232\n"
    "s_cbranch_vccnz 124b\n"
    "125:\n"
    "v_mfma_f32_16x16x32_bf16 v[112:115], v[196:199], v[176:179], v[112:115]\n"
    "ds_read_b64_tr_b16 v[192:193], v210 offset:16384\n"
    "ds_read_b64_tr_b16 v[194:195], v210 offset:17408\n"
    "v_mfma_f32_16x16x32_bf16 v[116:119], v[196:199], v[180:183], v[116:119]\n"
    "ds_read_b128 v[160:163], v208 offset:0\n"
    "ds_read_b128 v[164:167], v208 offset:2048\n"
    "v_mfma_f32_16x16x32_bf16 v[120:123], v[196:199], v[184:187], v[120:123]\n"
    "v_mfma_f32_16x16x32_bf16 v[124:127], v[196:199], v[188:191], v[124:127]\n"
    "v_mfma_f32_16x16x32_bf16 v[128:131], v[200:203], v[176:179], v[128:131]\n"
    "s_add_u32 m0, s49, 65536\n"
    "ds_read_b64_tr_b16 v[196:197], v210 offset:32768\n"
    "ds_read_b64_tr_b16 v[198:199], v210 offset:33792\n"
    "v_mfma_f32_16x16x32_bf16 v[132:135], v[200:203], v[180:183], v[132:135]\n"
    "global_load_lds_dwordx4 v228, s[44:45]\n"
    "ds_read_b128 v[168:171], v208 offset:4096\n"
    "v_mfma_f32_16x16x32_bf16 v[136:139], v[200:203], v[184:187], v[136:139]\n"
    "v_mfma_f32_16x16x32_bf16 v[140:143], v[200:203], v[188:191], v[140:143]\n"
    "s_waitcnt lgkmcnt(7)\n"
    "v_mfma_f32_16x16x32_bf16 v[144:147], v[204:207], v[176:179], v[144:147]\n"
    "s_add_u32 m0, s49, 81920\n"
    "ds_read_b64_tr_b16 v[200:201], v211 offset:16384\n"
    "ds_read_b64_tr_b16 v[202:203], v211 offset:17408\n"
    "v_mfma_f32_16x16x32_bf16 v[148:151], v[204:207], v[180:183], v[148:151]\n"
    "global_load_lds_dwordx4 v228, s[46:47]\n"
    "ds_read_b128 v[172:175], v208 offset:6144\n"
    "v_mfma_f32_16x16x32_bf16 v[152:155], v[204:207], v[184:187], v[152:155]\n"
    "v_mfma_f32_16x16x32_bf16 v[156:159], v[204:207], v[188:191], v[156:159]\n"
    "s_waitcnt lgkmcnt(0)\n"
    "v_mfma_f32_16x16x32_bf16 v[0:3], v[192:195], v[160:163], v[0:3]\n"
    "ds_read_b64_tr_b16 v[204:205], v211 offset:32768\n"
    "ds_read_b64_tr_b16 v[206:207], v211 offset:33792\n"
    "v_mfma_f32_16x16x32_bf16 v[4:7], v[192:195], v[164:167], v[4:7]\n"
    "ds_read_b128 v[176:179], v209 offset:0\n"
    "v_mfma_f32_16x16x32_bf16 v[8:11], v[192:195], v[168:171], v[8:11]\n"
    "v_mfma_f32_16x16x32_bf16 v[12:15], v[192:195], v[172:175], v[12:15]\n"
    "s_waitcnt vmcnt(7)\n"
    "s_add_u32 s52, s52, 1\n"
    "v_mov_b32 v233, s52\n"
    "s_mov_b32 s54, s52\n"
    "s_mov_b64 exec, 1\n"
    "ds_write_b32 v230, v233\n"
    "s_mov_b64 exec, -1\n"
    "v_mfma_f32_16x16x32_bf16 v[16:19], v[196:199], v[160:163], v[16:19]\n"
    "ds_read_b64_tr_b16 v[192:193], v212 offset:16384\n"
    "ds_read_b64_tr_b16 v[194:195], v212 offset:17408\n"
    "v_mfma_f32_16x16x32_bf16 v[20:23], v[196:199], v[164:167], v[20:23]\n"
    "v_mfma_f32_16x16x32_bf16 v[24:27], v[196:199], v[168:171], v[24:27]\n"
    "v_mfma_f32_16x16x32_bf16 v[28:31], v[196:199], v[172:175], v[28:31]\n"
    "v_mfma_f32_16x16x32_bf16 v[32:35], v[200:203], v[160:163], v[32:35]\n"
    "ds_read_b64_tr_b16 v[196:197], v212 offset:32768\n"
    "ds_read_b64_tr_b16 v[198:199], v212 offset:33792\n"
    "v_mfma_f32_16x16x32_bf16 v[36:39], v[200:203], v[164:167], v[36:39]\n"
    "ds_read_b128 v[180:183], v209 offset:2048\n"
    "v_mfma_f32_16x16x32_bf16 v[40:43], v[200:203], v[168:171], v[40:43]\n"
    "v_mfma_f32_16x16x32_bf16 v[44:47], v[200:203], v[172:175], v[44:47]\n"
    "s_waitcnt lgkmcnt(7)\n"
    "v_mfma_f32_16x16x32_bf16 v[48:51], v[204:207], v[160:163], v[48:51]\n"
    "ds_read_b32 v232, v231\n"
    "v_mfma_f32_16x16x32_bf16 v[52:55], v[204:207], v[164:167], v[52:55]\n"
    "ds_read_b64_tr_b16 v[200:201], v211 offset:0\n"
    "ds_read_b64_tr_b16 v[202:203], v211 offset:1024\n"
    "v_mfma_f32_16x16x32_bf16 v[56:59], v[204:207], v[168:171], v[56:59]\n"
    "v_mfma_f32_16x16x32_bf16 v[60:63], v[204:207], v[172:175], v[60:63]\n"
    "s_waitcnt lgkmcnt(6)\n"
    "s_waitcnt lgkmcnt(2)\n"
    "v_cmp_gt_u32 vcc, s54, v232\n"
    "s_cbranch_vccz 127f\n"
    "126:\n"
    "s_sleep 1\n"
    "ds_read_b32 v232, v231\n"
    "s_waitcnt lgkmcnt(0)\n"
    "v_cmp_gt_u32 vcc, s54, v232\n"
    "s_cbranch_vccnz 126b\n"
    "127:\n"
    "v_mfma_f32_16x16x32_bf16 v[64:67], v[192:195], v[160:163], v[64:67]\n"
    "s_add_u32 m0, s49, 57344\n"
    "ds_read_b64_tr_b16 v[204:205], v212 offset:0\n"
    "ds_read_b64_tr_b16 v[206:207], v212 offset:1024\n"
    "v_mfma_f32_16x16x32_bf16 v[68:71], v[192:195], v[164:167], v[68:71]\n"
    "s_mov_b64 exec, s[50:51]\n"
    "global_load_lds_dwordx4 v227, s[42:43]\n"
    "s_mov_b64 exec, -1\n"
    "ds_read_b128 v[184:187], v209 offset:4096\n"
    "v_mfma_f32_16x16x32_bf16 v[72:75], v[192:195], v[168:171], v[72:75]\n"
    "v_mfma_f32_16x16x32_bf16 v[76:79], v[192:195], v[172:175], v[76:79]\n"
    "v_mfma_f32_16x16x32_bf16 v[80:83], v[196:199], v[160:163], v[80:83]\n"
    "s_add_u32 m0, s49, 73728\n"
    "ds_read_b64_tr_b16 v[192:193], v214 offset:0\n"
    "ds_read_b64_tr_b16 v[194:195], v214 offset:1024\n"
    "v_mfma_f32_16x16x32_bf16 v[84:87], v[196:199], v[164:167], v[84:87]\n"
    "global_load_lds_dwordx4 v229, s[44:45]\n"
    "v_mfma_f32_16x16x32_bf16 v[88:91], v[196:199], v[168:171], v[88:91]\n"
    "v_mfma_f32_16x16x32_bf16 v[92:95], v[196:199], v[172:175], v[92:95]\n"
    "s_waitcnt lgkmcnt(5)\n"
    "v_mfma_f32_16x16x32_bf16 v[96:99], v[200:203], v[160:163], v[96:99]\n"
    "s_add_u32 m0, s49, 90112\n"
    "ds_read_b64_tr_b16 v[196:197], v215 offset:0\n"
    "ds_read_b64_tr_b16 v[198:199], v215 offset:1024\n"
    "v_mfma_f32_16x16x32_bf16 v[100:103], v[200:203], v[164:167], v[100:103]\n"
    "global_load_lds_dwordx4 v229, s[46:47]\n"
    "ds_read_b128 v[188:191], v209 offset:6144\n"
    "v_mfma_f32_16x16x32_bf16 v[104:107], v[200:203], v[168:171], v[104:107]\n"
    "v_mfma_f32_16x16x32_bf16 v[108:111], v[200:203], v[172:175], v[108:111]\n"
    "s_waitcnt lgkmcnt(6)\n"
    "v_mfma_f32_16x16x32_bf16 v[112:115], v[204:207], v[160:163], v[112:115]\n"
    "ds_read_b64_tr_b16 v[200:201], v210 offset:24576\n"
    "ds_read_b64_tr_b16 v[202:203], v210 offset:25600\n"
    "v_mfma_f32_16x16x32_bf16 v[116:119], v[204:207], v[164:167], v[116:119]\n"
    "v_mfma_f32_16x16x32_bf16 v[120:123], v[204:207], v[168:171], v[120:123]\n"
    "v_mfma_f32_16x16x32_bf16 v[124:127], v[204:207], v[172:175], v[124:127]\n"
    "s_waitcnt lgkmcnt(5)\n"
    "v_mfma_f32_16x16x32_bf16 v[128:131], v[192:195], v[160:163], v[128:131]\n"
    "ds_read_b64_tr_b16 v[204:205], v210 offset:40960\n"
    "ds_read_b64_tr_b16 v[206:207], v210 offset:41984\n"
    "v_mfma_f32_16x16x32_bf16 v[132:135], v[192:195], v[164:167], v[132:135]\n"
    "s_add_u32 s40, s40, 128\n"
    "s_addc_u32 s41, s41, 0\n"
    "s_add_u32 s42, s42, 393216\n"
    "s_addc_u32 s43, s43, 0\n"
    "s_add_u32 s44, s44, 458752\n"
    "s_addc_u32 s45, s45, 0\n"
    "s_add_u32 s46, s46, 458752\n"
    "s_addc_u32 s47, s47, 0\n"
    "v_mfma_f32_16x16x32_bf16 v[136:139], v[192:195], v[168:171], v[136:139]\n"
    "v_mfma_f32_16x16x32_bf16 v[140:143], v[192:195], v[172:175], v[140:143]\n"
    "s_waitcnt lgkmcnt(5)\n"
    "v_mfma_f32_16x16x32_bf16 v[144:147], v[196:199], v[160:163], v[144:147]\n"
    "ds_read_b64_tr_b16 v[192:193], v211 offset:24576\n"
    "ds_read_b64_tr_b16 v[194:195], v211 offset:25600\n"
    "v_mfma_f32_16x16x32_bf16 v[148:151], v[196:199], v[164:167], v[148:151]\n"
    "v_mfma_f32_16x16x32_bf16 v[152:155], v[196:199], v[168:171], v[152:155]\n"
    "v_mfma_f32_16x16x32_bf16 v[156:159], v[196:199], v[172:175], v[156:159]\n"
    "s_waitcnt lgkmcnt(4)\n"
    "v_mfma_f32_16x16x32_bf16 v[0:3], v[200:203], v[176:179], v[0:3]\n"
    "ds_read_b64_tr_b16 v[196:197], v211 offset:40960\n"
    "ds_read_b64_tr_b16 v[198:199], v211 offset:41984\n"
    "v_mfma_f32_16x16x32_bf16 v[4:7], v[200:203], v[180:183], v[4:7]\n"
    "v_mfma_f32_16x16x32_bf16 v[8:11], v[200:203], v[184:187], v[8:11]\n"
    "v_mfma_f32_16x16x32_bf16 v[12:15], v[200:203], v[188:191], v[12:15]\n"
    "s_waitcnt lgkmcnt(4)\n"
    "v_mfma_f32_16x16x32_bf16 v[16:19], v[204:207], v[176:179], v[16:19]\n"
    "ds_read_b64_tr_b16 v[200:201], v212 offset:24576\n"
    "ds_read_b64_tr_b16 v[202:203], v212 offset:25600\n"
    "v_mfma_f32_16x16x32_bf16 v[20:23], v[204:207], v[180:183], v[20:23]\n"
    "v_mfma_f32_16x16x32_bf16 v[24:27], v[204:207], v[184:187], v[24:27]\n"
    "v_mfma_f32_16x16x32_bf16 v[28:31], v[204:207], v[188:191], v[28:31]\n"
    "s_waitcnt lgkmcnt(4)\n"
    "v_mfma_f32_16x16x32_bf16 v[32:35], v[192:195], v[176:179], v[32:35]\n"
    "ds_read_b64_tr_b16 v[204:205], v212 offset:40960\n"
    "ds_read_b64_tr_b16 v[206:207], v212 offset:41984\n"
    "v_mfma_f32_16x16x32_bf16 v[36:39], v[192:195], v[180:183], v[36:39]\n"
    "v_mfma_f32_16x16x32_bf16 v[40:43], v[192:195], v[184:187], v[40:43]\n"
    "v_mfma_f32_16x16x32_bf16 v[44:47], v[192:195], v[188:191], v[44:47]\n"
    "s_waitcnt lgkmcnt(4)\n"
    "v_mfma_f32_16x16x32_bf16 v[48:51], v[196:199], v[176:179], v[48:51]\n"
    "ds_read_b64_tr_b16 v[192:193], v211 offset:8192\n"
    "ds_read_b64_tr_b16 v[194:195], v211 offset:9216\n"
    "v_mfma_f32_16x16x32_bf16 v[52:55], v[196:199], v[180:183], v[52:55]\n"
    "v_mfma_f32_16x16x32_bf16 v[56:59], v[196:199], v[184:187], v[56:59]\n"
    "v_mfma_f32_16x16x32_bf16 v[60:63], v[196:199], v[188:191], v[60:63]\n"
    "s_waitcnt lgkmcnt(4)\n"
    "s_waitcnt vmcnt(3)\n"
    "s_add_u32 s52, s52, 1\n"
    "v_mov_b32 v233, s52\n"
    "s_mov_b32 s54, s52\n"
    "s_mov_b64 exec, 1\n"
    "ds_write_b32 v230, v233\n"
    "s_mov_b64 exec, -1\n"
    "v_mfma_f32_16x16x32_bf16 v[64:67], v[200:203], v[176:179], v[64:67]\n"
    "ds_read_b64_tr_b16 v[196:197], v212 offset:8192\n"
    "ds_read_b64_tr_b16 v[198:199], v212 offset:9216\n"
    "v_mfma_f32_16x16x32_bf16 v[68:71], v[200:203], v[180:183], v[68:71]\n"
    "v_mfma_f32_16x16x32_bf16 v[72:75], v[200:203], v[184:187], v[72:75]\n"
    "v_mfma_f32_16x16x32_bf16 v[76:79], v[200:203], v[188:191], v[76:79]\n"
    "s_waitcnt lgkmcnt(5)\n"
    "v_mfma_f32_16x16x32_bf16 v[80:83], v[204:207], v[176:179], v[80:83]\n"
    "ds_read_b64_tr_b16 v[200:201], v214 offset:8192\n"
    "ds_read_b64_tr_b16 v[202:203], v214 offset:9216\n"
    "v_mfma_f32_16x16x32_bf16 v[84:87], v[204:207], v[180:183], v[84:87]\n"
    "v_mfma_f32_16x16x32_bf16 v[88:91], v[204:207], v[184:187], v[88:91]\n"
    "v_mfma_f32_16x16x32_bf16 v[92:95], v[204:207], v[188:191], v[92:95]\n"
    "s_waitcnt lgkmcnt(5)\n"
    "v_mfma_f32_16x16x32_bf16 v[96:99], v[192:195], v[176:179], v[96:99]\n"
    "ds_read_b32 v232, v231\n"
    "v_mfma_f32_16x16x32_bf16 v[100:103], v[192:195], v[180:183], v[100:103]\n"
    "ds_read_b64_tr_b16 v[204:205], v215 offset:8192\n"
    "ds_read_b64_tr_b16 v[206:207], v215 offset:9216\n"
    "v_mfma_f32_16x16x32_bf16 v[104:107], v[192:195], v[184:187], v[104:107]\n"
    "v_mfma_f32_16x16x32_bf16 v[108:111], v[192:195], v[188:191], v[108:111]\n"
    "s_waitcnt lgkmcnt(5)\n"
    "s_waitcnt lgkmcnt(2)\n"
    "v_cmp_gt_u32 vcc, s54, v232\n"
    "s_cbranch_vccz 129f\n"
    "128:\n"
    "s_sleep 1\n"
    "ds_read_b32 v232, v231\n"
    "s_waitcnt lgkmcnt(0)\n"
    "v_cmp_gt_u32 vcc, s54, v232\n"
    "s_cbranch_vccnz 128b\n"
    "129:\n"
    "v_mfma_f32_16x16x32_bf16 v[112:115], v[196:199], v[176:179], v[112:115]\n"
    "ds_read_b64_tr_b16 v[192:193], v216 offset:16384\n"
    "ds_read_b64_tr_b16 v[194:195], v216 offset:17408\n"
    "v_mfma_f32_16x16x32_bf16 v[116:119], v[196:199], v[180:183], v[116:119]\n"
    "ds_read_b128 v[160:163], v208 offset:32768\n"
    "ds_read_b128 v[164:167], v208 offset:34816\n"
    "v_mfma_f32_16x16x32_bf16 v[120:123], v[196:199], v[184:187], v[120:123]\n"
    "v_mfma_f32_16x16x32_bf16 v[124:127], v[196:199], v[188:191], v[124:127]\n"
    "v_mfma_f32_16x16x32_bf16 v[128:131], v[200:203], v[176:179], v[128:131]\n"
    "ds_read_b64_tr_b16 v[196:197], v216 offset:32768\n"
    "ds_read_b64_tr_b16 v[198:199], v216 offset:33792\n"
    "v_mfma_f32_16x16x32_bf16 v[132:135], v[200:203], v[180:183], v[132:135]\n"
    "ds_read_b128 v[168:171], v208 offset:36864\n"
    "v_mfma_f32_16x16x32_bf16 v[136:139], v[200:203], v[184:187], v[136:139]\n"
    "v_mfma_f32_16x16x32_bf16 v[140:143], v[200:203], v[188:191], v[140:143]\n"
    "s_waitcnt lgkmcnt(7)\n"
    "v_mfma_f32_16x16x32_bf16 v[144:147], v[204:207], v[176:179], v[144:147]\n"
    "ds_read_b64_tr_b16 v[200:201], v217 offset:16384\n"
    "ds_read_b64_tr_b16 v[202:203], v217 offset:17408\n"
    "v_mfma_f32_16x16x32_bf16 v[148:151], v[204:207], v[180:183], v[148:151]\n"
    "ds_read_b128 v[172:175], v208 offset:38912\n"
    "v_mfma_f32_16x16x32_bf16 v[152:155], v[204:207], v[184:187], v[152:155]\n"
    "v_mfma_f32_16x16x32_bf16 v[156:159], v[204:207], v[188:191], v[156:159]\n"
    "s_waitcnt lgkmcnt(0)\n"
    "v_mfma_f32_16x16x32_bf16 v[0:3], v[192:195], v[160:163], v[0:3]\n"
    "ds_read_b64_tr_b16 v[204:205], v217 offset:32768\n"
    "ds_read_b64_tr_b16 v[206:207], v217 offset:33792\n"
    "v_mfma_f32_16x16x32_bf16 v[4:7], v[192:195], v[164:167], v[4:7]\n"
    "ds_read_b128 v[176:179], v209 offset:32768\n"
    "v_mfma_f32_16x16x32_bf16 v[8:11], v[192:195], v[168:171], v[8:11]\n"
    "v_mfma_f32_16x16x32_bf16 v[12:15], v[192:195], v[172:175], v[12:15]\n"
    "s_waitcnt vmcnt(0)\n"
    "s_add_u32 s52, s52, 1\n"
    "v_mov_b32 v233, s52\n"
    "s_mov_b32 s54, s52\n"
    "s_mov_b64 exec, 1\n"
    "ds_write_b32 v230, v233\n"
    "s_mov_b64 exec, -1\n"
    "v_mfma_f32_16x16x32_bf16 v[16:19], v[196:199], v[160:163], v[16:19]\n"
    "ds_read_b64_tr_b16 v[192:193], v218 offset:16384\n"
    "ds_read_b64_tr_b16 v[194:195], v218 offset:17408\n"
    "v_mfma_f32_16x16x32_bf16 v[20:23], v[196:199], v[164:167], v[20:23]\n"
    "v_mfma_f32_16x16x32_bf16 v[24:27], v[196:199], v[168:171], v[24:27]\n"
    "v_mfma_f32_16x16x32_bf16 v[28:31], v[196:199], v[172:175], v[28:31]\n"
    "v_mfma_f32_16x16x32_bf16 v[32:35], v[200:203], v[160:163], v[32:35]\n"
    "ds_read_b64_tr_b16 v[196:197], v218 offset:32768\n"
    "ds_read_b64_tr_b16 v[198:199], v218 offset:33792\n"
    "v_mfma_f32_16x16x32_bf16 v[36:39], v[200:203], v[164:167], v[36:39]\n"
    "ds_read_b128 v[180:183], v209 offset:34816\n"
    "v_mfma_f32_16x16x32_bf16 v[40:43], v[200:203], v[168:171], v[40:43]\n"
    "v_mfma_f32_16x16x32_bf16 v[44:47], v[200:203], v[172:175], v[44:47]\n"
    "s_waitcnt lgkmcnt(7)\n"
    "v_mfma_f32_16x16x32_bf16 v[48:51], v[204:207], v[160:163], v[48:51]\n"
    "ds_read_b32 v232, v231\n"
    "v_mfma_f32_16x16x32_bf16 v[52:55], v[204:207], v[164:167], v[52:55]\n"
    "ds_read_b64_tr_b16 v[200:201], v217 offset:0\n"
    "ds_read_b64_tr_b16 v[202:203], v217 offset:1024\n"
    "v_mfma_f32_16x16x32_bf16 v[56:59], v[204:207], v[168:171], v[56:59]\n"
    "v_mfma_f32_16x16x32_bf16 v[60:63], v[204:207], v[172:175], v[60:63]\n"
    "s_waitcnt lgkmcnt(6)\n"
    "s_waitcnt lgkmcnt(2)\n"
    "v_cmp_gt_u32 vcc, s54, v232\n"
    "s_cbranch_vccz 131f\n"
    "130:\n"
    "s_sleep 1\n"
    "ds_read_b32 v232, v231\n"
    "s_waitcnt lgkmcnt(0)\n"
    "v_cmp_gt_u32 vcc, s54, v232\n"
    "s_cbranch_vccnz 130b\n"
    "131:\n"
    "v_mfma_f32_16x16x32_bf16 v[64:67], v[192:195], v[160:163], v[64:67]\n"
    "ds_read_b64_tr_b16 v[204:205], v218 offset:0\n"
    "ds_read_b64_tr_b16 v[206:207], v218 offset:1024\n"
    "v_mfma_f32_16x16x32_bf16 v[68:71], v[192:195], v[164:167], v[68:71]\n"
    "ds_read_b128 v[184:187], v209 offset:36864\n"
    "v_mfma_f32_16x16x32_bf16 v[72:75], v[192:195], v[168:171], v[72:75]\n"
    "v_mfma_f32_16x16x32_bf16 v[76:79], v[192:195], v[172:175], v[76:79]\n"
    "v_mfma_f32_16x16x32_bf16 v[80:83], v[196:199], v[160:163], v[80:83]\n"
    "ds_read_b64_tr_b16 v[192:193], v220 offset:0\n"
    "ds_read_b64_tr_b16 v[194:195], v220 offset:1024\n"
    "v_mfma_f32_16x16x32_bf16 v[84:87], v[196:199], v[164:167], v[84:87]\n"
    "v_mfma_f32_16x16x32_bf16 v[88:91], v[196:199], v[168:171], v[88:91]\n"
    "v_mfma_f32_16x16x32_bf16 v[92:95], v[196:199], v[172:175], v[92:95]\n"
    "s_waitcnt lgkmcnt(5)\n"
    "v_mfma_f32_16x16x32_bf16 v[96:99], v[200:203], v[160:163], v[96:99]\n"
    "ds_read_b64_tr_b16 v[196:197], v221 offset:0\n"
    "ds_read_b64_tr_b16 v[198:199], v221 offset:1024\n"
    "v_mfma_f32_16x16x32_bf16 v[100:103], v[200:203], v[164:167], v[100:103]\n"
    "ds_read_b128 v[188:191], v209 offset:38912\n"
    "v_mfma_f32_16x16x32_bf16 v[104:107], v[200:203], v[168:171], v[104:107]\n"
    "v_mfma_f32_16x16x32_bf16 v[108:111], v[200:203], v[172:175], v[108:111]\n"
    "s_waitcnt lgkmcnt(6)\n"
    "v_mfma_f32_16x16x32_bf16 v[112:115], v[204:207], v[160:163], v[112:115]\n"
    "ds_read_b64_tr_b16 v[200:201], v216 offset:24576\n"
    "ds_read_b64_tr_b16 v[202:203], v216 offset:25600\n"
    "v_mfma_f32_16x16x32_bf16 v[116:119], v[204:207], v[164:167], v[116:119]\n"
    "v_mfma_f32_16x16x32_bf16 v[120:123], v[204:207], v[168:171], v[120:123]\n"
    "v_mfma_f32_16x16x32_bf16 v[124:127], v[204:207], v[172:175], v[124:127]\n"
    "s_waitcnt lgkmcnt(5)\n"
    "v_mfma_f32_16x16x32_bf16 v[128:131], v[192:195], v[160:163], v[128:131]\n"
    "ds_read_b64_tr_b16 v[204:205], v216 offset:40960\n"
    "ds_read_b64_tr_b16 v[206:207], v216 offset:41984\n"
    "v_mfma_f32_16x16x32_bf16 v[132:135], v[192:195], v[164:167], v[132:135]\n"
    "s_add_u32 s40, s40, 128\n"
    "s_addc_u32 s41, s41, 0\n"
    "s_add_u32 s42, s42, 393216\n"
    "s_addc_u32 s43, s43, 0\n"
    "s_add_u32 s44, s44, 458752\n"
    "s_addc_u32 s45, s45, 0\n"
    "s_add_u32 s46, s46, 458752\n"
    "s_addc_u32 s47, s47, 0\n"
    "v_mfma_f32_16x16x32_bf16 v[136:139], v[192:195], v[168:171], v[136:139]\n"
    "v_mfma_f32_16x16x32_bf16 v[140:143], v[192:195], v[172:175], v[140:143]\n"
    "s_waitcnt lgkmcnt(5)\n"
    "v_mfma_f32_16x16x32_bf16 v[144:147], v[196:199], v[160:163], v[144:147]\n"
    "ds_read_b64_tr_b16 v[192:193], v217 offset:24576\n"
    "ds_read_b64_tr_b16 v[194:195], v217 offset:25600\n"
    "v_mfma_f32_16x16x32_bf16 v[148:151], v[196:199], v[164:167], v[148:151]\n"
    "v_mfma_f32_16x16x32_bf16 v[152:155], v[196:199], v[168:171], v[152:155]\n"
    "v_mfma_f32_16x16x32_bf16 v[156:159], v[196:199], v[172:175], v[156:159]\n"
    "s_waitcnt lgkmcnt(4)\n"
    "v_mfma_f32_16x16x32_bf16 v[0:3], v[200:203], v[176:179], v[0:3]\n"
    "ds_read_b64_tr_b16 v[196:197], v217 offset:40960\n"
    "ds_read_b64_tr_b16 v[198:199], v217 offset:41984\n"
    "v_mfma_f32_16x16x32_bf16 v[4:7], v[200:203], v[180:183], v[4:7]\n"
    "v_mfma_f32_16x16x32_bf16 v[8:11], v[200:203], v[184:187], v[8:11]\n"
    "v_mfma_f32_16x16x32_bf16 v[12:15], v[200:203], v[188:191], v[12:15]\n"
    "s_waitcnt lgkmcnt(4)\n"
    "v_mfma_f32_16x16x32_bf16 v[16:19], v[204:207], v[176:179], v[16:19]\n"
    "ds_read_b64_tr_b16 v[200:201], v218 offset:24576\n"
    "ds_read_b64_tr_b16 v[202:203], v218 offset:25600\n"
    "v_mfma_f32_16x16x32_bf16 v[20:23], v[204:207], v[180:183], v[20:23]\n"
    "v_mfma_f32_16x16x32_bf16 v[24:27], v[204:207], v[184:187], v[24:27]\n"
    "v_mfma_f32_16x16x32_bf16 v[28:31], v[204:207], v[188:191], v[28:31]\n"
    "s_waitcnt lgkmcnt(4)\n"
    "v_mfma_f32_16x16x32_bf16 v[32:35], v[192:195], v[176:179], v[32:35]\n"
    "ds_read_b64_tr_b16 v[204:205], v218 offset:40960\n"
    "ds_read_b64_tr_b16 v[206:207], v218 offset:41984\n"
    "v_mfma_f32_16x16x32_bf16 v[36:39], v[192:195], v[180:183], v[36:39]\n"
    "v_mfma_f32_16x16x32_bf16 v[40:43], v[192:195], v[184:187], v[40:43]\n"
    "v_mfma_f32_16x16x32_bf16 v[44:47], v[192:195], v[188:191], v[44:47]\n"
    "s_waitcnt lgkmcnt(4)\n"
    "v_mfma_f32_16x16x32_bf16 v[48:51], v[196:199], v[176:179], v[48:51]\n"
    "ds_read_b64_tr_b16 v[192:193], v217 offset:8192\n"
    "ds_read_b64_tr_b16 v[194:195], v217 offset:9216\n"
    "v_mfma_f32_16x16x32_bf16 v[52:55], v[196:199], v[180:183], v[52:55]\n"
    "v_mfma_f32_16x16x32_bf16 v[56:59], v[196:199], v[184:187], v[56:59]\n"
    "v_mfma_f32_16x16x32_bf16 v[60:63], v[196:199], v[188:191], v[60:63]\n"
    "s_waitcnt lgkmcnt(4)\n"
    "v_mfma_f32_16x16x32_bf16 v[64:67], v[200:203], v[176:179], v[64:67]\n"
    "ds_read_b64_tr_b16 v[196:197], v218 offset:8192\n"
    "ds_read_b64_tr_b16 v[198:199], v218 offset:9216\n"
    "v_mfma_f32_16x16x32_bf16 v[68:71], v[200:203], v[180:183], v[68:71]\n"
    "v_mfma_f32_16x16x32_bf16 v[72:75], v[200:203], v[184:187], v[72:75]\n"
    "v_mfma_f32_16x16x32_bf16 v[76:79], v[200:203], v[188:191], v[76:79]\n"
    "s_waitcnt lgkmcnt(4)\n"
    "v_mfma_f32_16x16x32_bf16 v[80:83], v[204:207], v[176:179], v[80:83]\n"
    "ds_read_b64_tr_b16 v[200:201], v220 offset:8192\n"
    "ds_read_b64_tr_b16 v[202:203], v220 offset:9216\n"
    "v_mfma_f32_16x16x32_bf16 v[84:87], v[204:207], v[180:183], v[84:87]\n"
    "v_mfma_f32_16x16x32_bf16 v[88:91], v[204:207], v[184:187], v[88:91]\n"
    "v_mfma_f32_16x16x32_bf16 v[92:95], v[204:207], v[188:191], v[92:95]\n"
    "s_waitcnt lgkmcnt(4)\n"
    "v_mfma_f32_16x16x32_bf16 v[96:99], v[192:195], v[176:179], v[96:99]\n"
    "ds_read_b64_tr_b16 v[204:205], v221 offset:8192\n"
    "ds_read_b64_tr_b16 v[206:207], v221 offset:9216\n"
    "v_mfma_f32_16x16x32_bf16 v[100:103], v[192:195], v[180:183], v[100:103]\n"
    "v_mfma_f32_16x16x32_bf16 v[104:107], v[192:195], v[184:187], v[104:107]\n"
    "v_mfma_f32_16x16x32_bf16 v[108:111], v[192:195], v[188:191], v[108:111]\n"
    "s_waitcnt lgkmcnt(4)\n"
    "v_mfma_f32_16x16x32_bf16 v[112:115], v[196:199], v[176:179], v[112:115]\n"
    "v_mfma_f32_16x16x32_bf16 v[116:119], v[196:199], v[180:183], v[116:119]\n"
    "v_mfma_f32_16x16x32_bf16 v[120:123], v[196:199], v[184:187], v[120:123]\n"
    "v_mfma_f32_16x16x32_bf16 v[124:127], v[196:199], v[188:191], v[124:127]\n"
    "s_waitcnt lgkmcnt(2)\n"
    "v_mfma_f32_16x16x32_bf16 v[128:131], v[200:203], v[176:179], v[128:131]\n"
    "v_mfma_f32_16x16x32_bf16 v[132:135], v[200:203], v[180:183], v[132:135]\n"
    "v_mfma_f32_16x16x32_bf16 v[136:139], v[200:203], v[184:187], v[136:139]\n"
    "v_mfma_f32_16x16x32_bf16 v[140:143], v[200:203], v[188:191], v[140:143]\n"
    "s_waitcnt lgkmcnt(0)\n"
    "v_mfma_f32_16x16x32_bf16 v[144:147], v[204:207], v[176:179], v[144:147]\n"
    "v_mfma_f32_16x16x32_bf16 v[148:151], v[204:207], v[180:183], v[148:151]\n"
    "v_mfma_f32_16x16x32_bf16 v[152:155], v[204:207], v[184:187], v[152:155]\n"
    "v_mfma_f32_16x16x32_bf16 v[156:159], v[204:207], v[188:191], v[156:159]\n"
    "s_nop 15\n"

        : "={v[0:15]}"(acc[0]), "={v[16:31]}"(acc[1]), "={v[32:47]}"(acc[2]), "={v[48:63]}"(acc[3]), "={v[64:79]}"(acc[4]), "={v[80:95]}"(acc[5]), "={v[96:111]}"(acc[6]),
          "={v[112:127]}"(acc[7]), "={v[128:143]}"(acc[8]), "={v[144:159]}"(acc[9]), "+{v[224:239]}"(v1), "+{s[40:47]}"(s0)
        : "{v[208:223]}"(v0), "{s[48:51]}"(s1)
        : "memory", "scc", "vcc", "s52", "s53", "s54", "s56", "s57", "s58",
          "v160", "v161", "v162", "v163", "v164", "v165", "v166", "v167", "v168", "v169", "v170", "v171", "v172", "v173", "v174", "v175", "v176", "v177", "v178", "v179",
          "v180", "v181", "v182", "v183", "v184", "v185", "v186", "v187", "v188", "v189", "v190", "v191", "v192", "v193", "v194", "v195", "v196", "v197", "v198", "v199",
          "v200", "v201", "v202", "v203", "v204", "v205", "v206", "v207");
}

constexpr int LDS_Q = 65536;
constexpr int LDS_H = 81920;
constexpr int LDS_G = LDS_H + 256 * 224;
constexpr int LDS_VT = 139520;
DI bool epilogue(const Params& a, const unsigned lds0, const f32x16 (&accv)[10], const int w, const int rb, const int ct, const unsigned tag, const float gpart) {
    const int lane = lane_id();
    const int wr = w & 3, wc = w >> 2, fr = lane & 15, fq = lane >> 4;
    auto acc = [&](int m, int n) { return (f32x4){accv[n][4 * m], accv[n][4 * m + 1], accv[n][4 * m + 2], accv[n][4 * m + 3]}; };
    const int row0 = rb * 256 + wr * 64 + fr;
    const int cperm = ((fq & 1) << 4) | ((fq >> 1) << 3);
    asm volatile("s_barrier" ::: "memory");

    {
        auto pack = [&](int m, int nlo, int nhi, float sc) {
            const f32x4 lo = acc(m, nlo) * sc, hi = acc(m, nhi) * sc;
            auto r0 = __builtin_amdgcn_permlane16_swap(cvtpk(lo[0], lo[1]), cvtpk(hi[0], hi[1]), false, false);
            auto r1 = __builtin_amdgcn_permlane16_swap(cvtpk(lo[2], lo[3]), cvtpk(hi[2], hi[3]), false, false);
            return (u32x4){r0[0], r1[0], r0[1], r1[1]}; };
        const int lrow = (wr & 1) * 64 + fr;
        const unsigned slot = lds0 + (wr >> 1) * 16384;
        bf16_t* gdst = a.kv + (size_t)ct * (S_ * 32) + (size_t)row0 * 32 + cperm;
        if (wc) {
#pragma unroll
            for (int m = 0; m < 4; ++m) *(LAS u32x4*)(size_t)(lds0 + LDS_VT + (wr * 64 + m * 16 + fr) * 64 + cperm * 2) = pack(m, 6, 7, 1.0f);
        } else {
#pragma unroll
            for (int m = 0; m < 4; ++m) { const u32x4 v = pack(m, 7, 9, 1.0f); st16w(gdst + m * 16 * 32, v);
                *(LAS u32x4*)(size_t)(slot + (lrow + m * 16) * 64 + (((cperm >> 3) ^ (((lrow + m * 16) >> 2) & 3)) * 16)) = v; }
#pragma unroll
            for (int m = 0; m < 4; ++m) *(LAS u32x4*)(size_t)(lds0 + LDS_Q + (wr * 64 + m * 16 + fr) * 64 + cperm * 2) = pack(m, 8, 6, QSCALE);
        }
    }
    auto swiglu = [&](int m, int ng, unsigned (&o)[2]) {
        float hv[4];
#pragma unroll
        for (int e = 0; e < 4; ++e) { const float gt = accv[ng][4 * m + e], up = accv[ng + 1][4 * m + e]; hv[e] = gt * up * __builtin_amdgcn_rcpf(1.0f + __builtin_amdgcn_exp2f(-1.4426950408889634f * gt)); }
        o[0] = cvtpk(hv[0], hv[1]); o[1] = cvtpk(hv[2], hv[3]);
    };
    auto hrow = [&](int m) {
        unsigned p0[2], p1[2], p2[2], p3[2];
        swiglu(m, 0, p0); swiglu(m, 2, p1); swiglu(m, 4, p2);
        const unsigned hp = lds0 + LDS_H + (wr * 64 + m * 16 + fr) * 224 + wc * 96;
        {   auto r0 = __builtin_amdgcn_permlane16_swap(p0[0], p1[0], false, false);
            auto r1 = __builtin_amdgcn_permlane16_swap(p0[1], p1[1], false, false);
            *(LAS u32x4*)(size_t)(hp + cperm * 2) = (u32x4){r0[0], r1[0], r0[1], r1[1]}; }
        if (wc) {
            swiglu(m, 8, p3);
            auto r0 = __builtin_amdgcn_permlane16_swap(p2[0], p3[0], false, false);
            auto r1 = __builtin_amdgcn_permlane16_swap(p2[1], p3[1], false, false);
            *(LAS u32x4*)(size_t)(hp + 64 + cperm * 2) = (u32x4){r0[0], r1[0], r0[1], r1[1]};
        } else {
            *(LAS u32x2*)(size_t)(hp + 64 + fq * 8) = (u32x2){p2[0], p2[1]};
        }
    };
    SBAR();
    hrow(0);
    SBAR();
    asm volatile("s_waitcnt lgkmcnt(0)\n\ts_barrier" ::: "memory");
    {
        const int g = lane >> 4, i16 = lane & 15;
#pragma unroll
        for (int j = 0; j < 2; ++j) {
            const int d = 16 * j + i16;
            const unsigned src = lds0 + LDS_VT + (w * 32 + g * 8 + (i16 >> 2)) * 64 + j * 32 + (i16 & 3) * 8;
            const bf16x8 v = cat8(ldtr(LP(src)), ldtr(LP(src) + 256));
            const int ch = (w & 3) * 4 + g, tl = w >> 2;
            st16w((char*)(a.kv + (size_t)(32 + ct) * (S_ * 32)) + (size_t)(rb * 2 + tl) * 8192 + d * 256 + ch * 16, __builtin_bit_cast(u32x4, v));
            *(LAS bf16x8*)(size_t)(lds0 + tl * 16384 + 8192 + d * 256 + ((ch ^ (d & 15)) * 16)) = v;
        }
    }
    SBAR();

    float gsum = gpart;
#pragma unroll
    for (int o = 32; o >= 1; o >>= 1) gsum += __shfl_xor(gsum, o);
    hrow(1);
    const float gval = 1.0f / (1.0f + __expf(-gsum));
    SBAR();

    asm volatile("s_waitcnt vmcnt(0) lgkmcnt(0)\n\ts_barrier" ::: "memory");

    if (w == 0 && lane == 0) flag_set(a.flags + FLAG_Q + ct * 8 + rb, tag);
    if (lane == 0) *(LAS float*)(size_t)(lds0 + LDS_G + w * 4) = gval;
    unsigned kvf = 0;
    if (w == 4 || w == 5) kvf = flag_get(a.flags + FLAG_Q + ct * 8 + (lane & 7));
    SBAR();
    hrow(2);
    SBAR();
    bool early = false;
    if (w == 4 || w == 5) {
        early = __builtin_amdgcn_ballot_w64((lane & 7) != rb && kvf != tag) == 0ull;
        if (early) { p2::kv_dma(a, lds0, ct, rb, w - 4, lane, 2); p2::kv_dma(a, lds0, ct, rb, w - 4, lane, 3); }
    }
    SBAR();
    hrow(3);

    asm volatile("s_waitcnt lgkmcnt(0)" ::: "memory");
    return early;
}
}

namespace p3 { constexpr int KSPLIT = 2752; DI void producer_prologue(const Params& a, const unsigned lds0, const int pw, const int lane); }
namespace p2 {
constexpr int SLOT = 16384, NTILE = S_ / 128;

struct Ctx { int h, qb, rb3h; unsigned tag; };

#define VMB(N) asm volatile("s_waitcnt vmcnt(" #N ")\n\ts_barrier" ::: "memory")
DI void kv_dma(const Params& a, const unsigned lds0, const int h, const int qb, const int hw, const int lane, const int T) {
    const char* gK = (const char*)(a.kv + (size_t)h * (S_ * 32)) + hw * 4096;
    const char* gV = (const char*)(a.kv + (size_t)(32 + h) * (S_ * 32)) + hw * 4096;
    const unsigned voffK = (unsigned)((lane >> 2) * 64 + (((lane & 3) ^ ((lane >> 4) & 3)) * 16));
    const size_t off = (size_t)((((qb + (T >> 1)) & 7) * 2 + (T & 1)) * 8192);
#pragma unroll
    for (int i = 0; i < 4; ++i) glds16c(voffK, gK + off + i * 1024, lds0 + hw * 4096 + (T & 3) * SLOT + i * 1024);
#pragma unroll
    for (int i = 0; i < 4; ++i) glds16c((unsigned)((lane >> 4) * 256 + (((lane & 15) ^ (4 * i + (lane >> 4))) * 16)), gV + off + i * 1024, lds0 + 8192 + hw * 4096 + (T & 3) * SLOT + i * 1024);
}
DI void helper(const Params& a, const unsigned lds0, const Ctx& c, const int w, const bool early) {
    const int lane = lane_id();
    const int hw = w - 4, h = c.h;
    if (hw < 2) {
        const char* gK = (const char*)(a.kv + (size_t)h * (S_ * 32)) + hw * 4096;
        const char* gV = (const char*)(a.kv + (size_t)(32 + h) * (S_ * 32)) + hw * 4096;
        const unsigned voffK = (unsigned)((lane >> 2) * 64 + (((lane & 3) ^ ((lane >> 4) & 3)) * 16));
        unsigned voffV[4];
#pragma unroll
        for (int i = 0; i < 4; ++i) voffV[i] = (unsigned)((lane >> 4) * 256 + (((lane & 15) ^ (4 * i + (lane >> 4))) * 16));
        const unsigned dK = lds0 + hw * 4096, dV = lds0 + 8192 + hw * 4096;
#define P2_KOFF(T) ((size_t)((((c.qb + ((T) >> 1)) & 7) * 2 + ((T) & 1)) * 8192))
#define P2_DMA(T) do { _Pragma("unroll") for (int i = 0; i < 4; ++i) glds16c(voffK, gK + P2_KOFF(T) + i * 1024, dK + ((T) & 3) * SLOT + i * 1024); \
                       _Pragma("unroll") for (int i = 0; i < 4; ++i) glds16c(voffV[i], gV + P2_KOFF(T) + i * 1024, dV + ((T) & 3) * SLOT + i * 1024); } while (0)
        if (!early) {
            wait_flags(a.flags + FLAG_Q + h * 8 + (lane & 7), c.tag, (lane & 7) == c.qb);
            P2_DMA(2); P2_DMA(3);
        }
#pragma unroll 1
        for (int k = 1; k <= 12; ++k) { VMB(8); P2_DMA(k + 3); }
        VMB(8);
        VMB(0);
#undef P2_DMA
    } else {
        const int sw = hw - 2;
        const char* hb = (const char*)(a.ho + (size_t)(c.qb * 256 + sw * 128) * HO_LD + h * 112);
        unsigned hoff[7];
#pragma unroll
        for (int i = 0; i < 7; ++i) { const int ch = i * 64 + lane, row = (ch * 4682) >> 16, col = ch - row * 14; hoff[i] = (unsigned)(row * (HO_LD * 2) + col * 16); asm volatile("" : "+v"(hoff[i])); }
        const unsigned hsrc = lds0 + p1::LDS_H + sw * (128 * 224) + lane * 16;
        const unsigned pfoff = (unsigned)((lane >> 3) * 2048 + (lane & 7) * 128);
#define P2_HST(i) do { const u32x4 v = *(const LAS u32x4*)(LP(hsrc) + (i) * 1024); \
            asm volatile("global_store_dwordx4 %0, %1, %2 sc1\n\ts_nop 1" :: "v"(hoff[(i) % 7]), "v"(v), "s"(hb + (size_t)((i) / 7) * 32 * (HO_LD * 2)) : "memory"); } while (0)
        unsigned pfd = 0;
        auto pf = [&](int i) {
            const int xg = blockIdx.x & 7, j = blockIdx.x >> 3, kh = (xg & 1) ^ 1, cg = (xg >> 1) & 1, half = kh ? 29 : 43, last = (kh ? 1856 : 2752) - 8;
            int r8 = (((j * 2 + sw) * half) & ~7) + i * 8; r8 = r8 > last ? last : r8;
            const int k = (kh ? p3::KSPLIT : 0) + r8;
            const char* base = (k < F_ ? (const char*)a.wd + (size_t)k * 2048 : (const char*)a.wo + (size_t)(k - F_) * 2048) + cg * 1024;
            asm volatile("global_load_dword %0, %1, %2" : "+v"(pfd) : "v"(pfoff), "s"(base) : "memory"); };
        asm volatile("s_barrier" ::: "memory");
#pragma unroll
        for (int k = 0; k < 7; ++k) { P2_HST(4 * k); P2_HST(4 * k + 1); P2_HST(4 * k + 2); P2_HST(4 * k + 3); if (k < 6) { pf(k); asm volatile("s_barrier" ::: "memory"); } }
#undef P2_HST
        const unsigned gv = *(const LAS unsigned*)(size_t)(lds0 + p1::LDS_G + (sw * 4 + (lane & 3)) * 4);
        if (lane < 4) st4w(a.g + c.qb * 256 + h * 8 + sw * 4 + lane, gv);
        asm volatile("s_barrier" ::: "memory");
        asm volatile("s_barrier" ::: "memory");
        asm volatile("s_waitcnt vmcnt(0)\n\ts_barrier" : "+v"(pfd) :: "memory");
        if (sw == 0 && lane == 0) flag_set(a.flags + FLAG_H + c.qb * 32 + h, c.tag);
        asm volatile("s_barrier" ::: "memory");
        asm volatile("s_barrier" ::: "memory");
        asm volatile("s_barrier" ::: "memory");
        wait_flags(a.flags + FLAG_H + (lane < 32 ? c.rb3h * 32 + lane : c.qb * 32 + (lane - 32)), c.tag);
        asm volatile("s_waitcnt vmcnt(0)\n\ts_barrier" ::: "memory");
    }
    p3::producer_prologue(a, lds0, hw, lane);
}

DI unsigned addr_k(const unsigned lds0, const int lane, const int s) { const int r = lane & 31, hh = lane >> 5, pr = (r & 19) | ((r & 4) << 1) | ((r & 8) >> 1);
    return lds0 + pr * 64 + (((2 * s + hh) ^ ((pr >> 2) & 3)) * 16); }
DI unsigned addr_v(const unsigned lds0, const int lane, const int u, const int s) { const int d = lane & 31, hh = lane >> 5;
    return lds0 + d * 256 + (((4 * u + 2 * s + hh) ^ (d & 15)) * 16); }

DI bool attn_fast(const Params& a, const unsigned lds0, const Ctx& c, const int w, f32x16& oa, f32x16& ob, float& la, float& lb, float& ga, float& gb, bf16x8 (&q)[2][2]) {
    const int lane = lane_id();
    const int r = lane & 31, hh = lane >> 5;
#pragma unroll
    for (int b = 0; b < 2; ++b)
#pragma unroll
        for (int s = 0; s < 2; ++s) q[b][s] = ld128(LP(lds0 + p1::LDS_Q + (w * 64 + b * 32 + r) * 64 + s * 32 + hh * 16));
    const unsigned rK0 = addr_k(lds0, lane, 0), rK1 = addr_k(lds0, lane, 1);
    const short one = (short)0x3f80; const bool sel = ((lane & 15) == ((lane >> 4) & 1));
    const short sv = sel ? one : (short)0; bf16x8 selA = {sv, sv, sv, sv, sv, sv, sv, sv};
    f32x4 lsa = {0.f, 0.f, 0.f, 0.f}, lsb = {0.f, 0.f, 0.f, 0.f};
    f32x16 z;
#pragma unroll
    for (int i = 0; i < 16; ++i) z[i] = 0.f;
    oa = z; ob = z;

    bf16x8 k0 = ld128(LP(rK0)), k1 = ld128(LP(rK1));
    f32x16 nma, nmb, sa, sb;
    {
        f32x16 s = MFMA32(k0, q[0][0], z); s = MFMA32(k1, q[0][1], s);
        f32x16 t = MFMA32(k0, q[1][0], z); t = MFMA32(k1, q[1][1], t);
        float m = s[0], n = t[0];
#pragma unroll
        for (int i = 1; i < 16; ++i) { m = __builtin_fmaxf(m, s[i]); n = __builtin_fmaxf(n, t[i]); }
        auto rm = __builtin_amdgcn_permlane32_swap(__float_as_uint(m), __float_as_uint(m), false, false);
        auto rn = __builtin_amdgcn_permlane32_swap(__float_as_uint(n), __float_as_uint(n), false, false);
        m = __builtin_fmaxf(__uint_as_float(rm[0]), __uint_as_float(rm[1])); n = __builtin_fmaxf(__uint_as_float(rn[0]), __uint_as_float(rn[1]));
#pragma unroll
        for (int i = 0; i < 16; ++i) { nma[i] = -m; nmb[i] = -n; }
    }
    sa = MFMA32(k0, q[0][0], nma); sa = MFMA32(k1, q[0][1], sa);
    sb = z;
    u32x4 pz = {0u, 0u, 0u, 0u}, pb0 = pz, pb1 = pz;
    bf16x8 vz0 = {0, 0, 0, 0, 0, 0, 0, 0}, vz1 = vz0;
    unsigned rv[8];
#pragma unroll
    for (int i = 0; i < 8; ++i) rv[i] = addr_v(lds0, lane, i >> 1, i & 1);
    const float* gp = a.g + c.qb * 256 + w * 64 + r;
    asm volatile("s_nop 15\n"
    "s_setprio 2\n"
    "s_mov_b32 s40, 0\n"
    "9:\n"
    "v_cvt_pk_bf16_f32 v151, v94, v95\n"
    "v_mfma_f32_32x32x16_bf16 v[80:95], v[96:99], v[128:131], v[48:63]\n"
    "v_exp_f32 v64, v64\n"
    "v_exp_f32 v65, v65\n"
    "v_exp_f32 v66, v66\n"
    "v_mfma_f32_32x32x16_bf16 v[80:95], v[100:103], v[132:135], v[80:95]\n"
    "ds_read_b128 v[96:99], v164 offset:2048\n"
    "ds_read_b128 v[100:103], v165 offset:2048\n"
    "v_exp_f32 v67, v67\n"
    "v_cvt_pk_bf16_f32 v136, v64, v65\n"
    "v_exp_f32 v68, v68\n"
    "v_exp_f32 v69, v69\n"
    "v_mfma_f32_32x32x16_bf16 v[16:31], v[112:115], v[144:147], v[16:31]\n"
    "ds_read_b128 v[104:107], v166 offset:8192\n"
    "ds_read_b128 v[108:111], v167 offset:8192\n"
    "v_cvt_pk_bf16_f32 v137, v66, v67\n"
    "v_exp_f32 v70, v70\n"
    "v_exp_f32 v71, v71\n"
    "v_cvt_pk_bf16_f32 v138, v68, v69\n"
    "v_mfma_f32_16x16x32_bf16 v[160:163], v[152:155], v[144:147], v[160:163]\n"
    "v_exp_f32 v72, v72\n"
    "v_exp_f32 v73, v73\n"
    "v_mfma_f32_32x32x16_bf16 v[16:31], v[116:119], v[148:151], v[16:31]\n"
    "v_cvt_pk_bf16_f32 v139, v70, v71\n"
    "v_exp_f32 v74, v74\n"
    "v_exp_f32 v75, v75\n"
    "v_cvt_pk_bf16_f32 v140, v72, v73\n"
    "v_mfma_f32_16x16x32_bf16 v[160:163], v[152:155], v[148:151], v[160:163]\n"
    "v_exp_f32 v76, v76\n"
    "v_exp_f32 v77, v77\n"
    "v_cvt_pk_bf16_f32 v141, v74, v75\n"
    "v_exp_f32 v78, v78\n"
    "v_exp_f32 v79, v79\n"
    "v_cvt_pk_bf16_f32 v142, v76, v77\n"
    "s_waitcnt lgkmcnt(0)\n"
    "v_cvt_pk_bf16_f32 v143, v78, v79\n"
    "v_mfma_f32_32x32x16_bf16 v[64:79], v[96:99], v[120:123], v[32:47]\n"
    "v_exp_f32 v80, v80\n"
    "v_exp_f32 v81, v81\n"
    "v_exp_f32 v82, v82\n"
    "v_mfma_f32_32x32x16_bf16 v[64:79], v[100:103], v[124:127], v[64:79]\n"
    "v_exp_f32 v83, v83\n"
    "v_cvt_pk_bf16_f32 v144, v80, v81\n"
    "v_exp_f32 v84, v84\n"
    "v_exp_f32 v85, v85\n"
    "v_mfma_f32_32x32x16_bf16 v[0:15], v[104:107], v[136:139], v[0:15]\n"
    "v_cvt_pk_bf16_f32 v145, v82, v83\n"
    "v_exp_f32 v86, v86\n"
    "v_exp_f32 v87, v87\n"
    "v_cvt_pk_bf16_f32 v146, v84, v85\n"
    "v_mfma_f32_16x16x32_bf16 v[156:159], v[152:155], v[136:139], v[156:159]\n"
    "v_exp_f32 v88, v88\n"
    "v_exp_f32 v89, v89\n"
    "v_mfma_f32_32x32x16_bf16 v[0:15], v[108:111], v[140:143], v[0:15]\n"
    "v_cvt_pk_bf16_f32 v147, v86, v87\n"
    "v_exp_f32 v90, v90\n"
    "v_exp_f32 v91, v91\n"
    "v_cvt_pk_bf16_f32 v148, v88, v89\n"
    "v_mfma_f32_16x16x32_bf16 v[156:159], v[152:155], v[140:143], v[156:159]\n"
    "v_exp_f32 v92, v92\n"
    "v_exp_f32 v93, v93\n"
    "v_cvt_pk_bf16_f32 v149, v90, v91\n"
    "v_exp_f32 v94, v94\n"
    "v_exp_f32 v95, v95\n"
    "v_cvt_pk_bf16_f32 v150, v92, v93\n"
    "v_cvt_pk_bf16_f32 v151, v94, v95\n"
    "v_mfma_f32_32x32x16_bf16 v[80:95], v[96:99], v[128:131], v[48:63]\n"
    "v_exp_f32 v64, v64\n"
    "v_exp_f32 v65, v65\n"
    "v_exp_f32 v66, v66\n"
    "v_mfma_f32_32x32x16_bf16 v[80:95], v[100:103], v[132:135], v[80:95]\n"
    "ds_read_b128 v[96:99], v164 offset:4096\n"
    "ds_read_b128 v[100:103], v165 offset:4096\n"
    "v_exp_f32 v67, v67\n"
    "v_cvt_pk_bf16_f32 v136, v64, v65\n"
    "v_exp_f32 v68, v68\n"
    "v_exp_f32 v69, v69\n"
    "v_mfma_f32_32x32x16_bf16 v[16:31], v[104:107], v[144:147], v[16:31]\n"
    "ds_read_b128 v[112:115], v168 offset:8192\n"
    "ds_read_b128 v[116:119], v169 offset:8192\n"
    "v_cvt_pk_bf16_f32 v137, v66, v67\n"
    "v_exp_f32 v70, v70\n"
    "v_exp_f32 v71, v71\n"
    "v_cvt_pk_bf16_f32 v138, v68, v69\n"
    "v_mfma_f32_16x16x32_bf16 v[160:163], v[152:155], v[144:147], v[160:163]\n"
    "v_exp_f32 v72, v72\n"
    "v_exp_f32 v73, v73\n"
    "v_mfma_f32_32x32x16_bf16 v[16:31], v[108:111], v[148:151], v[16:31]\n"
    "v_cvt_pk_bf16_f32 v139, v70, v71\n"
    "v_exp_f32 v74, v74\n"
    "v_exp_f32 v75, v75\n"
    "v_cvt_pk_bf16_f32 v140, v72, v73\n"
    "v_mfma_f32_16x16x32_bf16 v[160:163], v[152:155], v[148:151], v[160:163]\n"
    "v_exp_f32 v76, v76\n"
    "v_exp_f32 v77, v77\n"
    "v_cvt_pk_bf16_f32 v141, v74, v75\n"
    "v_exp_f32 v78, v78\n"
    "v_exp_f32 v79, v79\n"
    "v_cvt_pk_bf16_f32 v142, v76, v77\n"
    "s_waitcnt lgkmcnt(0)\n"
    "v_cvt_pk_bf16_f32 v143, v78, v79\n"
    "v_mfma_f32_32x32x16_bf16 v[64:79], v[96:99], v[120:123], v[32:47]\n"
    "v_exp_f32 v80, v80\n"
    "v_exp_f32 v81, v81\n"
    "v_exp_f32 v82, v82\n"
    "v_mfma_f32_32x32x16_bf16 v[64:79], v[100:103], v[124:127], v[64:79]\n"
    "v_exp_f32 v83, v83\n"
    "v_cvt_pk_bf16_f32 v144, v80, v81\n"
    "v_exp_f32 v84, v84\n"
    "v_exp_f32 v85, v85\n"
    "v_mfma_f32_32x32x16_bf16 v[0:15], v[112:115], v[136:139], v[0:15]\n"
    "v_cvt_pk_bf16_f32 v145, v82, v83\n"
    "v_exp_f32 v86, v86\n"
    "v_exp_f32 v87, v87\n"
    "v_cvt_pk_bf16_f32 v146, v84, v85\n"
    "v_mfma_f32_16x16x32_bf16 v[156:159], v[152:155], v[136:139], v[156:159]\n"
    "v_exp_f32 v88, v88\n"
    "v_exp_f32 v89, v89\n"
    "v_mfma_f32_32x32x16_bf16 v[0:15], v[116:119], v[140:143], v[0:15]\n"
    "v_cvt_pk_bf16_f32 v147, v86, v87\n"
    "v_exp_f32 v90, v90\n"
    "v_exp_f32 v91, v91\n"
    "v_cvt_pk_bf16_f32 v148, v88, v89\n"
    "v_mfma_f32_16x16x32_bf16 v[156:159], v[152:155], v[140:143], v[156:159]\n"
    "v_exp_f32 v92, v92\n"
    "v_exp_f32 v93, v93\n"
    "v_cvt_pk_bf16_f32 v149, v90, v91\n"
    "v_exp_f32 v94, v94\n"
    "v_exp_f32 v95, v95\n"
    "v_cvt_pk_bf16_f32 v150, v92, v93\n"
    "v_cvt_pk_bf16_f32 v151, v94, v95\n"
    "v_mfma_f32_32x32x16_bf16 v[80:95], v[96:99], v[128:131], v[48:63]\n"
    "v_exp_f32 v64, v64\n"
    "v_exp_f32 v65, v65\n"
    "v_exp_f32 v66, v66\n"
    "v_mfma_f32_32x32x16_bf16 v[80:95], v[100:103], v[132:135], v[80:95]\n"
    "ds_read_b128 v[96:99], v164 offset:6144\n"
    "ds_read_b128 v[100:103], v165 offset:6144\n"
    "v_exp_f32 v67, v67\n"
    "v_cvt_pk_bf16_f32 v136, v64, v65\n"
    "v_exp_f32 v68, v68\n"
    "v_exp_f32 v69, v69\n"
    "v_mfma_f32_32x32x16_bf16 v[16:31], v[112:115], v[144:147], v[16:31]\n"
    "ds_read_b128 v[104:107], v170 offset:8192\n"
    "ds_read_b128 v[108:111], v171 offset:8192\n"
    "v_cvt_pk_bf16_f32 v137, v66, v67\n"
    "v_exp_f32 v70, v70\n"
    "v_exp_f32 v71, v71\n"
    "v_cvt_pk_bf16_f32 v138, v68, v69\n"
    "v_mfma_f32_16x16x32_bf16 v[160:163], v[152:155], v[144:147], v[160:163]\n"
    "v_exp_f32 v72, v72\n"
    "v_exp_f32 v73, v73\n"
    "v_mfma_f32_32x32x16_bf16 v[16:31], v[116:119], v[148:151], v[16:31]\n"
    "v_cvt_pk_bf16_f32 v139, v70, v71\n"
    "v_exp_f32 v74, v74\n"
    "v_exp_f32 v75, v75\n"
    "v_cvt_pk_bf16_f32 v140, v72, v73\n"
    "v_mfma_f32_16x16x32_bf16 v[160:163], v[152:155], v[148:151], v[160:163]\n"
    "v_exp_f32 v76, v76\n"
    "v_exp_f32 v77, v77\n"
    "v_cvt_pk_bf16_f32 v141, v74, v75\n"
    "v_exp_f32 v78, v78\n"
    "v_exp_f32 v79, v79\n"
    "v_cvt_pk_bf16_f32 v142, v76, v77\n"
    "s_waitcnt lgkmcnt(0)\n"
    "v_cvt_pk_bf16_f32 v143, v78, v79\n"
    "v_mfma_f32_32x32x16_bf16 v[64:79], v[96:99], v[120:123], v[32:47]\n"
    "v_exp_f32 v80, v80\n"
    "v_exp_f32 v81, v81\n"
    "v_exp_f32 v82, v82\n"
    "v_mfma_f32_32x32x16_bf16 v[64:79], v[100:103], v[124:127], v[64:79]\n"
    "v_exp_f32 v83, v83\n"
    "v_cvt_pk_bf16_f32 v144, v80, v81\n"
    "v_exp_f32 v84, v84\n"
    "v_exp_f32 v85, v85\n"
    "v_mfma_f32_32x32x16_bf16 v[0:15], v[104:107], v[136:139], v[0:15]\n"
    "v_cvt_pk_bf16_f32 v145, v82, v83\n"
    "v_exp_f32 v86, v86\n"
    "v_exp_f32 v87, v87\n"
    "v_cvt_pk_bf16_f32 v146, v84, v85\n"
    "v_mfma_f32_16x16x32_bf16 v[156:159], v[152:155], v[136:139], v[156:159]\n"
    "v_exp_f32 v88, v88\n"
    "v_exp_f32 v89, v89\n"
    "v_mfma_f32_32x32x16_bf16 v[0:15], v[108:111], v[140:143], v[0:15]\n"
    "v_cvt_pk_bf16_f32 v147, v86, v87\n"
    "v_exp_f32 v90, v90\n"
    "v_exp_f32 v91, v91\n"
    "v_cvt_pk_bf16_f32 v148, v88, v89\n"
    "v_mfma_f32_16x16x32_bf16 v[156:159], v[152:155], v[140:143], v[156:159]\n"
    "v_exp_f32 v92, v92\n"
    "v_exp_f32 v93, v93\n"
    "v_cvt_pk_bf16_f32 v149, v90, v91\n"
    "v_exp_f32 v94, v94\n"
    "v_exp_f32 v95, v95\n"
    "v_cvt_pk_bf16_f32 v150, v92, v93\n"
    "s_cmp_eq_u32 s40, 0\n"
    "s_cbranch_scc1 11f\n"
    "s_barrier\n"
    "11:\n"
    "v_cvt_pk_bf16_f32 v151, v94, v95\n"
    "v_mfma_f32_32x32x16_bf16 v[80:95], v[96:99], v[128:131], v[48:63]\n"
    "v_exp_f32 v64, v64\n"
    "v_exp_f32 v65, v65\n"
    "v_exp_f32 v66, v66\n"
    "v_mfma_f32_32x32x16_bf16 v[80:95], v[100:103], v[132:135], v[80:95]\n"
    "ds_read_b128 v[96:99], v164 offset:16384\n"
    "ds_read_b128 v[100:103], v165 offset:16384\n"
    "v_exp_f32 v67, v67\n"
    "v_cvt_pk_bf16_f32 v136, v64, v65\n"
    "v_exp_f32 v68, v68\n"
    "v_exp_f32 v69, v69\n"
    "v_mfma_f32_32x32x16_bf16 v[16:31], v[104:107], v[144:147], v[16:31]\n"
    "ds_read_b128 v[112:115], v172 offset:8192\n"
    "ds_read_b128 v[116:119], v173 offset:8192\n"
    "v_cvt_pk_bf16_f32 v137, v66, v67\n"
    "v_exp_f32 v70, v70\n"
    "v_exp_f32 v71, v71\n"
    "v_cvt_pk_bf16_f32 v138, v68, v69\n"
    "v_mfma_f32_16x16x32_bf16 v[160:163], v[152:155], v[144:147], v[160:163]\n"
    "v_exp_f32 v72, v72\n"
    "v_exp_f32 v73, v73\n"
    "v_mfma_f32_32x32x16_bf16 v[16:31], v[108:111], v[148:151], v[16:31]\n"
    "v_cvt_pk_bf16_f32 v139, v70, v71\n"
    "v_exp_f32 v74, v74\n"
    "v_exp_f32 v75, v75\n"
    "v_cvt_pk_bf16_f32 v140, v72, v73\n"
    "v_mfma_f32_16x16x32_bf16 v[160:163], v[152:155], v[148:151], v[160:163]\n"
    "v_exp_f32 v76, v76\n"
    "v_exp_f32 v77, v77\n"
    "v_cvt_pk_bf16_f32 v141, v74, v75\n"
    "v_exp_f32 v78, v78\n"
    "v_exp_f32 v79, v79\n"
    "v_cvt_pk_bf16_f32 v142, v76, v77\n"
    "s_waitcnt lgkmcnt(0)\n"
    "v_cvt_pk_bf16_f32 v143, v78, v79\n"
    "v_mfma_f32_32x32x16_bf16 v[64:79], v[96:99], v[120:123], v[32:47]\n"
    "v_exp_f32 v80, v80\n"
    "v_exp_f32 v81, v81\n"
    "v_exp_f32 v82, v82\n"
    "v_mfma_f32_32x32x16_bf16 v[64:79], v[100:103], v[124:127], v[64:79]\n"
    "v_exp_f32 v83, v83\n"
    "v_cvt_pk_bf16_f32 v144, v80, v81\n"
    "v_exp_f32 v84, v84\n"
    "v_exp_f32 v85, v85\n"
    "v_mfma_f32_32x32x16_bf16 v[0:15], v[112:115], v[136:139], v[0:15]\n"
    "v_cvt_pk_bf16_f32 v145, v82, v83\n"
    "v_exp_f32 v86, v86\n"
    "v_exp_f32 v87, v87\n"
    "v_cvt_pk_bf16_f32 v146, v84, v85\n"
    "v_mfma_f32_16x16x32_bf16 v[156:159], v[152:155], v[136:139], v[156:159]\n"
    "v_exp_f32 v88, v88\n"
    "v_exp_f32 v89, v89\n"
    "v_mfma_f32_32x32x16_bf16 v[0:15], v[116:119], v[140:143], v[0:15]\n"
    "v_cvt_pk_bf16_f32 v147, v86, v87\n"
    "v_exp_f32 v90, v90\n"
    "v_exp_f32 v91, v91\n"
    "v_cvt_pk_bf16_f32 v148, v88, v89\n"
    "v_mfma_f32_16x16x32_bf16 v[156:159], v[152:155], v[140:143], v[156:159]\n"
    "v_exp_f32 v92, v92\n"
    "v_exp_f32 v93, v93\n"
    "v_cvt_pk_bf16_f32 v149, v90, v91\n"
    "v_exp_f32 v94, v94\n"
    "v_exp_f32 v95, v95\n"
    "v_cvt_pk_bf16_f32 v150, v92, v93\n"
    "v_cvt_pk_bf16_f32 v151, v94, v95\n"
    "v_mfma_f32_32x32x16_bf16 v[80:95], v[96:99], v[128:131], v[48:63]\n"
    "v_exp_f32 v64, v64\n"
    "v_exp_f32 v65, v65\n"
    "v_exp_f32 v66, v66\n"
    "v_mfma_f32_32x32x16_bf16 v[80:95], v[100:103], v[132:135], v[80:95]\n"
    "ds_read_b128 v[96:99], v164 offset:18432\n"
    "ds_read_b128 v[100:103], v165 offset:18432\n"
    "v_exp_f32 v67, v67\n"
    "v_cvt_pk_bf16_f32 v136, v64, v65\n"
    "v_exp_f32 v68, v68\n"
    "v_exp_f32 v69, v69\n"
    "v_mfma_f32_32x32x16_bf16 v[16:31], v[112:115], v[144:147], v[16:31]\n"
    "ds_read_b128 v[104:107], v166 offset:24576\n"
    "ds_read_b128 v[108:111], v167 offset:24576\n"
    "v_cvt_pk_bf16_f32 v137, v66, v67\n"
    "v_exp_f32 v70, v70\n"
    "v_exp_f32 v71, v71\n"
    "v_cvt_pk_bf16_f32 v138, v68, v69\n"
    "v_mfma_f32_16x16x32_bf16 v[160:163], v[152:155], v[144:147], v[160:163]\n"
    "v_exp_f32 v72, v72\n"
    "v_exp_f32 v73, v73\n"
    "v_mfma_f32_32x32x16_bf16 v[16:31], v[116:119], v[148:151], v[16:31]\n"
    "v_cvt_pk_bf16_f32 v139, v70, v71\n"
    "v_exp_f32 v74, v74\n"
    "v_exp_f32 v75, v75\n"
    "v_cvt_pk_bf16_f32 v140, v72, v73\n"
    "v_mfma_f32_16x16x32_bf16 v[160:163], v[152:155], v[148:151], v[160:163]\n"
    "v_exp_f32 v76, v76\n"
    "v_exp_f32 v77, v77\n"
    "v_cvt_pk_bf16_f32 v141, v74, v75\n"
    "v_exp_f32 v78, v78\n"
    "v_exp_f32 v79, v79\n"
    "v_cvt_pk_bf16_f32 v142, v76, v77\n"
    "s_waitcnt lgkmcnt(0)\n"
    "v_cvt_pk_bf16_f32 v143, v78, v79\n"
    "v_mfma_f32_32x32x16_bf16 v[64:79], v[96:99], v[120:123], v[32:47]\n"
    "v_exp_f32 v80, v80\n"
    "v_exp_f32 v81, v81\n"
    "v_exp_f32 v82, v82\n"
    "v_mfma_f32_32x32x16_bf16 v[64:79], v[100:103], v[124:127], v[64:79]\n"
    "v_exp_f32 v83, v83\n"
    "v_cvt_pk_bf16_f32 v144, v80, v81\n"
    "v_exp_f32 v84, v84\n"
    "v_exp_f32 v85, v85\n"
    "v_mfma_f32_32x32x16_bf16 v[0:15], v[104:107], v[136:139], v[0:15]\n"
    "v_cvt_pk_bf16_f32 v145, v82, v83\n"
    "v_exp_f32 v86, v86\n"
    "v_exp_f32 v87, v87\n"
    "v_cvt_pk_bf16_f32 v146, v84, v85\n"
    "v_mfma_f32_16x16x32_bf16 v[156:159], v[152:155], v[136:139], v[156:159]\n"
    "v_exp_f32 v88, v88\n"
    "v_exp_f32 v89, v89\n"
    "v_mfma_f32_32x32x16_bf16 v[0:15], v[108:111], v[140:143], v[0:15]\n"
    "v_cvt_pk_bf16_f32 v147, v86, v87\n"
    "v_exp_f32 v90, v90\n"
    "v_exp_f32 v91, v91\n"
    "v_cvt_pk_bf16_f32 v148, v88, v89\n"
    "v_mfma_f32_16x16x32_bf16 v[156:159], v[152:155], v[140:143], v[156:159]\n"
    "v_exp_f32 v92, v92\n"
    "v_exp_f32 v93, v93\n"
    "v_cvt_pk_bf16_f32 v149, v90, v91\n"
    "v_exp_f32 v94, v94\n"
    "v_exp_f32 v95, v95\n"
    "v_cvt_pk_bf16_f32 v150, v92, v93\n"
    "v_cvt_pk_bf16_f32 v151, v94, v95\n"
    "v_mfma_f32_32x32x16_bf16 v[80:95], v[96:99], v[128:131], v[48:63]\n"
    "v_exp_f32 v64, v64\n"
    "v_exp_f32 v65, v65\n"
    "v_exp_f32 v66, v66\n"
    "v_mfma_f32_32x32x16_bf16 v[80:95], v[100:103], v[132:135], v[80:95]\n"
    "ds_read_b128 v[96:99], v164 offset:20480\n"
    "ds_read_b128 v[100:103], v165 offset:20480\n"
    "v_exp_f32 v67, v67\n"
    "v_cvt_pk_bf16_f32 v136, v64, v65\n"
    "v_exp_f32 v68, v68\n"
    "v_exp_f32 v69, v69\n"
    "v_mfma_f32_32x32x16_bf16 v[16:31], v[104:107], v[144:147], v[16:31]\n"
    "ds_read_b128 v[112:115], v168 offset:24576\n"
    "ds_read_b128 v[116:119], v169 offset:24576\n"
    "v_cvt_pk_bf16_f32 v137, v66, v67\n"
    "v_exp_f32 v70, v70\n"
    "v_exp_f32 v71, v71\n"
    "v_cvt_pk_bf16_f32 v138, v68, v69\n"
    "v_mfma_f32_16x16x32_bf16 v[160:163], v[152:155], v[144:147], v[160:163]\n"
    "v_exp_f32 v72, v72\n"
    "v_exp_f32 v73, v73\n"
    "v_mfma_f32_32x32x16_bf16 v[16:31], v[108:111], v[148:151], v[16:31]\n"
    "v_cvt_pk_bf16_f32 v139, v70, v71\n"
    "v_exp_f32 v74, v74\n"
    "v_exp_f32 v75, v75\n"
    "v_cvt_pk_bf16_f32 v140, v72, v73\n"
    "v_mfma_f32_16x16x32_bf16 v[160:163], v[152:155], v[148:151], v[160:163]\n"
    "v_exp_f32 v76, v76\n"
    "v_exp_f32 v77, v77\n"
    "v_cvt_pk_bf16_f32 v141, v74, v75\n"
    "v_exp_f32 v78, v78\n"
    "v_exp_f32 v79, v79\n"
    "v_cvt_pk_bf16_f32 v142, v76, v77\n"
    "s_waitcnt lgkmcnt(0)\n"
    "v_cvt_pk_bf16_f32 v143, v78, v79\n"
    "v_mfma_f32_32x32x16_bf16 v[64:79], v[96:99], v[120:123], v[32:47]\n"
    "v_exp_f32 v80, v80\n"
    "v_exp_f32 v81, v81\n"
    "v_exp_f32 v82, v82\n"
    "v_mfma_f32_32x32x16_bf16 v[64:79], v[100:103], v[124:127], v[64:79]\n"
    "v_exp_f32 v83, v83\n"
    "v_cvt_pk_bf16_f32 v144, v80, v81\n"
    "v_exp_f32 v84, v84\n"
    "v_exp_f32 v85, v85\n"
    "v_mfma_f32_32x32x16_bf16 v[0:15], v[112:115], v[136:139], v[0:15]\n"
    "v_cvt_pk_bf16_f32 v145, v82, v83\n"
    "v_exp_f32 v86, v86\n"
    "v_exp_f32 v87, v87\n"
    "v_cvt_pk_bf16_f32 v146, v84, v85\n"
    "v_mfma_f32_16x16x32_bf16 v[156:159], v[152:155], v[136:139], v[156:159]\n"
    "v_exp_f32 v88, v88\n"
    "v_exp_f32 v89, v89\n"
    "v_mfma_f32_32x32x16_bf16 v[0:15], v[116:119], v[140:143], v[0:15]\n"
    "v_cvt_pk_bf16_f32 v147, v86, v87\n"
    "v_exp_f32 v90, v90\n"
    "v_exp_f32 v91, v91\n"
    "v_cvt_pk_bf16_f32 v148, v88, v89\n"
    "v_mfma_f32_16x16x32_bf16 v[156:159], v[152:155], v[140:143], v[156:159]\n"
    "v_exp_f32 v92, v92\n"
    "v_exp_f32 v93, v93\n"
    "v_cvt_pk_bf16_f32 v149, v90, v91\n"
    "v_exp_f32 v94, v94\n"
    "v_exp_f32 v95, v95\n"
    "v_cvt_pk_bf16_f32 v150, v92, v93\n"
    "v_cvt_pk_bf16_f32 v151, v94, v95\n"
    "v_mfma_f32_32x32x16_bf16 v[80:95], v[96:99], v[128:131], v[48:63]\n"
    "v_exp_f32 v64, v64\n"
    "v_exp_f32 v65, v65\n"
    "v_exp_f32 v66, v66\n"
    "v_mfma_f32_32x32x16_bf16 v[80:95], v[100:103], v[132:135], v[80:95]\n"
    "ds_read_b128 v[96:99], v164 offset:22528\n"
    "ds_read_b128 v[100:103], v165 offset:22528\n"
    "v_exp_f32 v67, v67\n"
    "v_cvt_pk_bf16_f32 v136, v64, v65\n"
    "v_exp_f32 v68, v68\n"
    "v_exp_f32 v69, v69\n"
    "v_mfma_f32_32x32x16_bf16 v[16:31], v[112:115], v[144:147], v[16:31]\n"
    "ds_read_b128 v[104:107], v170 offset:24576\n"
    "ds_read_b128 v[108:111], v171 offset:24576\n"
    "v_cvt_pk_bf16_f32 v137, v66, v67\n"
    "v_exp_f32 v70, v70\n"
    "v_exp_f32 v71, v71\n"
    "v_cvt_pk_bf16_f32 v138, v68, v69\n"
    "v_mfma_f32_16x16x32_bf16 v[160:163], v[152:155], v[144:147], v[160:163]\n"
    "v_exp_f32 v72, v72\n"
    "v_exp_f32 v73, v73\n"
    "v_mfma_f32_32x32x16_bf16 v[16:31], v[116:119], v[148:151], v[16:31]\n"
    "v_cvt_pk_bf16_f32 v139, v70, v71\n"
    "v_exp_f32 v74, v74\n"
    "v_exp_f32 v75, v75\n"
    "v_cvt_pk_bf16_f32 v140, v72, v73\n"
    "v_mfma_f32_16x16x32_bf16 v[160:163], v[152:155], v[148:151], v[160:163]\n"
    "v_exp_f32 v76, v76\n"
    "v_exp_f32 v77, v77\n"
    "v_cvt_pk_bf16_f32 v141, v74, v75\n"
    "v_exp_f32 v78, v78\n"
    "v_exp_f32 v79, v79\n"
    "v_cvt_pk_bf16_f32 v142, v76, v77\n"
    "s_waitcnt lgkmcnt(0)\n"
    "v_cvt_pk_bf16_f32 v143, v78, v79\n"
    "v_mfma_f32_32x32x16_bf16 v[64:79], v[96:99], v[120:123], v[32:47]\n"
    "v_exp_f32 v80, v80\n"
    "v_exp_f32 v81, v81\n"
    "v_exp_f32 v82, v82\n"
    "v_mfma_f32_32x32x16_bf16 v[64:79], v[100:103], v[124:127], v[64:79]\n"
    "v_exp_f32 v83, v83\n"
    "v_cvt_pk_bf16_f32 v144, v80, v81\n"
    "v_exp_f32 v84, v84\n"
    "v_exp_f32 v85, v85\n"
    "v_mfma_f32_32x32x16_bf16 v[0:15], v[104:107], v[136:139], v[0:15]\n"
    "v_cvt_pk_bf16_f32 v145, v82, v83\n"
    "v_exp_f32 v86, v86\n"
    "v_exp_f32 v87, v87\n"
    "v_cvt_pk_bf16_f32 v146, v84, v85\n"
    "v_mfma_f32_16x16x32_bf16 v[156:159], v[152:155], v[136:139], v[156:159]\n"
    "v_exp_f32 v88, v88\n"
    "v_exp_f32 v89, v89\n"
    "v_mfma_f32_32x32x16_bf16 v[0:15], v[108:111], v[140:143], v[0:15]\n"
    "v_cvt_pk_bf16_f32 v147, v86, v87\n"
    "v_exp_f32 v90, v90\n"
    "v_exp_f32 v91, v91\n"
    "v_cvt_pk_bf16_f32 v148, v88, v89\n"
    "v_mfma_f32_16x16x32_bf16 v[156:159], v[152:155], v[140:143], v[156:159]\n"
    "v_exp_f32 v92, v92\n"
    "v_exp_f32 v93, v93\n"
    "v_cvt_pk_bf16_f32 v149, v90, v91\n"
    "v_exp_f32 v94, v94\n"
    "v_exp_f32 v95, v95\n"
    "v_cvt_pk_bf16_f32 v150, v92, v93\n"
    "s_barrier\n"
    "v_cvt_pk_bf16_f32 v151, v94, v95\n"
    "v_mfma_f32_32x32x16_bf16 v[80:95], v[96:99], v[128:131], v[48:63]\n"
    "v_exp_f32 v64, v64\n"
    "v_exp_f32 v65, v65\n"
    "v_exp_f32 v66, v66\n"
    "v_mfma_f32_32x32x16_bf16 v[80:95], v[100:103], v[132:135], v[80:95]\n"
    "ds_read_b128 v[96:99], v164 offset:32768\n"
    "ds_read_b128 v[100:103], v165 offset:32768\n"
    "v_exp_f32 v67, v67\n"
    "v_cvt_pk_bf16_f32 v136, v64, v65\n"
    "v_exp_f32 v68, v68\n"
    "v_exp_f32 v69, v69\n"
    "v_mfma_f32_32x32x16_bf16 v[16:31], v[104:107], v[144:147], v[16:31]\n"
    "ds_read_b128 v[112:115], v172 offset:24576\n"
    "ds_read_b128 v[116:119], v173 offset:24576\n"
    "v_cvt_pk_bf16_f32 v137, v66, v67\n"
    "v_exp_f32 v70, v70\n"
    "v_exp_f32 v71, v71\n"
    "v_cvt_pk_bf16_f32 v138, v68, v69\n"
    "v_mfma_f32_16x16x32_bf16 v[160:163], v[152:155], v[144:147], v[160:163]\n"
    "v_exp_f32 v72, v72\n"
    "v_exp_f32 v73, v73\n"
    "v_mfma_f32_32x32x16_bf16 v[16:31], v[108:111], v[148:151], v[16:31]\n"
    "v_cvt_pk_bf16_f32 v139, v70, v71\n"
    "v_exp_f32 v74, v74\n"
    "v_exp_f32 v75, v75\n"
    "v_cvt_pk_bf16_f32 v140, v72, v73\n"
    "v_mfma_f32_16x16x32_bf16 v[160:163], v[152:155], v[148:151], v[160:163]\n"
    "v_exp_f32 v76, v76\n"
    "v_exp_f32 v77, v77\n"
    "v_cvt_pk_bf16_f32 v141, v74, v75\n"
    "v_exp_f32 v78, v78\n"
    "v_exp_f32 v79, v79\n"
    "v_cvt_pk_bf16_f32 v142, v76, v77\n"
    "s_waitcnt lgkmcnt(0)\n"
    "v_cvt_pk_bf16_f32 v143, v78, v79\n"
    "v_mfma_f32_32x32x16_bf16 v[64:79], v[96:99], v[120:123], v[32:47]\n"
    "v_exp_f32 v80, v80\n"
    "v_exp_f32 v81, v81\n"
    "v_exp_f32 v82, v82\n"
    "v_mfma_f32_32x32x16_bf16 v[64:79], v[100:103], v[124:127], v[64:79]\n"
    "v_exp_f32 v83, v83\n"
    "v_cvt_pk_bf16_f32 v144, v80, v81\n"
    "v_exp_f32 v84, v84\n"
    "v_exp_f32 v85, v85\n"
    "v_mfma_f32_32x32x16_bf16 v[0:15], v[112:115], v[136:139], v[0:15]\n"
    "v_cvt_pk_bf16_f32 v145, v82, v83\n"
    "v_exp_f32 v86, v86\n"
    "v_exp_f32 v87, v87\n"
    "v_cvt_pk_bf16_f32 v146, v84, v85\n"
    "v_mfma_f32_16x16x32_bf16 v[156:159], v[152:155], v[136:139], v[156:159]\n"
    "v_exp_f32 v88, v88\n"
    "v_exp_f32 v89, v89\n"
    "v_mfma_f32_32x32x16_bf16 v[0:15], v[116:119], v[140:143], v[0:15]\n"
    "v_cvt_pk_bf16_f32 v147, v86, v87\n"
    "v_exp_f32 v90, v90\n"
    "v_exp_f32 v91, v91\n"
    "v_cvt_pk_bf16_f32 v148, v88, v89\n"
    "v_mfma_f32_16x16x32_bf16 v[156:159], v[152:155], v[140:143], v[156:159]\n"
    "v_exp_f32 v92, v92\n"
    "v_exp_f32 v93, v93\n"
    "v_cvt_pk_bf16_f32 v149, v90, v91\n"
    "v_exp_f32 v94, v94\n"
    "v_exp_f32 v95, v95\n"
    "v_cvt_pk_bf16_f32 v150, v92, v93\n"
    "v_cvt_pk_bf16_f32 v151, v94, v95\n"
    "v_mfma_f32_32x32x16_bf16 v[80:95], v[96:99], v[128:131], v[48:63]\n"
    "v_exp_f32 v64, v64\n"
    "v_exp_f32 v65, v65\n"
    "v_exp_f32 v66, v66\n"
    "v_mfma_f32_32x32x16_bf16 v[80:95], v[100:103], v[132:135], v[80:95]\n"
    "ds_read_b128 v[96:99], v164 offset:34816\n"
    "ds_read_b128 v[100:103], v165 offset:34816\n"
    "v_exp_f32 v67, v67\n"
    "v_cvt_pk_bf16_f32 v136, v64, v65\n"
    "v_exp_f32 v68, v68\n"
    "v_exp_f32 v69, v69\n"
    "v_mfma_f32_32x32x16_bf16 v[16:31], v[112:115], v[144:147], v[16:31]\n"
    "ds_read_b128 v[104:107], v166 offset:40960\n"
    "ds_read_b128 v[108:111], v167 offset:40960\n"
    "v_cvt_pk_bf16_f32 v137, v66, v67\n"
    "v_exp_f32 v70, v70\n"
    "v_exp_f32 v71, v71\n"
    "v_cvt_pk_bf16_f32 v138, v68, v69\n"
    "v_mfma_f32_16x16x32_bf16 v[160:163], v[152:155], v[144:147], v[160:163]\n"
    "v_exp_f32 v72, v72\n"
    "v_exp_f32 v73, v73\n"
    "v_mfma_f32_32x32x16_bf16 v[16:31], v[116:119], v[148:151], v[16:31]\n"
    "v_cvt_pk_bf16_f32 v139, v70, v71\n"
    "v_exp_f32 v74, v74\n"
    "v_exp_f32 v75, v75\n"
    "v_cvt_pk_bf16_f32 v140, v72, v73\n"
    "v_mfma_f32_16x16x32_bf16 v[160:163], v[152:155], v[148:151], v[160:163]\n"
    "v_exp_f32 v76, v76\n"
    "v_exp_f32 v77, v77\n"
    "v_cvt_pk_bf16_f32 v141, v74, v75\n"
    "v_exp_f32 v78, v78\n"
    "v_exp_f32 v79, v79\n"
    "v_cvt_pk_bf16_f32 v142, v76, v77\n"
    "s_waitcnt lgkmcnt(0)\n"
    "v_cvt_pk_bf16_f32 v143, v78, v79\n"
    "v_mfma_f32_32x32x16_bf16 v[64:79], v[96:99], v[120:123], v[32:47]\n"
    "v_exp_f32 v80, v80\n"
    "v_exp_f32 v81, v81\n"
    "v_exp_f32 v82, v82\n"
    "v_mfma_f32_32x32x16_bf16 v[64:79], v[100:103], v[124:127], v[64:79]\n"
    "v_exp_f32 v83, v83\n"
    "v_cvt_pk_bf16_f32 v144, v80, v81\n"
    "v_exp_f32 v84, v84\n"
    "v_exp_f32 v85, v85\n"
    "v_mfma_f32_32x32x16_bf16 v[0:15], v[104:107], v[136:139], v[0:15]\n"
    "v_cvt_pk_bf16_f32 v145, v82, v83\n"
    "v_exp_f32 v86, v86\n"
    "v_exp_f32 v87, v87\n"
    "v_cvt_pk_bf16_f32 v146, v84, v85\n"
    "v_mfma_f32_16x16x32_bf16 v[156:159], v[152:155], v[136:139], v[156:159]\n"
    "v_exp_f32 v88, v88\n"
    "v_exp_f32 v89, v89\n"
    "v_mfma_f32_32x32x16_bf16 v[0:15], v[108:111], v[140:143], v[0:15]\n"
    "v_cvt_pk_bf16_f32 v147, v86, v87\n"
    "v_exp_f32 v90, v90\n"
    "v_exp_f32 v91, v91\n"
    "v_cvt_pk_bf16_f32 v148, v88, v89\n"
    "v_mfma_f32_16x16x32_bf16 v[156:159], v[152:155], v[140:143], v[156:159]\n"
    "v_exp_f32 v92, v92\n"
    "v_exp_f32 v93, v93\n"
    "v_cvt_pk_bf16_f32 v149, v90, v91\n"
    "v_exp_f32 v94, v94\n"
    "v_exp_f32 v95, v95\n"
    "v_cvt_pk_bf16_f32 v150, v92, v93\n"
    "v_cvt_pk_bf16_f32 v151, v94, v95\n"
    "v_mfma_f32_32x32x16_bf16 v[80:95], v[96:99], v[128:131], v[48:63]\n"
    "v_exp_f32 v64, v64\n"
    "v_exp_f32 v65, v65\n"
    "v_exp_f32 v66, v66\n"
    "v_mfma_f32_32x32x16_bf16 v[80:95], v[100:103], v[132:135], v[80:95]\n"
    "ds_read_b128 v[96:99], v164 offset:36864\n"
    "ds_read_b128 v[100:103], v165 offset:36864\n"
    "v_exp_f32 v67, v67\n"
    "v_cvt_pk_bf16_f32 v136, v64, v65\n"
    "v_exp_f32 v68, v68\n"
    "v_exp_f32 v69, v69\n"
    "v_mfma_f32_32x32x16_bf16 v[16:31], v[104:107], v[144:147], v[16:31]\n"
    "ds_read_b128 v[112:115], v168 offset:40960\n"
    "ds_read_b128 v[116:119], v169 offset:40960\n"
    "v_cvt_pk_bf16_f32 v137, v66, v67\n"
    "v_exp_f32 v70, v70\n"
    "v_exp_f32 v71, v71\n"
    "v_cvt_pk_bf16_f32 v138, v68, v69\n"
    "v_mfma_f32_16x16x32_bf16 v[160:163], v[152:155], v[144:147], v[160:163]\n"
    "v_exp_f32 v72, v72\n"
    "v_exp_f32 v73, v73\n"
    "v_mfma_f32_32x32x16_bf16 v[16:31], v[108:111], v[148:151], v[16:31]\n"
    "v_cvt_pk_bf16_f32 v139, v70, v71\n"
    "v_exp_f32 v74, v74\n"
    "v_exp_f32 v75, v75\n"
    "v_cvt_pk_bf16_f32 v140, v72, v73\n"
    "v_mfma_f32_16x16x32_bf16 v[160:163], v[152:155], v[148:151], v[160:163]\n"
    "v_exp_f32 v76, v76\n"
    "v_exp_f32 v77, v77\n"
    "v_cvt_pk_bf16_f32 v141, v74, v75\n"
    "v_exp_f32 v78, v78\n"
    "v_exp_f32 v79, v79\n"
    "v_cvt_pk_bf16_f32 v142, v76, v77\n"
    "s_waitcnt lgkmcnt(0)\n"
    "v_cvt_pk_bf16_f32 v143, v78, v79\n"
    "v_mfma_f32_32x32x16_bf16 v[64:79], v[96:99], v[120:123], v[32:47]\n"
    "v_exp_f32 v80, v80\n"
    "v_exp_f32 v81, v81\n"
    "v_exp_f32 v82, v82\n"
    "v_mfma_f32_32x32x16_bf16 v[64:79], v[100:103], v[124:127], v[64:79]\n"
    "v_exp_f32 v83, v83\n"
    "v_cvt_pk_bf16_f32 v144, v80, v81\n"
    "v_exp_f32 v84, v84\n"
    "v_exp_f32 v85, v85\n"
    "v_mfma_f32_32x32x16_bf16 v[0:15], v[112:115], v[136:139], v[0:15]\n"
    "v_cvt_pk_bf16_f32 v145, v82, v83\n"
    "v_exp_f32 v86, v86\n"
    "v_exp_f32 v87, v87\n"
    "v_cvt_pk_bf16_f32 v146, v84, v85\n"
    "v_mfma_f32_16x16x32_bf16 v[156:159], v[152:155], v[136:139], v[156:159]\n"
    "v_exp_f32 v88, v88\n"
    "v_exp_f32 v89, v89\n"
    "v_mfma_f32_32x32x16_bf16 v[0:15], v[116:119], v[140:143], v[0:15]\n"
    "v_cvt_pk_bf16_f32 v147, v86, v87\n"
    "v_exp_f32 v90, v90\n"
    "v_exp_f32 v91, v91\n"
    "v_cvt_pk_bf16_f32 v148, v88, v89\n"
    "v_mfma_f32_16x16x32_bf16 v[156:159], v[152:155], v[140:143], v[156:159]\n"
    "v_exp_f32 v92, v92\n"
    "v_exp_f32 v93, v93\n"
    "v_cvt_pk_bf16_f32 v149, v90, v91\n"
    "v_exp_f32 v94, v94\n"
    "v_exp_f32 v95, v95\n"
    "v_cvt_pk_bf16_f32 v150, v92, v93\n"
    "v_cvt_pk_bf16_f32 v151, v94, v95\n"
    "v_mfma_f32_32x32x16_bf16 v[80:95], v[96:99], v[128:131], v[48:63]\n"
    "v_exp_f32 v64, v64\n"
    "v_exp_f32 v65, v65\n"
    "v_exp_f32 v66, v66\n"
    "v_mfma_f32_32x32x16_bf16 v[80:95], v[100:103], v[132:135], v[80:95]\n"
    "ds_read_b128 v[96:99], v164 offset:38912\n"
    "ds_read_b128 v[100:103], v165 offset:38912\n"
    "v_exp_f32 v67, v67\n"
    "v_cvt_pk_bf16_f32 v136, v64, v65\n"
    "v_exp_f32 v68, v68\n"
    "v_exp_f32 v69, v69\n"
    "v_mfma_f32_32x32x16_bf16 v[16:31], v[112:115], v[144:147], v[16:31]\n"
    "ds_read_b128 v[104:107], v170 offset:40960\n"
    "ds_read_b128 v[108:111], v171 offset:40960\n"
    "v_cvt_pk_bf16_f32 v137, v66, v67\n"
    "v_exp_f32 v70, v70\n"
    "v_exp_f32 v71, v71\n"
    "v_cvt_pk_bf16_f32 v138, v68, v69\n"
    "v_mfma_f32_16x16x32_bf16 v[160:163], v[152:155], v[144:147], v[160:163]\n"
    "v_exp_f32 v72, v72\n"
    "v_exp_f32 v73, v73\n"
    "v_mfma_f32_32x32x16_bf16 v[16:31], v[116:119], v[148:151], v[16:31]\n"
    "v_cvt_pk_bf16_f32 v139, v70, v71\n"
    "v_exp_f32 v74, v74\n"
    "v_exp_f32 v75, v75\n"
    "v_cvt_pk_bf16_f32 v140, v72, v73\n"
    "v_mfma_f32_16x16x32_bf16 v[160:163], v[152:155], v[148:151], v[160:163]\n"
    "v_exp_f32 v76, v76\n"
    "v_exp_f32 v77, v77\n"
    "v_cvt_pk_bf16_f32 v141, v74, v75\n"
    "v_exp_f32 v78, v78\n"
    "v_exp_f32 v79, v79\n"
    "v_cvt_pk_bf16_f32 v142, v76, v77\n"
    "s_waitcnt lgkmcnt(0)\n"
    "v_cvt_pk_bf16_f32 v143, v78, v79\n"
    "v_mfma_f32_32x32x16_bf16 v[64:79], v[96:99], v[120:123], v[32:47]\n"
    "v_exp_f32 v80, v80\n"
    "v_exp_f32 v81, v81\n"
    "v_exp_f32 v82, v82\n"
    "v_mfma_f32_32x32x16_bf16 v[64:79], v[100:103], v[124:127], v[64:79]\n"
    "v_exp_f32 v83, v83\n"
    "v_cvt_pk_bf16_f32 v144, v80, v81\n"
    "v_exp_f32 v84, v84\n"
    "v_exp_f32 v85, v85\n"
    "v_mfma_f32_32x32x16_bf16 v[0:15], v[104:107], v[136:139], v[0:15]\n"
    "v_cvt_pk_bf16_f32 v145, v82, v83\n"
    "v_exp_f32 v86, v86\n"
    "v_exp_f32 v87, v87\n"
    "v_cvt_pk_bf16_f32 v146, v84, v85\n"
    "v_mfma_f32_16x16x32_bf16 v[156:159], v[152:155], v[136:139], v[156:159]\n"
    "v_exp_f32 v88, v88\n"
    "v_exp_f32 v89, v89\n"
    "v_mfma_f32_32x32x16_bf16 v[0:15], v[108:111], v[140:143], v[0:15]\n"
    "v_cvt_pk_bf16_f32 v147, v86, v87\n"
    "v_exp_f32 v90, v90\n"
    "v_exp_f32 v91, v91\n"
    "v_cvt_pk_bf16_f32 v148, v88, v89\n"
    "v_mfma_f32_16x16x32_bf16 v[156:159], v[152:155], v[140:143], v[156:159]\n"
    "v_exp_f32 v92, v92\n"
    "v_exp_f32 v93, v93\n"
    "v_cvt_pk_bf16_f32 v149, v90, v91\n"
    "v_exp_f32 v94, v94\n"
    "v_exp_f32 v95, v95\n"
    "v_cvt_pk_bf16_f32 v150, v92, v93\n"
    "s_barrier\n"
    "s_cmp_lg_u32 s40, 3\n"
    "s_cbranch_scc1 12f\n"
    "global_load_dword v176, v[174:175], off sc1\n"
    "global_load_dword v177, v[174:175], off offset:128 sc1\n"
    "12:\n"
    "v_cvt_pk_bf16_f32 v151, v94, v95\n"
    "v_mfma_f32_32x32x16_bf16 v[80:95], v[96:99], v[128:131], v[48:63]\n"
    "v_exp_f32 v64, v64\n"
    "v_exp_f32 v65, v65\n"
    "v_exp_f32 v66, v66\n"
    "v_mfma_f32_32x32x16_bf16 v[80:95], v[100:103], v[132:135], v[80:95]\n"
    "ds_read_b128 v[96:99], v164 offset:49152\n"
    "ds_read_b128 v[100:103], v165 offset:49152\n"
    "v_exp_f32 v67, v67\n"
    "v_cvt_pk_bf16_f32 v136, v64, v65\n"
    "v_exp_f32 v68, v68\n"
    "v_exp_f32 v69, v69\n"
    "v_mfma_f32_32x32x16_bf16 v[16:31], v[104:107], v[144:147], v[16:31]\n"
    "ds_read_b128 v[112:115], v172 offset:40960\n"
    "ds_read_b128 v[116:119], v173 offset:40960\n"
    "v_cvt_pk_bf16_f32 v137, v66, v67\n"
    "v_exp_f32 v70, v70\n"
    "v_exp_f32 v71, v71\n"
    "v_cvt_pk_bf16_f32 v138, v68, v69\n"
    "v_mfma_f32_16x16x32_bf16 v[160:163], v[152:155], v[144:147], v[160:163]\n"
    "v_exp_f32 v72, v72\n"
    "v_exp_f32 v73, v73\n"
    "v_mfma_f32_32x32x16_bf16 v[16:31], v[108:111], v[148:151], v[16:31]\n"
    "v_cvt_pk_bf16_f32 v139, v70, v71\n"
    "v_exp_f32 v74, v74\n"
    "v_exp_f32 v75, v75\n"
    "v_cvt_pk_bf16_f32 v140, v72, v73\n"
    "v_mfma_f32_16x16x32_bf16 v[160:163], v[152:155], v[148:151], v[160:163]\n"
    "v_exp_f32 v76, v76\n"
    "v_exp_f32 v77, v77\n"
    "v_cvt_pk_bf16_f32 v141, v74, v75\n"
    "v_exp_f32 v78, v78\n"
    "v_exp_f32 v79, v79\n"
    "v_cvt_pk_bf16_f32 v142, v76, v77\n"
    "s_waitcnt lgkmcnt(0)\n"
    "v_cvt_pk_bf16_f32 v143, v78, v79\n"
    "v_mfma_f32_32x32x16_bf16 v[64:79], v[96:99], v[120:123], v[32:47]\n"
    "v_exp_f32 v80, v80\n"
    "v_exp_f32 v81, v81\n"
    "v_exp_f32 v82, v82\n"
    "v_mfma_f32_32x32x16_bf16 v[64:79], v[100:103], v[124:127], v[64:79]\n"
    "v_exp_f32 v83, v83\n"
    "v_cvt_pk_bf16_f32 v144, v80, v81\n"
    "v_exp_f32 v84, v84\n"
    "v_exp_f32 v85, v85\n"
    "v_mfma_f32_32x32x16_bf16 v[0:15], v[112:115], v[136:139], v[0:15]\n"
    "v_cvt_pk_bf16_f32 v145, v82, v83\n"
    "v_exp_f32 v86, v86\n"
    "v_exp_f32 v87, v87\n"
    "v_cvt_pk_bf16_f32 v146, v84, v85\n"
    "v_mfma_f32_16x16x32_bf16 v[156:159], v[152:155], v[136:139], v[156:159]\n"
    "v_exp_f32 v88, v88\n"
    "v_exp_f32 v89, v89\n"
    "v_mfma_f32_32x32x16_bf16 v[0:15], v[116:119], v[140:143], v[0:15]\n"
    "v_cvt_pk_bf16_f32 v147, v86, v87\n"
    "v_exp_f32 v90, v90\n"
    "v_exp_f32 v91, v91\n"
    "v_cvt_pk_bf16_f32 v148, v88, v89\n"
    "v_mfma_f32_16x16x32_bf16 v[156:159], v[152:155], v[140:143], v[156:159]\n"
    "v_exp_f32 v92, v92\n"
    "v_exp_f32 v93, v93\n"
    "v_cvt_pk_bf16_f32 v149, v90, v91\n"
    "v_exp_f32 v94, v94\n"
    "v_exp_f32 v95, v95\n"
    "v_cvt_pk_bf16_f32 v150, v92, v93\n"
    "v_cvt_pk_bf16_f32 v151, v94, v95\n"
    "v_mfma_f32_32x32x16_bf16 v[80:95], v[96:99], v[128:131], v[48:63]\n"
    "v_exp_f32 v64, v64\n"
    "v_exp_f32 v65, v65\n"
    "v_exp_f32 v66, v66\n"
    "v_mfma_f32_32x32x16_bf16 v[80:95], v[100:103], v[132:135], v[80:95]\n"
    "ds_read_b128 v[96:99], v164 offset:51200\n"
    "ds_read_b128 v[100:103], v165 offset:51200\n"
    "v_exp_f32 v67, v67\n"
    "v_cvt_pk_bf16_f32 v136, v64, v65\n"
    "v_exp_f32 v68, v68\n"
    "v_exp_f32 v69, v69\n"
    "v_mfma_f32_32x32x16_bf16 v[16:31], v[112:115], v[144:147], v[16:31]\n"
    "ds_read_b128 v[104:107], v166 offset:57344\n"
    "ds_read_b128 v[108:111], v167 offset:57344\n"
    "v_cvt_pk_bf16_f32 v137, v66, v67\n"
    "v_exp_f32 v70, v70\n"
    "v_exp_f32 v71, v71\n"
    "v_cvt_pk_bf16_f32 v138, v68, v69\n"
    "v_mfma_f32_16x16x32_bf16 v[160:163], v[152:155], v[144:147], v[160:163]\n"
    "v_exp_f32 v72, v72\n"
    "v_exp_f32 v73, v73\n"
    "v_mfma_f32_32x32x16_bf16 v[16:31], v[116:119], v[148:151], v[16:31]\n"
    "v_cvt_pk_bf16_f32 v139, v70, v71\n"
    "v_exp_f32 v74, v74\n"
    "v_exp_f32 v75, v75\n"
    "v_cvt_pk_bf16_f32 v140, v72, v73\n"
    "v_mfma_f32_16x16x32_bf16 v[160:163], v[152:155], v[148:151], v[160:163]\n"
    "v_exp_f32 v76, v76\n"
    "v_exp_f32 v77, v77\n"
    "v_cvt_pk_bf16_f32 v141, v74, v75\n"
    "v_exp_f32 v78, v78\n"
    "v_exp_f32 v79, v79\n"
    "v_cvt_pk_bf16_f32 v142, v76, v77\n"
    "s_waitcnt lgkmcnt(0)\n"
    "v_cvt_pk_bf16_f32 v143, v78, v79\n"
    "v_mfma_f32_32x32x16_bf16 v[64:79], v[96:99], v[120:123], v[32:47]\n"
    "v_exp_f32 v80, v80\n"
    "v_exp_f32 v81, v81\n"
    "v_exp_f32 v82, v82\n"
    "v_mfma_f32_32x32x16_bf16 v[64:79], v[100:103], v[124:127], v[64:79]\n"
    "v_exp_f32 v83, v83\n"
    "v_cvt_pk_bf16_f32 v144, v80, v81\n"
    "v_exp_f32 v84, v84\n"
    "v_exp_f32 v85, v85\n"
    "v_mfma_f32_32x32x16_bf16 v[0:15], v[104:107], v[136:139], v[0:15]\n"
    "v_cvt_pk_bf16_f32 v145, v82, v83\n"
    "v_exp_f32 v86, v86\n"
    "v_exp_f32 v87, v87\n"
    "v_cvt_pk_bf16_f32 v146, v84, v85\n"
    "v_mfma_f32_16x16x32_bf16 v[156:159], v[152:155], v[136:139], v[156:159]\n"
    "v_exp_f32 v88, v88\n"
    "v_exp_f32 v89, v89\n"
    "v_mfma_f32_32x32x16_bf16 v[0:15], v[108:111], v[140:143], v[0:15]\n"
    "v_cvt_pk_bf16_f32 v147, v86, v87\n"
    "v_exp_f32 v90, v90\n"
    "v_exp_f32 v91, v91\n"
    "v_cvt_pk_bf16_f32 v148, v88, v89\n"
    "v_mfma_f32_16x16x32_bf16 v[156:159], v[152:155], v[140:143], v[156:159]\n"
    "v_exp_f32 v92, v92\n"
    "v_exp_f32 v93, v93\n"
    "v_cvt_pk_bf16_f32 v149, v90, v91\n"
    "v_exp_f32 v94, v94\n"
    "v_exp_f32 v95, v95\n"
    "v_cvt_pk_bf16_f32 v150, v92, v93\n"
    "v_cvt_pk_bf16_f32 v151, v94, v95\n"
    "v_mfma_f32_32x32x16_bf16 v[80:95], v[96:99], v[128:131], v[48:63]\n"
    "v_exp_f32 v64, v64\n"
    "v_exp_f32 v65, v65\n"
    "v_exp_f32 v66, v66\n"
    "v_mfma_f32_32x32x16_bf16 v[80:95], v[100:103], v[132:135], v[80:95]\n"
    "ds_read_b128 v[96:99], v164 offset:53248\n"
    "ds_read_b128 v[100:103], v165 offset:53248\n"
    "v_exp_f32 v67, v67\n"
    "v_cvt_pk_bf16_f32 v136, v64, v65\n"
    "v_exp_f32 v68, v68\n"
    "v_exp_f32 v69, v69\n"
    "v_mfma_f32_32x32x16_bf16 v[16:31], v[104:107], v[144:147], v[16:31]\n"
    "ds_read_b128 v[112:115], v168 offset:57344\n"
    "ds_read_b128 v[116:119], v169 offset:57344\n"
    "v_cvt_pk_bf16_f32 v137, v66, v67\n"
    "v_exp_f32 v70, v70\n"
    "v_exp_f32 v71, v71\n"
    "v_cvt_pk_bf16_f32 v138, v68, v69\n"
    "v_mfma_f32_16x16x32_bf16 v[160:163], v[152:155], v[144:147], v[160:163]\n"
    "v_exp_f32 v72, v72\n"
    "v_exp_f32 v73, v73\n"
    "v_mfma_f32_32x32x16_bf16 v[16:31], v[108:111], v[148:151], v[16:31]\n"
    "v_cvt_pk_bf16_f32 v139, v70, v71\n"
    "v_exp_f32 v74, v74\n"
    "v_exp_f32 v75, v75\n"
    "v_cvt_pk_bf16_f32 v140, v72, v73\n"
    "v_mfma_f32_16x16x32_bf16 v[160:163], v[152:155], v[148:151], v[160:163]\n"
    "v_exp_f32 v76, v76\n"
    "v_exp_f32 v77, v77\n"
    "v_cvt_pk_bf16_f32 v141, v74, v75\n"
    "v_exp_f32 v78, v78\n"
    "v_exp_f32 v79, v79\n"
    "v_cvt_pk_bf16_f32 v142, v76, v77\n"
    "s_waitcnt lgkmcnt(0)\n"
    "v_cvt_pk_bf16_f32 v143, v78, v79\n"
    "v_mfma_f32_32x32x16_bf16 v[64:79], v[96:99], v[120:123], v[32:47]\n"
    "v_exp_f32 v80, v80\n"
    "v_exp_f32 v81, v81\n"
    "v_exp_f32 v82, v82\n"
    "v_mfma_f32_32x32x16_bf16 v[64:79], v[100:103], v[124:127], v[64:79]\n"
    "v_exp_f32 v83, v83\n"
    "v_cvt_pk_bf16_f32 v144, v80, v81\n"
    "v_exp_f32 v84, v84\n"
    "v_exp_f32 v85, v85\n"
    "v_mfma_f32_32x32x16_bf16 v[0:15], v[112:115], v[136:139], v[0:15]\n"
    "v_cvt_pk_bf16_f32 v145, v82, v83\n"
    "v_exp_f32 v86, v86\n"
    "v_exp_f32 v87, v87\n"
    "v_cvt_pk_bf16_f32 v146, v84, v85\n"
    "v_mfma_f32_16x16x32_bf16 v[156:159], v[152:155], v[136:139], v[156:159]\n"
    "v_exp_f32 v88, v88\n"
    "v_exp_f32 v89, v89\n"
    "v_mfma_f32_32x32x16_bf16 v[0:15], v[116:119], v[140:143], v[0:15]\n"
    "v_cvt_pk_bf16_f32 v147, v86, v87\n"
    "v_exp_f32 v90, v90\n"
    "v_exp_f32 v91, v91\n"
    "v_cvt_pk_bf16_f32 v148, v88, v89\n"
    "v_mfma_f32_16x16x32_bf16 v[156:159], v[152:155], v[140:143], v[156:159]\n"
    "v_exp_f32 v92, v92\n"
    "v_exp_f32 v93, v93\n"
    "v_cvt_pk_bf16_f32 v149, v90, v91\n"
    "v_exp_f32 v94, v94\n"
    "v_exp_f32 v95, v95\n"
    "v_cvt_pk_bf16_f32 v150, v92, v93\n"
    "v_cvt_pk_bf16_f32 v151, v94, v95\n"
    "v_mfma_f32_32x32x16_bf16 v[80:95], v[96:99], v[128:131], v[48:63]\n"
    "v_exp_f32 v64, v64\n"
    "v_exp_f32 v65, v65\n"
    "v_exp_f32 v66, v66\n"
    "v_mfma_f32_32x32x16_bf16 v[80:95], v[100:103], v[132:135], v[80:95]\n"
    "ds_read_b128 v[96:99], v164 offset:55296\n"
    "ds_read_b128 v[100:103], v165 offset:55296\n"
    "v_exp_f32 v67, v67\n"
    "v_cvt_pk_bf16_f32 v136, v64, v65\n"
    "v_exp_f32 v68, v68\n"
    "v_exp_f32 v69, v69\n"
    "v_mfma_f32_32x32x16_bf16 v[16:31], v[112:115], v[144:147], v[16:31]\n"
    "ds_read_b128 v[104:107], v170 offset:57344\n"
    "ds_read_b128 v[108:111], v171 offset:57344\n"
    "v_cvt_pk_bf16_f32 v137, v66, v67\n"
    "v_exp_f32 v70, v70\n"
    "v_exp_f32 v71, v71\n"
    "v_cvt_pk_bf16_f32 v138, v68, v69\n"
    "v_mfma_f32_16x16x32_bf16 v[160:163], v[152:155], v[144:147], v[160:163]\n"
    "v_exp_f32 v72, v72\n"
    "v_exp_f32 v73, v73\n"
    "v_mfma_f32_32x32x16_bf16 v[16:31], v[116:119], v[148:151], v[16:31]\n"
    "v_cvt_pk_bf16_f32 v139, v70, v71\n"
    "v_exp_f32 v74, v74\n"
    "v_exp_f32 v75, v75\n"
    "v_cvt_pk_bf16_f32 v140, v72, v73\n"
    "v_mfma_f32_16x16x32_bf16 v[160:163], v[152:155], v[148:151], v[160:163]\n"
    "v_exp_f32 v76, v76\n"
    "v_exp_f32 v77, v77\n"
    "v_cvt_pk_bf16_f32 v141, v74, v75\n"
    "v_exp_f32 v78, v78\n"
    "v_exp_f32 v79, v79\n"
    "v_cvt_pk_bf16_f32 v142, v76, v77\n"
    "s_waitcnt lgkmcnt(0)\n"
    "v_cvt_pk_bf16_f32 v143, v78, v79\n"
    "v_mfma_f32_32x32x16_bf16 v[64:79], v[96:99], v[120:123], v[32:47]\n"
    "v_exp_f32 v80, v80\n"
    "v_exp_f32 v81, v81\n"
    "v_exp_f32 v82, v82\n"
    "v_mfma_f32_32x32x16_bf16 v[64:79], v[100:103], v[124:127], v[64:79]\n"
    "v_exp_f32 v83, v83\n"
    "v_cvt_pk_bf16_f32 v144, v80, v81\n"
    "v_exp_f32 v84, v84\n"
    "v_exp_f32 v85, v85\n"
    "v_mfma_f32_32x32x16_bf16 v[0:15], v[104:107], v[136:139], v[0:15]\n"
    "v_cvt_pk_bf16_f32 v145, v82, v83\n"
    "v_exp_f32 v86, v86\n"
    "v_exp_f32 v87, v87\n"
    "v_cvt_pk_bf16_f32 v146, v84, v85\n"
    "v_mfma_f32_16x16x32_bf16 v[156:159], v[152:155], v[136:139], v[156:159]\n"
    "v_exp_f32 v88, v88\n"
    "v_exp_f32 v89, v89\n"
    "v_mfma_f32_32x32x16_bf16 v[0:15], v[108:111], v[140:143], v[0:15]\n"
    "v_cvt_pk_bf16_f32 v147, v86, v87\n"
    "v_exp_f32 v90, v90\n"
    "v_exp_f32 v91, v91\n"
    "v_cvt_pk_bf16_f32 v148, v88, v89\n"
    "v_mfma_f32_16x16x32_bf16 v[156:159], v[152:155], v[140:143], v[156:159]\n"
    "v_exp_f32 v92, v92\n"
    "v_exp_f32 v93, v93\n"
    "v_cvt_pk_bf16_f32 v149, v90, v91\n"
    "v_exp_f32 v94, v94\n"
    "v_exp_f32 v95, v95\n"
    "v_cvt_pk_bf16_f32 v150, v92, v93\n"
    "s_cmp_eq_u32 s40, 3\n"
    "s_cbranch_scc1 13f\n"
    "s_barrier\n"
    "13:\n"
    "v_cvt_pk_bf16_f32 v151, v94, v95\n"
    "v_mfma_f32_32x32x16_bf16 v[80:95], v[96:99], v[128:131], v[48:63]\n"
    "v_exp_f32 v64, v64\n"
    "v_exp_f32 v65, v65\n"
    "v_exp_f32 v66, v66\n"
    "v_mfma_f32_32x32x16_bf16 v[80:95], v[100:103], v[132:135], v[80:95]\n"
    "ds_read_b128 v[96:99], v164 offset:0\n"
    "ds_read_b128 v[100:103], v165 offset:0\n"
    "v_exp_f32 v67, v67\n"
    "v_cvt_pk_bf16_f32 v136, v64, v65\n"
    "v_exp_f32 v68, v68\n"
    "v_exp_f32 v69, v69\n"
    "v_mfma_f32_32x32x16_bf16 v[16:31], v[104:107], v[144:147], v[16:31]\n"
    "ds_read_b128 v[112:115], v172 offset:57344\n"
    "ds_read_b128 v[116:119], v173 offset:57344\n"
    "v_cvt_pk_bf16_f32 v137, v66, v67\n"
    "v_exp_f32 v70, v70\n"
    "v_exp_f32 v71, v71\n"
    "v_cvt_pk_bf16_f32 v138, v68, v69\n"
    "v_mfma_f32_16x16x32_bf16 v[160:163], v[152:155], v[144:147], v[160:163]\n"
    "v_exp_f32 v72, v72\n"
    "v_exp_f32 v73, v73\n"
    "v_mfma_f32_32x32x16_bf16 v[16:31], v[108:111], v[148:151], v[16:31]\n"
    "v_cvt_pk_bf16_f32 v139, v70, v71\n"
    "v_exp_f32 v74, v74\n"
    "v_exp_f32 v75, v75\n"
    "v_cvt_pk_bf16_f32 v140, v72, v73\n"
    "v_mfma_f32_16x16x32_bf16 v[160:163], v[152:155], v[148:151], v[160:163]\n"
    "v_exp_f32 v76, v76\n"
    "v_exp_f32 v77, v77\n"
    "v_cvt_pk_bf16_f32 v141, v74, v75\n"
    "v_exp_f32 v78, v78\n"
    "v_exp_f32 v79, v79\n"
    "v_cvt_pk_bf16_f32 v142, v76, v77\n"
    "s_waitcnt lgkmcnt(0)\n"
    "v_cvt_pk_bf16_f32 v143, v78, v79\n"
    "v_mfma_f32_32x32x16_bf16 v[64:79], v[96:99], v[120:123], v[32:47]\n"
    "v_exp_f32 v80, v80\n"
    "v_exp_f32 v81, v81\n"
    "v_exp_f32 v82, v82\n"
    "v_mfma_f32_32x32x16_bf16 v[64:79], v[100:103], v[124:127], v[64:79]\n"
    "v_exp_f32 v83, v83\n"
    "v_cvt_pk_bf16_f32 v144, v80, v81\n"
    "v_exp_f32 v84, v84\n"
    "v_exp_f32 v85, v85\n"
    "v_mfma_f32_32x32x16_bf16 v[0:15], v[112:115], v[136:139], v[0:15]\n"
    "v_cvt_pk_bf16_f32 v145, v82, v83\n"
    "v_exp_f32 v86, v86\n"
    "v_exp_f32 v87, v87\n"
    "v_cvt_pk_bf16_f32 v146, v84, v85\n"
    "v_mfma_f32_16x16x32_bf16 v[156:159], v[152:155], v[136:139], v[156:159]\n"
    "v_exp_f32 v88, v88\n"
    "v_exp_f32 v89, v89\n"
    "v_mfma_f32_32x32x16_bf16 v[0:15], v[116:119], v[140:143], v[0:15]\n"
    "v_cvt_pk_bf16_f32 v147, v86, v87\n"
    "v_exp_f32 v90, v90\n"
    "v_exp_f32 v91, v91\n"
    "v_cvt_pk_bf16_f32 v148, v88, v89\n"
    "v_mfma_f32_16x16x32_bf16 v[156:159], v[152:155], v[140:143], v[156:159]\n"
    "v_exp_f32 v92, v92\n"
    "v_exp_f32 v93, v93\n"
    "v_cvt_pk_bf16_f32 v149, v90, v91\n"
    "v_exp_f32 v94, v94\n"
    "v_exp_f32 v95, v95\n"
    "v_cvt_pk_bf16_f32 v150, v92, v93\n"
    "s_add_u32 s40, s40, 1\n"
    "s_cmp_lt_u32 s40, 4\n"
    "s_cbranch_scc1 9b\n"
    "v_cvt_pk_bf16_f32 v151, v94, v95\n"
    "s_nop 1\n"
    "v_mfma_f32_32x32x16_bf16 v[16:31], v[112:115], v[144:147], v[16:31]\n"
    "v_mfma_f32_16x16x32_bf16 v[160:163], v[152:155], v[144:147], v[160:163]\n"
    "v_mfma_f32_32x32x16_bf16 v[16:31], v[116:119], v[148:151], v[16:31]\n"
    "v_mfma_f32_16x16x32_bf16 v[160:163], v[152:155], v[148:151], v[160:163]\n"
    "s_setprio 0\n"
    "s_waitcnt vmcnt(0)\n"
    "s_nop 15\n"

        : "+{v[0:15]}"(oa),
          "+{v[16:31]}"(ob), "+{v[64:79]}"(sa), "+{v[80:95]}"(sb), "+{v[96:99]}"(k0), "+{v[100:103]}"(k1), "+{v[112:115]}"(vz0), "+{v[116:119]}"(vz1),
          "+{v[144:147]}"(pb0), "+{v[148:151]}"(pb1), "+{v[156:159]}"(lsa), "+{v[160:163]}"(lsb), "={v176}"(ga), "={v177}"(gb)
        : "{v[32:47]}"(nma), "{v[48:63]}"(nmb), "{v[120:123]}"(q[0][0]), "{v[124:127]}"(q[0][1]), "{v[128:131]}"(q[1][0]), "{v[132:135]}"(q[1][1]), "{v[152:155]}"(selA),
          "{v164}"(rK0), "{v165}"(rK1), "{v166}"(rv[0]), "{v167}"(rv[1]), "{v168}"(rv[2]), "{v169}"(rv[3]), "{v170}"(rv[4]), "{v171}"(rv[5]), "{v172}"(rv[6]), "{v173}"(rv[7]),
          "{v[174:175]}"(gp)
        : "memory", "scc", "s40", "s44", "s45", "s46", "v104", "v105", "v106", "v107", "v108", "v109", "v110", "v111", "v136", "v137", "v138", "v139", "v140", "v141", "v142", "v143");
    { const float l0 = __shfl(lsa[0], r & 15), l1 = __shfl(lsa[1], r & 15); la = (r < 16) ? l0 : l1; }
    { const float l0 = __shfl(lsb[0], r & 15), l1 = __shfl(lsb[1], r & 15); lb = (r < 16) ? l0 : l1; }
    bool ok = la < 3.0e38f && lb < 3.0e38f;
#pragma unroll
    for (int i = 0; i < 16; ++i) ok = ok && (__builtin_fabsf(oa[i]) < 3.0e38f) && (__builtin_fabsf(ob[i]) < 3.0e38f);
    return ok;
}

DI void attn_safe(const Params& a, const Ctx& c, const bf16x8 (&q)[2][2], f32x16& oa, f32x16& ob, float& la, float& lb) {
    const int lane = lane_id();
    const int r = lane & 31, hh = lane >> 5, pr = (r & 19) | ((r & 4) << 1) | ((r & 8) >> 1);
    const char* gK = (const char*)(a.kv + (size_t)c.h * (S_ * 32)) + pr * 64 + hh * 16;
    const char* gV = (const char*)(a.kv + (size_t)(32 + c.h) * (S_ * 32)) + r * 256 + hh * 16;
#pragma unroll
    for (int i = 0; i < 16; ++i) { oa[i] = 0.f; ob[i] = 0.f; }
    float m[2] = {-INFINITY, -INFINITY}, l[2] = {0.f, 0.f};
#pragma unroll 1
    for (int u = 0; u < S_ / 32; ++u) {
        bf16x8 k0, k1, v0, v1;
        const char* pk = gK + (size_t)u * 2048; const char* pv = gV + (size_t)(u >> 2) * 8192 + (u & 3) * 64;
        asm volatile("global_load_dwordx4 %0, %4, off sc1\n\tglobal_load_dwordx4 %1, %4, off offset:32 sc1\n\tglobal_load_dwordx4 %2, %5, off sc1\n\tglobal_load_dwordx4 %3, %5, off offset:32 sc1\n\ts_waitcnt vmcnt(0)"
                     : "=&v"(k0), "=&v"(k1), "=&v"(v0), "=&v"(v1) : "v"(pk), "v"(pv) : "memory");
#pragma unroll
        for (int b = 0; b < 2; ++b) {
            f32x16& o = b ? ob : oa;
            f32x16 z;
#pragma unroll
            for (int i = 0; i < 16; ++i) z[i] = 0.f;
            f32x16 s = MFMA32(k0, q[b][0], z); s = MFMA32(k1, q[b][1], s);
            float mx = s[0];
#pragma unroll
            for (int i = 1; i < 16; ++i) mx = __builtin_fmaxf(mx, s[i]);
            auto rr = __builtin_amdgcn_permlane32_swap(__float_as_uint(mx), __float_as_uint(mx), false, false);
            mx = __builtin_fmaxf(__uint_as_float(rr[0]), __uint_as_float(rr[1]));
            const float mn = __builtin_fmaxf(m[b], mx);
            const float al = (mn == m[b]) ? 1.0f : __builtin_amdgcn_exp2f(m[b] - mn);
            const float mref = (mn == -INFINITY) ? 0.f : mn;
            m[b] = mn;
            float ps = 0.f; u32x4 p0, p1;
#pragma unroll
            for (int i = 0; i < 16; ++i) s[i] = __builtin_amdgcn_exp2f(s[i] - mref);
#pragma unroll
            for (int i = 0; i < 4; ++i) { p0[i] = cvtpk(s[2 * i], s[2 * i + 1]); p1[i] = cvtpk(s[8 + 2 * i], s[8 + 2 * i + 1]); }
#pragma unroll
            for (int i = 0; i < 4; ++i) ps += __uint_as_float(p0[i] << 16) + __uint_as_float(p0[i] & 0xffff0000u) + __uint_as_float(p1[i] << 16) + __uint_as_float(p1[i] & 0xffff0000u);
            l[b] = l[b] * al + ps;
#pragma unroll
            for (int i = 0; i < 16; ++i) o[i] *= al;
            o = MFMA32(v0, __builtin_bit_cast(bf16x8, p0), o);
            o = MFMA32(v1, __builtin_bit_cast(bf16x8, p1), o);
        }
    }
    { auto rr = __builtin_amdgcn_permlane32_swap(__float_as_uint(l[0]), __float_as_uint(l[0]), false, false); la = __uint_as_float(rr[0]) + __uint_as_float(rr[1]); }
    { auto rr = __builtin_amdgcn_permlane32_swap(__float_as_uint(l[1]), __float_as_uint(l[1]), false, false); lb = __uint_as_float(rr[0]) + __uint_as_float(rr[1]); }
}

DI void store_o(const Params& a, const Ctx& c, const int row32, const int lane, const f32x16& o, const float sc) {
    store_tile32(a.ho + (size_t)row32 * HO_LD + F_ + c.h * 32, HO_LD, o * sc, lane);
}
#undef VMB
#undef P2_KOFF
}

namespace p3 {
constexpr int NS = 5, STAGE = 32768;
DI int kh_of() { return (blockIdx.x & 1) ^ 1; }
DI int rb_of() { return ((blockIdx.x & 7) >> 2) * 8 + ((blockIdx.x >> 3) & 7); }
DI int cb_of() { return (((blockIdx.x & 7) >> 1) & 1) * 4 + (blockIdx.x >> 6); }

struct Prod { unsigned voffA0, voffA1, voffB, dA, dB; const char* gA; const char* gWd; const char* gWo; int nH; };
DI Prod prod_setup(const Params& a, const unsigned lds0, const int pw, const int lane) {
    const int kh = kh_of(), rb = rb_of(), cb = cb_of();
    Prod P;
    P.nH = kh ? 13 : 43;
    P.voffA0 = (unsigned)((lane >> 3) * (HO_LD * 2) + (((lane & 7) ^ (lane >> 4)) * 16));
    P.voffA1 = (unsigned)((lane >> 3) * (HO_LD * 2) + (((lane & 7) ^ (4 + (lane >> 4))) * 16));
    P.voffB = (unsigned)((lane >> 4) * 2048 + (((lane & 15) ^ ((lane >> 4) << 2)) * 16));
    P.gA = (const char*)a.ho + (size_t)(rb * 128 + pw * 32) * (HO_LD * 2) + (kh ? KSPLIT * 2 : 0);
    P.gWd = (const char*)a.wd + (size_t)((kh ? KSPLIT : 0) + pw * 16) * 2048 + cb * 256;
    P.gWo = (const char*)a.wo + (size_t)(pw * 16) * 2048 + cb * 256 - (size_t)P.nH * (64 * 2048);
    P.dA = lds0 + pw * 4096; P.dB = lds0 + 16384 + pw * 4096;
    return P;
}
DI void prod_issue(const Prod& P, const int t, const unsigned so) {
    const char* sa = P.gA + (size_t)t * 128; const char* sb = (t < P.nH ? P.gWd : P.gWo) + (size_t)t * (64 * 2048);
#pragma unroll
    for (int i = 0; i < 4; ++i) glds16c((i & 1) ? P.voffA1 : P.voffA0, sa + (size_t)i * 8 * (HO_LD * 2), P.dA + so + i * 1024);
#pragma unroll
    for (int i = 0; i < 4; ++i) glds16(P.voffB, sb + i * 4 * 2048, P.dB + so + i * 1024);
}
DI void producer_prologue(const Params& a, const unsigned lds0, const int pw, const int lane) {
    const Prod P = prod_setup(a, lds0, pw, lane);
#pragma unroll
    for (int t = 0; t < 3; ++t) prod_issue(P, t, (t + 2) * STAGE);
}

template <int KH>
DI void producer(const Params& a, const unsigned lds0, const int w, const unsigned tag) {
    constexpr int NKT = KH ? 29 : 43;
    const int lane = lane_id();
    const Prod P = prod_setup(a, lds0, w - 4, lane);
    asm volatile("s_waitcnt vmcnt(16)\n\ts_barrier" ::: "memory");
    prod_issue(P, 3, 0); prod_issue(P, 4, STAGE);
    unsigned so = 2 * STAGE;
#define P3_PSTEP(t) do { asm volatile("s_waitcnt vmcnt(24)\n\ts_barrier" ::: "memory"); \
        if ((t) + 4 < NKT) prod_issue(P, (t) + 4, so); \
        so = (so == (NS - 1) * STAGE) ? 0u : so + STAGE; } while (0)
    if constexpr (KH) {
        const unsigned* pollp = a.flags + FLAG_O + (rb_of() >> 1) * 32 + (lane & 31);
        for (int t = 1; t <= 5; ++t) P3_PSTEP(t);
        unsigned pollv = 0;
        asm volatile("global_load_dword %0, %1, off sc1" : "+v"(pollv) : "v"(pollp) : "memory");
        P3_PSTEP(6); P3_PSTEP(7);
        asm volatile("s_waitcnt vmcnt(16)" : "+v"(pollv) :: "memory");
        const bool okO = __builtin_amdgcn_ballot_w64(pollv != tag) == 0ull;
        P3_PSTEP(8);
        if (!okO) wait_flags(pollp, tag);
        for (int t = 9; t < NKT - 3; ++t) P3_PSTEP(t);
    } else {
        for (int t = 1; t < NKT - 3; ++t) P3_PSTEP(t);
    }
#undef P3_PSTEP
    asm volatile("s_waitcnt vmcnt(16)\n\ts_barrier" ::: "memory");
    if constexpr (!KH) { if (w == 4) wait_flags(a.flags + FLAG_X + (rb_of() * 8 + cb_of()) * 16, tag); }
    asm volatile("s_waitcnt vmcnt(8)\n\ts_barrier" ::: "memory");
    asm volatile("s_waitcnt vmcnt(0)\n\ts_barrier" ::: "memory");
    if constexpr (KH) asm volatile("s_barrier" ::: "memory");
}

template <int KH>
DI void consumer(const Params& a, const unsigned lds0, const int w, const unsigned tag) {
    typedef unsigned u32x16 __attribute__((ext_vector_type(16)));
    typedef float f32x32 __attribute__((ext_vector_type(32)));
    const int lane = lane_id();
    const int rb = rb_of(), cb = cb_of();
    const int tile = rb * 8 + cb;
    const int wr = w & 1, wc = w >> 1, r = lane & 31, hh = lane >> 5;
    const int row0 = rb * 128 + wr * 64 + r;
    float gg[2];
#pragma unroll
    for (int mi = 0; mi < 2; ++mi) asm volatile("global_load_dword %0, %1, off sc1" : "=v"(gg[mi]) : "v"(a.g + row0 + mi * 32) : "memory");
    u32x16 ad, ad2;
    {   const int q = (lane & 15) >> 2, p = lane & 3, g16 = lane >> 4;
#pragma unroll
        for (int sp = 0; sp < 3; ++sp) {
#pragma unroll
            for (int s = 0; s < 4; ++s) ad[4 * sp + s] = lds0 + sp * 2 * STAGE + (wr * 64 + r) * 128 + (((2 * s + hh) ^ ((r >> 1) & 7)) * 16);
#pragma unroll
            for (int ni = 0; ni < 2; ++ni) { const unsigned v = lds0 + sp * 2 * STAGE + 16384 + (8 * (g16 >> 1) + q) * 256 + (((2 * wc + ni) ^ q) * 64) + (g16 & 1) * 32 + p * 8;
                                             if (sp < 2) ad[12 + 2 * sp + ni] = v; else ad2[ni] = v; }
        }
#pragma unroll
        for (int i = 2; i < 16; ++i) ad2[i] = 0u;
    }
    f32x16 acc[4];
    f32x32 fb0, fb1;
#pragma unroll
    for (int i = 0; i < 4; ++i)
#pragma unroll
        for (int e = 0; e < 16; ++e) acc[i][e] = 0.f;
#pragma unroll
    for (int e = 0; e < 32; ++e) { fb0[e] = 0.f; fb1[e] = 0.f; }
#define P3_OPS : "+{v[0:15]}"(acc[0]), "+{v[16:31]}"(acc[1]), "+{v[32:47]}"(acc[2]), "+{v[48:63]}"(acc[3]), "+{v[64:95]}"(fb0), "+{v[96:127]}"(fb1)
#define P3_INS "{v[128:143]}"(ad), "{v[144:159]}"(ad2)
#define P3_SCALE() do { _Pragma("unroll") for (int mi = 0; mi < 2; ++mi) { const float om = 1.0f - gg[mi]; acc[2 * mi] *= om; acc[2 * mi + 1] *= om; } } while (0)
    asm volatile("s_barrier" ::: "memory");

    asm volatile(
    "s_nop 1\n"
    "ds_read_b64_tr_b16 v[80:81], v142 offset:0\n"
    "ds_read_b64_tr_b16 v[82:83], v142 offset:1024\n"
    "ds_read_b128 v[64:67], v132 offset:0\n"
    "ds_read_b128 v[72:75], v132 offset:4096\n"
    "ds_read_b64_tr_b16 v[88:89], v143 offset:0\n"
    "ds_read_b64_tr_b16 v[90:91], v143 offset:1024\n"
    "ds_read_b64_tr_b16 v[84:85], v142 offset:4096\n"
    "ds_read_b64_tr_b16 v[86:87], v142 offset:5120\n"
    "ds_read_b128 v[68:71], v133 offset:0\n"
    "ds_read_b128 v[76:79], v133 offset:4096\n"
    "ds_read_b64_tr_b16 v[92:93], v143 offset:4096\n"
    "ds_read_b64_tr_b16 v[94:95], v143 offset:5120\n"
    "s_waitcnt lgkmcnt(9)\n"
    "v_mfma_f32_32x32x16_bf16 v[0:15], v[80:83], v[64:67], v[0:15]\n"
    "ds_read_b64_tr_b16 v[112:113], v142 offset:8192\n"
    "ds_read_b64_tr_b16 v[114:115], v142 offset:9216\n"
    "s_waitcnt lgkmcnt(10)\n"
    "v_mfma_f32_32x32x16_bf16 v[32:47], v[80:83], v[72:75], v[32:47]\n"
    "ds_read_b128 v[96:99], v134 offset:0\n"
    "ds_read_b128 v[104:107], v134 offset:4096\n"
    "s_waitcnt lgkmcnt(10)\n"
    "v_mfma_f32_32x32x16_bf16 v[16:31], v[88:91], v[64:67], v[16:31]\n"
    "ds_read_b64_tr_b16 v[120:121], v143 offset:8192\n"
    "ds_read_b64_tr_b16 v[122:123], v143 offset:9216\n"
    "v_mfma_f32_32x32x16_bf16 v[48:63], v[88:91], v[72:75], v[48:63]\n"
    "ds_read_b64_tr_b16 v[116:117], v142 offset:12288\n"
    "ds_read_b64_tr_b16 v[118:119], v142 offset:13312\n"
    "s_waitcnt lgkmcnt(11)\n"
    "v_mfma_f32_32x32x16_bf16 v[0:15], v[84:87], v[68:71], v[0:15]\n"
    "ds_read_b128 v[100:103], v135 offset:0\n"
    "ds_read_b128 v[108:111], v135 offset:4096\n"
    "s_waitcnt lgkmcnt(12)\n"
    "v_mfma_f32_32x32x16_bf16 v[32:47], v[84:87], v[76:79], v[32:47]\n"
    "ds_read_b64_tr_b16 v[124:125], v143 offset:12288\n"
    "ds_read_b64_tr_b16 v[126:127], v143 offset:13312\n"
    "s_waitcnt lgkmcnt(12)\n"
    "v_mfma_f32_32x32x16_bf16 v[16:31], v[92:95], v[68:71], v[16:31]\n"
    "v_mfma_f32_32x32x16_bf16 v[48:63], v[92:95], v[76:79], v[48:63]\n"
    "s_waitcnt lgkmcnt(0)\n"
    "s_barrier\n"
    "v_mfma_f32_32x32x16_bf16 v[0:15], v[112:115], v[96:99], v[0:15]\n"
    "ds_read_b64_tr_b16 v[80:81], v142 offset:32768\n"
    "ds_read_b64_tr_b16 v[82:83], v142 offset:33792\n"
    "v_mfma_f32_32x32x16_bf16 v[32:47], v[112:115], v[104:107], v[32:47]\n"
    "ds_read_b128 v[64:67], v132 offset:32768\n"
    "ds_read_b128 v[72:75], v132 offset:36864\n"
    "v_mfma_f32_32x32x16_bf16 v[16:31], v[120:123], v[96:99], v[16:31]\n"
    "ds_read_b64_tr_b16 v[88:89], v143 offset:32768\n"
    "ds_read_b64_tr_b16 v[90:91], v143 offset:33792\n"
    "v_mfma_f32_32x32x16_bf16 v[48:63], v[120:123], v[104:107], v[48:63]\n"
    "ds_read_b64_tr_b16 v[84:85], v142 offset:36864\n"
    "ds_read_b64_tr_b16 v[86:87], v142 offset:37888\n"
    "v_mfma_f32_32x32x16_bf16 v[0:15], v[116:119], v[100:103], v[0:15]\n"
    "ds_read_b128 v[68:71], v133 offset:32768\n"
    "ds_read_b128 v[76:79], v133 offset:36864\n"
    "v_mfma_f32_32x32x16_bf16 v[32:47], v[116:119], v[108:111], v[32:47]\n"
    "ds_read_b64_tr_b16 v[92:93], v143 offset:36864\n"
    "ds_read_b64_tr_b16 v[94:95], v143 offset:37888\n"
    "v_mfma_f32_32x32x16_bf16 v[16:31], v[124:127], v[100:103], v[16:31]\n"
    "v_mfma_f32_32x32x16_bf16 v[48:63], v[124:127], v[108:111], v[48:63]\n"
    "s_waitcnt lgkmcnt(9)\n"
    "v_mfma_f32_32x32x16_bf16 v[0:15], v[80:83], v[64:67], v[0:15]\n"
    "ds_read_b64_tr_b16 v[112:113], v142 offset:40960\n"
    "ds_read_b64_tr_b16 v[114:115], v142 offset:41984\n"
    "s_waitcnt lgkmcnt(10)\n"
    "v_mfma_f32_32x32x16_bf16 v[32:47], v[80:83], v[72:75], v[32:47]\n"
    "ds_read_b128 v[96:99], v134 offset:32768\n"
    "ds_read_b128 v[104:107], v134 offset:36864\n"
    "s_waitcnt lgkmcnt(10)\n"
    "v_mfma_f32_32x32x16_bf16 v[16:31], v[88:91], v[64:67], v[16:31]\n"
    "ds_read_b64_tr_b16 v[120:121], v143 offset:40960\n"
    "ds_read_b64_tr_b16 v[122:123], v143 offset:41984\n"
    "v_mfma_f32_32x32x16_bf16 v[48:63], v[88:91], v[72:75], v[48:63]\n"
    "ds_read_b64_tr_b16 v[116:117], v142 offset:45056\n"
    "ds_read_b64_tr_b16 v[118:119], v142 offset:46080\n"
    "s_waitcnt lgkmcnt(11)\n"
    "v_mfma_f32_32x32x16_bf16 v[0:15], v[84:87], v[68:71], v[0:15]\n"
    "ds_read_b128 v[100:103], v135 offset:32768\n"
    "ds_read_b128 v[108:111], v135 offset:36864\n"
    "s_waitcnt lgkmcnt(12)\n"
    "v_mfma_f32_32x32x16_bf16 v[32:47], v[84:87], v[76:79], v[32:47]\n"
    "ds_read_b64_tr_b16 v[124:125], v143 offset:45056\n"
    "ds_read_b64_tr_b16 v[126:127], v143 offset:46080\n"
    "s_waitcnt lgkmcnt(12)\n"
    "v_mfma_f32_32x32x16_bf16 v[16:31], v[92:95], v[68:71], v[16:31]\n"
    "v_mfma_f32_32x32x16_bf16 v[48:63], v[92:95], v[76:79], v[48:63]\n"
    "s_waitcnt lgkmcnt(0)\n"
    "s_barrier\n"
    "v_mfma_f32_32x32x16_bf16 v[0:15], v[112:115], v[96:99], v[0:15]\n"
    "ds_read_b64_tr_b16 v[80:81], v144 offset:0\n"
    "ds_read_b64_tr_b16 v[82:83], v144 offset:1024\n"
    "v_mfma_f32_32x32x16_bf16 v[32:47], v[112:115], v[104:107], v[32:47]\n"
    "ds_read_b128 v[64:67], v136 offset:0\n"
    "ds_read_b128 v[72:75], v136 offset:4096\n"
    "v_mfma_f32_32x32x16_bf16 v[16:31], v[120:123], v[96:99], v[16:31]\n"
    "ds_read_b64_tr_b16 v[88:89], v145 offset:0\n"
    "ds_read_b64_tr_b16 v[90:91], v145 offset:1024\n"
    "v_mfma_f32_32x32x16_bf16 v[48:63], v[120:123], v[104:107], v[48:63]\n"
    "ds_read_b64_tr_b16 v[84:85], v144 offset:4096\n"
    "ds_read_b64_tr_b16 v[86:87], v144 offset:5120\n"
    "v_mfma_f32_32x32x16_bf16 v[0:15], v[116:119], v[100:103], v[0:15]\n"
    "ds_read_b128 v[68:71], v137 offset:0\n"
    "ds_read_b128 v[76:79], v137 offset:4096\n"
    "v_mfma_f32_32x32x16_bf16 v[32:47], v[116:119], v[108:111], v[32:47]\n"
    "ds_read_b64_tr_b16 v[92:93], v145 offset:4096\n"
    "ds_read_b64_tr_b16 v[94:95], v145 offset:5120\n"
    "v_mfma_f32_32x32x16_bf16 v[16:31], v[124:127], v[100:103], v[16:31]\n"
    "v_mfma_f32_32x32x16_bf16 v[48:63], v[124:127], v[108:111], v[48:63]\n"
    "s_waitcnt lgkmcnt(9)\n"
    "v_mfma_f32_32x32x16_bf16 v[0:15], v[80:83], v[64:67], v[0:15]\n"
    "ds_read_b64_tr_b16 v[112:113], v144 offset:8192\n"
    "ds_read_b64_tr_b16 v[114:115], v144 offset:9216\n"
    "s_waitcnt lgkmcnt(10)\n"
    "v_mfma_f32_32x32x16_bf16 v[32:47], v[80:83], v[72:75], v[32:47]\n"
    "ds_read_b128 v[96:99], v138 offset:0\n"
    "ds_read_b128 v[104:107], v138 offset:4096\n"
    "s_waitcnt lgkmcnt(10)\n"
    "v_mfma_f32_32x32x16_bf16 v[16:31], v[88:91], v[64:67], v[16:31]\n"
    "ds_read_b64_tr_b16 v[120:121], v145 offset:8192\n"
    "ds_read_b64_tr_b16 v[122:123], v145 offset:9216\n"
    "v_mfma_f32_32x32x16_bf16 v[48:63], v[88:91], v[72:75], v[48:63]\n"
    "ds_read_b64_tr_b16 v[116:117], v144 offset:12288\n"
    "ds_read_b64_tr_b16 v[118:119], v144 offset:13312\n"
    "s_waitcnt lgkmcnt(11)\n"
    "v_mfma_f32_32x32x16_bf16 v[0:15], v[84:87], v[68:71], v[0:15]\n"
    "ds_read_b128 v[100:103], v139 offset:0\n"
    "ds_read_b128 v[108:111], v139 offset:4096\n"
    "s_waitcnt lgkmcnt(12)\n"
    "v_mfma_f32_32x32x16_bf16 v[32:47], v[84:87], v[76:79], v[32:47]\n"
    "ds_read_b64_tr_b16 v[124:125], v145 offset:12288\n"
    "ds_read_b64_tr_b16 v[126:127], v145 offset:13312\n"
    "s_waitcnt lgkmcnt(12)\n"
    "v_mfma_f32_32x32x16_bf16 v[16:31], v[92:95], v[68:71], v[16:31]\n"
    "v_mfma_f32_32x32x16_bf16 v[48:63], v[92:95], v[76:79], v[48:63]\n"
    "s_waitcnt vmcnt(0) lgkmcnt(0)\n"
    "s_barrier\n"
    "v_mfma_f32_32x32x16_bf16 v[0:15], v[112:115], v[96:99], v[0:15]\n"
    "ds_read_b64_tr_b16 v[80:81], v140 offset:0\n"
    "ds_read_b64_tr_b16 v[82:83], v140 offset:1024\n"
    "v_mfma_f32_32x32x16_bf16 v[32:47], v[112:115], v[104:107], v[32:47]\n"
    "ds_read_b128 v[64:67], v128 offset:0\n"
    "ds_read_b128 v[72:75], v128 offset:4096\n"
    "v_mfma_f32_32x32x16_bf16 v[16:31], v[120:123], v[96:99], v[16:31]\n"
    "ds_read_b64_tr_b16 v[88:89], v141 offset:0\n"
    "ds_read_b64_tr_b16 v[90:91], v141 offset:1024\n"
    "v_mfma_f32_32x32x16_bf16 v[48:63], v[120:123], v[104:107], v[48:63]\n"
    "ds_read_b64_tr_b16 v[84:85], v140 offset:4096\n"
    "ds_read_b64_tr_b16 v[86:87], v140 offset:5120\n"
    "v_mfma_f32_32x32x16_bf16 v[0:15], v[116:119], v[100:103], v[0:15]\n"
    "ds_read_b128 v[68:71], v129 offset:0\n"
    "ds_read_b128 v[76:79], v129 offset:4096\n"
    "v_mfma_f32_32x32x16_bf16 v[32:47], v[116:119], v[108:111], v[32:47]\n"
    "ds_read_b64_tr_b16 v[92:93], v141 offset:4096\n"
    "ds_read_b64_tr_b16 v[94:95], v141 offset:5120\n"
    "v_mfma_f32_32x32x16_bf16 v[16:31], v[124:127], v[100:103], v[16:31]\n"
    "v_mfma_f32_32x32x16_bf16 v[48:63], v[124:127], v[108:111], v[48:63]\n"
    "s_nop 15\n"

        P3_OPS : P3_INS : "memory");
    asm volatile("" : "+v"(gg[0]), "+v"(gg[1]));
    if (w == 0 && lane == 0) flag_set(a.flags + FLAG_O + ((blockIdx.x >> 3) & 7) * 32 + (blockIdx.x & 7) * 4 + (blockIdx.x >> 6), tag);
    asm volatile(
    "s_nop 1\n"
    "s_waitcnt lgkmcnt(9)\n"
    "v_mfma_f32_32x32x16_bf16 v[0:15], v[80:83], v[64:67], v[0:15]\n"
    "ds_read_b64_tr_b16 v[112:113], v140 offset:8192\n"
    "ds_read_b64_tr_b16 v[114:115], v140 offset:9216\n"
    "s_waitcnt lgkmcnt(10)\n"
    "v_mfma_f32_32x32x16_bf16 v[32:47], v[80:83], v[72:75], v[32:47]\n"
    "ds_read_b128 v[96:99], v130 offset:0\n"
    "ds_read_b128 v[104:107], v130 offset:4096\n"
    "s_waitcnt lgkmcnt(10)\n"
    "v_mfma_f32_32x32x16_bf16 v[16:31], v[88:91], v[64:67], v[16:31]\n"
    "ds_read_b64_tr_b16 v[120:121], v141 offset:8192\n"
    "ds_read_b64_tr_b16 v[122:123], v141 offset:9216\n"
    "v_mfma_f32_32x32x16_bf16 v[48:63], v[88:91], v[72:75], v[48:63]\n"
    "ds_read_b64_tr_b16 v[116:117], v140 offset:12288\n"
    "ds_read_b64_tr_b16 v[118:119], v140 offset:13312\n"
    "s_waitcnt lgkmcnt(11)\n"
    "v_mfma_f32_32x32x16_bf16 v[0:15], v[84:87], v[68:71], v[0:15]\n"
    "ds_read_b128 v[100:103], v131 offset:0\n"
    "ds_read_b128 v[108:111], v131 offset:4096\n"
    "s_waitcnt lgkmcnt(12)\n"
    "v_mfma_f32_32x32x16_bf16 v[32:47], v[84:87], v[76:79], v[32:47]\n"
    "ds_read_b64_tr_b16 v[124:125], v141 offset:12288\n"
    "ds_read_b64_tr_b16 v[126:127], v141 offset:13312\n"
    "s_waitcnt lgkmcnt(12)\n"
    "v_mfma_f32_32x32x16_bf16 v[16:31], v[92:95], v[68:71], v[16:31]\n"
    "v_mfma_f32_32x32x16_bf16 v[48:63], v[92:95], v[76:79], v[48:63]\n"
    "s_waitcnt lgkmcnt(0)\n"
    "s_barrier\n"
    "v_mfma_f32_32x32x16_bf16 v[0:15], v[112:115], v[96:99], v[0:15]\n"
    "ds_read_b64_tr_b16 v[80:81], v140 offset:32768\n"
    "ds_read_b64_tr_b16 v[82:83], v140 offset:33792\n"
    "v_mfma_f32_32x32x16_bf16 v[32:47], v[112:115], v[104:107], v[32:47]\n"
    "ds_read_b128 v[64:67], v128 offset:32768\n"
    "ds_read_b128 v[72:75], v128 offset:36864\n"
    "v_mfma_f32_32x32x16_bf16 v[16:31], v[120:123], v[96:99], v[16:31]\n"
    "ds_read_b64_tr_b16 v[88:89], v141 offset:32768\n"
    "ds_read_b64_tr_b16 v[90:91], v141 offset:33792\n"
    "v_mfma_f32_32x32x16_bf16 v[48:63], v[120:123], v[104:107], v[48:63]\n"
    "ds_read_b64_tr_b16 v[84:85], v140 offset:36864\n"
    "ds_read_b64_tr_b16 v[86:87], v140 offset:37888\n"
    "v_mfma_f32_32x32x16_bf16 v[0:15], v[116:119], v[100:103], v[0:15]\n"
    "ds_read_b128 v[68:71], v129 offset:32768\n"
    "ds_read_b128 v[76:79], v129 offset:36864\n"
    "v_mfma_f32_32x32x16_bf16 v[32:47], v[116:119], v[108:111], v[32:47]\n"
    "ds_read_b64_tr_b16 v[92:93], v141 offset:36864\n"
    "ds_read_b64_tr_b16 v[94:95], v141 offset:37888\n"
    "v_mfma_f32_32x32x16_bf16 v[16:31], v[124:127], v[100:103], v[16:31]\n"
    "v_mfma_f32_32x32x16_bf16 v[48:63], v[124:127], v[108:111], v[48:63]\n"
    "s_waitcnt lgkmcnt(9)\n"
    "v_mfma_f32_32x32x16_bf16 v[0:15], v[80:83], v[64:67], v[0:15]\n"
    "ds_read_b64_tr_b16 v[112:113], v140 offset:40960\n"
    "ds_read_b64_tr_b16 v[114:115], v140 offset:41984\n"
    "s_waitcnt lgkmcnt(10)\n"
    "v_mfma_f32_32x32x16_bf16 v[32:47], v[80:83], v[72:75], v[32:47]\n"
    "ds_read_b128 v[96:99], v130 offset:32768\n"
    "ds_read_b128 v[104:107], v130 offset:36864\n"
    "s_waitcnt lgkmcnt(10)\n"
    "v_mfma_f32_32x32x16_bf16 v[16:31], v[88:91], v[64:67], v[16:31]\n"
    "ds_read_b64_tr_b16 v[120:121], v141 offset:40960\n"
    "ds_read_b64_tr_b16 v[122:123], v141 offset:41984\n"
    "v_mfma_f32_32x32x16_bf16 v[48:63], v[88:91], v[72:75], v[48:63]\n"
    "ds_read_b64_tr_b16 v[116:117], v140 offset:45056\n"
    "ds_read_b64_tr_b16 v[118:119], v140 offset:46080\n"
    "s_waitcnt lgkmcnt(11)\n"
    "v_mfma_f32_32x32x16_bf16 v[0:15], v[84:87], v[68:71], v[0:15]\n"
    "ds_read_b128 v[100:103], v131 offset:32768\n"
    "ds_read_b128 v[108:111], v131 offset:36864\n"
    "s_waitcnt lgkmcnt(12)\n"
    "v_mfma_f32_32x32x16_bf16 v[32:47], v[84:87], v[76:79], v[32:47]\n"
    "ds_read_b64_tr_b16 v[124:125], v141 offset:45056\n"
    "ds_read_b64_tr_b16 v[126:127], v141 offset:46080\n"
    "s_waitcnt lgkmcnt(12)\n"
    "v_mfma_f32_32x32x16_bf16 v[16:31], v[92:95], v[68:71], v[16:31]\n"
    "v_mfma_f32_32x32x16_bf16 v[48:63], v[92:95], v[76:79], v[48:63]\n"
    "s_waitcnt lgkmcnt(0)\n"
    "s_barrier\n"
    "v_mfma_f32_32x32x16_bf16 v[0:15], v[112:115], v[96:99], v[0:15]\n"
    "ds_read_b64_tr_b16 v[80:81], v142 offset:0\n"
    "ds_read_b64_tr_b16 v[82:83], v142 offset:1024\n"
    "v_mfma_f32_32x32x16_bf16 v[32:47], v[112:115], v[104:107], v[32:47]\n"
    "ds_read_b128 v[64:67], v132 offset:0\n"
    "ds_read_b128 v[72:75], v132 offset:4096\n"
    "v_mfma_f32_32x32x16_bf16 v[16:31], v[120:123], v[96:99], v[16:31]\n"
    "ds_read_b64_tr_b16 v[88:89], v143 offset:0\n"
    "ds_read_b64_tr_b16 v[90:91], v143 offset:1024\n"
    "v_mfma_f32_32x32x16_bf16 v[48:63], v[120:123], v[104:107], v[48:63]\n"
    "ds_read_b64_tr_b16 v[84:85], v142 offset:4096\n"
    "ds_read_b64_tr_b16 v[86:87], v142 offset:5120\n"
    "v_mfma_f32_32x32x16_bf16 v[0:15], v[116:119], v[100:103], v[0:15]\n"
    "ds_read_b128 v[68:71], v133 offset:0\n"
    "ds_read_b128 v[76:79], v133 offset:4096\n"
    "v_mfma_f32_32x32x16_bf16 v[32:47], v[116:119], v[108:111], v[32:47]\n"
    "ds_read_b64_tr_b16 v[92:93], v143 offset:4096\n"
    "ds_read_b64_tr_b16 v[94:95], v143 offset:5120\n"
    "v_mfma_f32_32x32x16_bf16 v[16:31], v[124:127], v[100:103], v[16:31]\n"
    "v_mfma_f32_32x32x16_bf16 v[48:63], v[124:127], v[108:111], v[48:63]\n"
    "s_nop 15\n"

        P3_OPS : P3_INS : "memory");
    float* xch = a.xch + ((size_t)tile * 4 + w) * 4096 + lane * 4;
    if constexpr (KH) {
        { int n = 1;
          asm volatile(
    "s_nop 1\n"
    "9:\n"
    "s_waitcnt lgkmcnt(9)\n"
    "v_mfma_f32_32x32x16_bf16 v[0:15], v[80:83], v[64:67], v[0:15]\n"
    "ds_read_b64_tr_b16 v[112:113], v142 offset:8192\n"
    "ds_read_b64_tr_b16 v[114:115], v142 offset:9216\n"
    "s_waitcnt lgkmcnt(10)\n"
    "v_mfma_f32_32x32x16_bf16 v[32:47], v[80:83], v[72:75], v[32:47]\n"
    "ds_read_b128 v[96:99], v134 offset:0\n"
    "ds_read_b128 v[104:107], v134 offset:4096\n"
    "s_waitcnt lgkmcnt(10)\n"
    "v_mfma_f32_32x32x16_bf16 v[16:31], v[88:91], v[64:67], v[16:31]\n"
    "ds_read_b64_tr_b16 v[120:121], v143 offset:8192\n"
    "ds_read_b64_tr_b16 v[122:123], v143 offset:9216\n"
    "v_mfma_f32_32x32x16_bf16 v[48:63], v[88:91], v[72:75], v[48:63]\n"
    "ds_read_b64_tr_b16 v[116:117], v142 offset:12288\n"
    "ds_read_b64_tr_b16 v[118:119], v142 offset:13312\n"
    "s_waitcnt lgkmcnt(11)\n"
    "v_mfma_f32_32x32x16_bf16 v[0:15], v[84:87], v[68:71], v[0:15]\n"
    "ds_read_b128 v[100:103], v135 offset:0\n"
    "ds_read_b128 v[108:111], v135 offset:4096\n"
    "s_waitcnt lgkmcnt(12)\n"
    "v_mfma_f32_32x32x16_bf16 v[32:47], v[84:87], v[76:79], v[32:47]\n"
    "ds_read_b64_tr_b16 v[124:125], v143 offset:12288\n"
    "ds_read_b64_tr_b16 v[126:127], v143 offset:13312\n"
    "s_waitcnt lgkmcnt(12)\n"
    "v_mfma_f32_32x32x16_bf16 v[16:31], v[92:95], v[68:71], v[16:31]\n"
    "v_mfma_f32_32x32x16_bf16 v[48:63], v[92:95], v[76:79], v[48:63]\n"
    "s_waitcnt lgkmcnt(0)\n"
    "s_barrier\n"
    "v_mfma_f32_32x32x16_bf16 v[0:15], v[112:115], v[96:99], v[0:15]\n"
    "ds_read_b64_tr_b16 v[80:81], v142 offset:32768\n"
    "ds_read_b64_tr_b16 v[82:83], v142 offset:33792\n"
    "v_mfma_f32_32x32x16_bf16 v[32:47], v[112:115], v[104:107], v[32:47]\n"
    "ds_read_b128 v[64:67], v132 offset:32768\n"
    "ds_read_b128 v[72:75], v132 offset:36864\n"
    "v_mfma_f32_32x32x16_bf16 v[16:31], v[120:123], v[96:99], v[16:31]\n"
    "ds_read_b64_tr_b16 v[88:89], v143 offset:32768\n"
    "ds_read_b64_tr_b16 v[90:91], v143 offset:33792\n"
    "v_mfma_f32_32x32x16_bf16 v[48:63], v[120:123], v[104:107], v[48:63]\n"
    "ds_read_b64_tr_b16 v[84:85], v142 offset:36864\n"
    "ds_read_b64_tr_b16 v[86:87], v142 offset:37888\n"
    "v_mfma_f32_32x32x16_bf16 v[0:15], v[116:119], v[100:103], v[0:15]\n"
    "ds_read_b128 v[68:71], v133 offset:32768\n"
    "ds_read_b128 v[76:79], v133 offset:36864\n"
    "v_mfma_f32_32x32x16_bf16 v[32:47], v[116:119], v[108:111], v[32:47]\n"
    "ds_read_b64_tr_b16 v[92:93], v143 offset:36864\n"
    "ds_read_b64_tr_b16 v[94:95], v143 offset:37888\n"
    "v_mfma_f32_32x32x16_bf16 v[16:31], v[124:127], v[100:103], v[16:31]\n"
    "v_mfma_f32_32x32x16_bf16 v[48:63], v[124:127], v[108:111], v[48:63]\n"
    "s_waitcnt lgkmcnt(9)\n"
    "v_mfma_f32_32x32x16_bf16 v[0:15], v[80:83], v[64:67], v[0:15]\n"
    "ds_read_b64_tr_b16 v[112:113], v142 offset:40960\n"
    "ds_read_b64_tr_b16 v[114:115], v142 offset:41984\n"
    "s_waitcnt lgkmcnt(10)\n"
    "v_mfma_f32_32x32x16_bf16 v[32:47], v[80:83], v[72:75], v[32:47]\n"
    "ds_read_b128 v[96:99], v134 offset:32768\n"
    "ds_read_b128 v[104:107], v134 offset:36864\n"
    "s_waitcnt lgkmcnt(10)\n"
    "v_mfma_f32_32x32x16_bf16 v[16:31], v[88:91], v[64:67], v[16:31]\n"
    "ds_read_b64_tr_b16 v[120:121], v143 offset:40960\n"
    "ds_read_b64_tr_b16 v[122:123], v143 offset:41984\n"
    "v_mfma_f32_32x32x16_bf16 v[48:63], v[88:91], v[72:75], v[48:63]\n"
    "ds_read_b64_tr_b16 v[116:117], v142 offset:45056\n"
    "ds_read_b64_tr_b16 v[118:119], v142 offset:46080\n"
    "s_waitcnt lgkmcnt(11)\n"
    "v_mfma_f32_32x32x16_bf16 v[0:15], v[84:87], v[68:71], v[0:15]\n"
    "ds_read_b128 v[100:103], v135 offset:32768\n"
    "ds_read_b128 v[108:111], v135 offset:36864\n"
    "s_waitcnt lgkmcnt(12)\n"
    "v_mfma_f32_32x32x16_bf16 v[32:47], v[84:87], v[76:79], v[32:47]\n"
    "ds_read_b64_tr_b16 v[124:125], v143 offset:45056\n"
    "ds_read_b64_tr_b16 v[126:127], v143 offset:46080\n"
    "s_waitcnt lgkmcnt(12)\n"
    "v_mfma_f32_32x32x16_bf16 v[16:31], v[92:95], v[68:71], v[16:31]\n"
    "v_mfma_f32_32x32x16_bf16 v[48:63], v[92:95], v[76:79], v[48:63]\n"
    "s_waitcnt lgkmcnt(0)\n"
    "s_barrier\n"
    "v_mfma_f32_32x32x16_bf16 v[0:15], v[112:115], v[96:99], v[0:15]\n"
    "ds_read_b64_tr_b16 v[80:81], v144 offset:0\n"
    "ds_read_b64_tr_b16 v[82:83], v144 offset:1024\n"
    "v_mfma_f32_32x32x16_bf16 v[32:47], v[112:115], v[104:107], v[32:47]\n"
    "ds_read_b128 v[64:67], v136 offset:0\n"
    "ds_read_b128 v[72:75], v136 offset:4096\n"
    "v_mfma_f32_32x32x16_bf16 v[16:31], v[120:123], v[96:99], v[16:31]\n"
    "ds_read_b64_tr_b16 v[88:89], v145 offset:0\n"
    "ds_read_b64_tr_b16 v[90:91], v145 offset:1024\n"
    "v_mfma_f32_32x32x16_bf16 v[48:63], v[120:123], v[104:107], v[48:63]\n"
    "ds_read_b64_tr_b16 v[84:85], v144 offset:4096\n"
    "ds_read_b64_tr_b16 v[86:87], v144 offset:5120\n"
    "v_mfma_f32_32x32x16_bf16 v[0:15], v[116:119], v[100:103], v[0:15]\n"
    "ds_read_b128 v[68:71], v137 offset:0\n"
    "ds_read_b128 v[76:79], v137 offset:4096\n"
    "v_mfma_f32_32x32x16_bf16 v[32:47], v[116:119], v[108:111], v[32:47]\n"
    "ds_read_b64_tr_b16 v[92:93], v145 offset:4096\n"
    "ds_read_b64_tr_b16 v[94:95], v145 offset:5120\n"
    "v_mfma_f32_32x32x16_bf16 v[16:31], v[124:127], v[100:103], v[16:31]\n"
    "v_mfma_f32_32x32x16_bf16 v[48:63], v[124:127], v[108:111], v[48:63]\n"
    "s_waitcnt lgkmcnt(9)\n"
    "v_mfma_f32_32x32x16_bf16 v[0:15], v[80:83], v[64:67], v[0:15]\n"
    "ds_read_b64_tr_b16 v[112:113], v144 offset:8192\n"
    "ds_read_b64_tr_b16 v[114:115], v144 offset:9216\n"
    "s_waitcnt lgkmcnt(10)\n"
    "v_mfma_f32_32x32x16_bf16 v[32:47], v[80:83], v[72:75], v[32:47]\n"
    "ds_read_b128 v[96:99], v138 offset:0\n"
    "ds_read_b128 v[104:107], v138 offset:4096\n"
    "s_waitcnt lgkmcnt(10)\n"
    "v_mfma_f32_32x32x16_bf16 v[16:31], v[88:91], v[64:67], v[16:31]\n"
    "ds_read_b64_tr_b16 v[120:121], v145 offset:8192\n"
    "ds_read_b64_tr_b16 v[122:123], v145 offset:9216\n"
    "v_mfma_f32_32x32x16_bf16 v[48:63], v[88:91], v[72:75], v[48:63]\n"
    "ds_read_b64_tr_b16 v[116:117], v144 offset:12288\n"
    "ds_read_b64_tr_b16 v[118:119], v144 offset:13312\n"
    "s_waitcnt lgkmcnt(11)\n"
    "v_mfma_f32_32x32x16_bf16 v[0:15], v[84:87], v[68:71], v[0:15]\n"
    "ds_read_b128 v[100:103], v139 offset:0\n"
    "ds_read_b128 v[108:111], v139 offset:4096\n"
    "s_waitcnt lgkmcnt(12)\n"
    "v_mfma_f32_32x32x16_bf16 v[32:47], v[84:87], v[76:79], v[32:47]\n"
    "ds_read_b64_tr_b16 v[124:125], v145 offset:12288\n"
    "ds_read_b64_tr_b16 v[126:127], v145 offset:13312\n"
    "s_waitcnt lgkmcnt(12)\n"
    "v_mfma_f32_32x32x16_bf16 v[16:31], v[92:95], v[68:71], v[16:31]\n"
    "v_mfma_f32_32x32x16_bf16 v[48:63], v[92:95], v[76:79], v[48:63]\n"
    "s_waitcnt lgkmcnt(0)\n"
    "s_barrier\n"
    "v_mfma_f32_32x32x16_bf16 v[0:15], v[112:115], v[96:99], v[0:15]\n"
    "ds_read_b64_tr_b16 v[80:81], v140 offset:0\n"
    "ds_read_b64_tr_b16 v[82:83], v140 offset:1024\n"
    "v_mfma_f32_32x32x16_bf16 v[32:47], v[112:115], v[104:107], v[32:47]\n"
    "ds_read_b128 v[64:67], v128 offset:0\n"
    "ds_read_b128 v[72:75], v128 offset:4096\n"
    "v_mfma_f32_32x32x16_bf16 v[16:31], v[120:123], v[96:99], v[16:31]\n"
    "ds_read_b64_tr_b16 v[88:89], v141 offset:0\n"
    "ds_read_b64_tr_b16 v[90:91], v141 offset:1024\n"
    "v_mfma_f32_32x32x16_bf16 v[48:63], v[120:123], v[104:107], v[48:63]\n"
    "ds_read_b64_tr_b16 v[84:85], v140 offset:4096\n"
    "ds_read_b64_tr_b16 v[86:87], v140 offset:5120\n"
    "v_mfma_f32_32x32x16_bf16 v[0:15], v[116:119], v[100:103], v[0:15]\n"
    "ds_read_b128 v[68:71], v129 offset:0\n"
    "ds_read_b128 v[76:79], v129 offset:4096\n"
    "v_mfma_f32_32x32x16_bf16 v[32:47], v[116:119], v[108:111], v[32:47]\n"
    "ds_read_b64_tr_b16 v[92:93], v141 offset:4096\n"
    "ds_read_b64_tr_b16 v[94:95], v141 offset:5120\n"
    "v_mfma_f32_32x32x16_bf16 v[16:31], v[124:127], v[100:103], v[16:31]\n"
    "v_mfma_f32_32x32x16_bf16 v[48:63], v[124:127], v[108:111], v[48:63]\n"
    "s_waitcnt lgkmcnt(9)\n"
    "v_mfma_f32_32x32x16_bf16 v[0:15], v[80:83], v[64:67], v[0:15]\n"
    "ds_read_b64_tr_b16 v[112:113], v140 offset:8192\n"
    "ds_read_b64_tr_b16 v[114:115], v140 offset:9216\n"
    "s_waitcnt lgkmcnt(10)\n"
    "v_mfma_f32_32x32x16_bf16 v[32:47], v[80:83], v[72:75], v[32:47]\n"
    "ds_read_b128 v[96:99], v130 offset:0\n"
    "ds_read_b128 v[104:107], v130 offset:4096\n"
    "s_waitcnt lgkmcnt(10)\n"
    "v_mfma_f32_32x32x16_bf16 v[16:31], v[88:91], v[64:67], v[16:31]\n"
    "ds_read_b64_tr_b16 v[120:121], v141 offset:8192\n"
    "ds_read_b64_tr_b16 v[122:123], v141 offset:9216\n"
    "v_mfma_f32_32x32x16_bf16 v[48:63], v[88:91], v[72:75], v[48:63]\n"
    "ds_read_b64_tr_b16 v[116:117], v140 offset:12288\n"
    "ds_read_b64_tr_b16 v[118:119], v140 offset:13312\n"
    "s_waitcnt lgkmcnt(11)\n"
    "v_mfma_f32_32x32x16_bf16 v[0:15], v[84:87], v[68:71], v[0:15]\n"
    "ds_read_b128 v[100:103], v131 offset:0\n"
    "ds_read_b128 v[108:111], v131 offset:4096\n"
    "s_waitcnt lgkmcnt(12)\n"
    "v_mfma_f32_32x32x16_bf16 v[32:47], v[84:87], v[76:79], v[32:47]\n"
    "ds_read_b64_tr_b16 v[124:125], v141 offset:12288\n"
    "ds_read_b64_tr_b16 v[126:127], v141 offset:13312\n"
    "s_waitcnt lgkmcnt(12)\n"
    "v_mfma_f32_32x32x16_bf16 v[16:31], v[92:95], v[68:71], v[16:31]\n"
    "v_mfma_f32_32x32x16_bf16 v[48:63], v[92:95], v[76:79], v[48:63]\n"
    "s_waitcnt lgkmcnt(0)\n"
    "s_barrier\n"
    "v_mfma_f32_32x32x16_bf16 v[0:15], v[112:115], v[96:99], v[0:15]\n"
    "ds_read_b64_tr_b16 v[80:81], v140 offset:32768\n"
    "ds_read_b64_tr_b16 v[82:83], v140 offset:33792\n"
    "v_mfma_f32_32x32x16_bf16 v[32:47], v[112:115], v[104:107], v[32:47]\n"
    "ds_read_b128 v[64:67], v128 offset:32768\n"
    "ds_read_b128 v[72:75], v128 offset:36864\n"
    "v_mfma_f32_32x32x16_bf16 v[16:31], v[120:123], v[96:99], v[16:31]\n"
    "ds_read_b64_tr_b16 v[88:89], v141 offset:32768\n"
    "ds_read_b64_tr_b16 v[90:91], v141 offset:33792\n"
    "v_mfma_f32_32x32x16_bf16 v[48:63], v[120:123], v[104:107], v[48:63]\n"
    "ds_read_b64_tr_b16 v[84:85], v140 offset:36864\n"
    "ds_read_b64_tr_b16 v[86:87], v140 offset:37888\n"
    "v_mfma_f32_32x32x16_bf16 v[0:15], v[116:119], v[100:103], v[0:15]\n"
    "ds_read_b128 v[68:71], v129 offset:32768\n"
    "ds_read_b128 v[76:79], v129 offset:36864\n"
    "v_mfma_f32_32x32x16_bf16 v[32:47], v[116:119], v[108:111], v[32:47]\n"
    "ds_read_b64_tr_b16 v[92:93], v141 offset:36864\n"
    "ds_read_b64_tr_b16 v[94:95], v141 offset:37888\n"
    "v_mfma_f32_32x32x16_bf16 v[16:31], v[124:127], v[100:103], v[16:31]\n"
    "v_mfma_f32_32x32x16_bf16 v[48:63], v[124:127], v[108:111], v[48:63]\n"
    "s_waitcnt lgkmcnt(9)\n"
    "v_mfma_f32_32x32x16_bf16 v[0:15], v[80:83], v[64:67], v[0:15]\n"
    "ds_read_b64_tr_b16 v[112:113], v140 offset:40960\n"
    "ds_read_b64_tr_b16 v[114:115], v140 offset:41984\n"
    "s_waitcnt lgkmcnt(10)\n"
    "v_mfma_f32_32x32x16_bf16 v[32:47], v[80:83], v[72:75], v[32:47]\n"
    "ds_read_b128 v[96:99], v130 offset:32768\n"
    "ds_read_b128 v[104:107], v130 offset:36864\n"
    "s_waitcnt lgkmcnt(10)\n"
    "v_mfma_f32_32x32x16_bf16 v[16:31], v[88:91], v[64:67], v[16:31]\n"
    "ds_read_b64_tr_b16 v[120:121], v141 offset:40960\n"
    "ds_read_b64_tr_b16 v[122:123], v141 offset:41984\n"
    "v_mfma_f32_32x32x16_bf16 v[48:63], v[88:91], v[72:75], v[48:63]\n"
    "ds_read_b64_tr_b16 v[116:117], v140 offset:45056\n"
    "ds_read_b64_tr_b16 v[118:119], v140 offset:46080\n"
    "s_waitcnt lgkmcnt(11)\n"
    "v_mfma_f32_32x32x16_bf16 v[0:15], v[84:87], v[68:71], v[0:15]\n"
    "ds_read_b128 v[100:103], v131 offset:32768\n"
    "ds_read_b128 v[108:111], v131 offset:36864\n"
    "s_waitcnt lgkmcnt(12)\n"
    "v_mfma_f32_32x32x16_bf16 v[32:47], v[84:87], v[76:79], v[32:47]\n"
    "ds_read_b64_tr_b16 v[124:125], v141 offset:45056\n"
    "ds_read_b64_tr_b16 v[126:127], v141 offset:46080\n"
    "s_waitcnt lgkmcnt(12)\n"
    "v_mfma_f32_32x32x16_bf16 v[16:31], v[92:95], v[68:71], v[16:31]\n"
    "v_mfma_f32_32x32x16_bf16 v[48:63], v[92:95], v[76:79], v[48:63]\n"
    "s_waitcnt lgkmcnt(0)\n"
    "s_barrier\n"
    "v_mfma_f32_32x32x16_bf16 v[0:15], v[112:115], v[96:99], v[0:15]\n"
    "ds_read_b64_tr_b16 v[80:81], v142 offset:0\n"
    "ds_read_b64_tr_b16 v[82:83], v142 offset:1024\n"
    "v_mfma_f32_32x32x16_bf16 v[32:47], v[112:115], v[104:107], v[32:47]\n"
    "ds_read_b128 v[64:67], v132 offset:0\n"
    "ds_read_b128 v[72:75], v132 offset:4096\n"
    "v_mfma_f32_32x32x16_bf16 v[16:31], v[120:123], v[96:99], v[16:31]\n"
    "ds_read_b64_tr_b16 v[88:89], v143 offset:0\n"
    "ds_read_b64_tr_b16 v[90:91], v143 offset:1024\n"
    "v_mfma_f32_32x32x16_bf16 v[48:63], v[120:123], v[104:107], v[48:63]\n"
    "ds_read_b64_tr_b16 v[84:85], v142 offset:4096\n"
    "ds_read_b64_tr_b16 v[86:87], v142 offset:5120\n"
    "v_mfma_f32_32x32x16_bf16 v[0:15], v[116:119], v[100:103], v[0:15]\n"
    "ds_read_b128 v[68:71], v133 offset:0\n"
    "ds_read_b128 v[76:79], v133 offset:4096\n"
    "v_mfma_f32_32x32x16_bf16 v[32:47], v[116:119], v[108:111], v[32:47]\n"
    "ds_read_b64_tr_b16 v[92:93], v143 offset:4096\n"
    "ds_read_b64_tr_b16 v[94:95], v143 offset:5120\n"
    "v_mfma_f32_32x32x16_bf16 v[16:31], v[124:127], v[100:103], v[16:31]\n"
    "v_mfma_f32_32x32x16_bf16 v[48:63], v[124:127], v[108:111], v[48:63]\n"
    "s_sub_u32 %[n], %[n], 1\n"
    "s_cmp_lg_u32 %[n], 0\n"
    "s_cbranch_scc1 9b\n"
    "s_nop 15\n"

            P3_OPS, [n] "+s"(n) : P3_INS : "memory", "scc"); }
        asm volatile(
    "s_nop 1\n"
    "s_waitcnt lgkmcnt(9)\n"
    "v_mfma_f32_32x32x16_bf16 v[0:15], v[80:83], v[64:67], v[0:15]\n"
    "ds_read_b64_tr_b16 v[112:113], v142 offset:8192\n"
    "ds_read_b64_tr_b16 v[114:115], v142 offset:9216\n"
    "s_waitcnt lgkmcnt(10)\n"
    "v_mfma_f32_32x32x16_bf16 v[32:47], v[80:83], v[72:75], v[32:47]\n"
    "ds_read_b128 v[96:99], v134 offset:0\n"
    "ds_read_b128 v[104:107], v134 offset:4096\n"
    "s_waitcnt lgkmcnt(10)\n"
    "v_mfma_f32_32x32x16_bf16 v[16:31], v[88:91], v[64:67], v[16:31]\n"
    "ds_read_b64_tr_b16 v[120:121], v143 offset:8192\n"
    "ds_read_b64_tr_b16 v[122:123], v143 offset:9216\n"
    "v_mfma_f32_32x32x16_bf16 v[48:63], v[88:91], v[72:75], v[48:63]\n"
    "ds_read_b64_tr_b16 v[116:117], v142 offset:12288\n"
    "ds_read_b64_tr_b16 v[118:119], v142 offset:13312\n"
    "s_waitcnt lgkmcnt(11)\n"
    "v_mfma_f32_32x32x16_bf16 v[0:15], v[84:87], v[68:71], v[0:15]\n"
    "ds_read_b128 v[100:103], v135 offset:0\n"
    "ds_read_b128 v[108:111], v135 offset:4096\n"
    "s_waitcnt lgkmcnt(12)\n"
    "v_mfma_f32_32x32x16_bf16 v[32:47], v[84:87], v[76:79], v[32:47]\n"
    "ds_read_b64_tr_b16 v[124:125], v143 offset:12288\n"
    "ds_read_b64_tr_b16 v[126:127], v143 offset:13312\n"
    "s_waitcnt lgkmcnt(12)\n"
    "v_mfma_f32_32x32x16_bf16 v[16:31], v[92:95], v[68:71], v[16:31]\n"
    "v_mfma_f32_32x32x16_bf16 v[48:63], v[92:95], v[76:79], v[48:63]\n"
    "s_waitcnt lgkmcnt(0)\n"
    "s_barrier\n"
    "v_mfma_f32_32x32x16_bf16 v[0:15], v[112:115], v[96:99], v[0:15]\n"
    "ds_read_b64_tr_b16 v[80:81], v142 offset:32768\n"
    "ds_read_b64_tr_b16 v[82:83], v142 offset:33792\n"
    "v_mfma_f32_32x32x16_bf16 v[32:47], v[112:115], v[104:107], v[32:47]\n"
    "ds_read_b128 v[64:67], v132 offset:32768\n"
    "ds_read_b128 v[72:75], v132 offset:36864\n"
    "v_mfma_f32_32x32x16_bf16 v[16:31], v[120:123], v[96:99], v[16:31]\n"
    "ds_read_b64_tr_b16 v[88:89], v143 offset:32768\n"
    "ds_read_b64_tr_b16 v[90:91], v143 offset:33792\n"
    "v_mfma_f32_32x32x16_bf16 v[48:63], v[120:123], v[104:107], v[48:63]\n"
    "ds_read_b64_tr_b16 v[84:85], v142 offset:36864\n"
    "ds_read_b64_tr_b16 v[86:87], v142 offset:37888\n"
    "v_mfma_f32_32x32x16_bf16 v[0:15], v[116:119], v[100:103], v[0:15]\n"
    "ds_read_b128 v[68:71], v133 offset:32768\n"
    "ds_read_b128 v[76:79], v133 offset:36864\n"
    "v_mfma_f32_32x32x16_bf16 v[32:47], v[116:119], v[108:111], v[32:47]\n"
    "ds_read_b64_tr_b16 v[92:93], v143 offset:36864\n"
    "ds_read_b64_tr_b16 v[94:95], v143 offset:37888\n"
    "v_mfma_f32_32x32x16_bf16 v[16:31], v[124:127], v[100:103], v[16:31]\n"
    "v_mfma_f32_32x32x16_bf16 v[48:63], v[124:127], v[108:111], v[48:63]\n"
    "s_waitcnt lgkmcnt(9)\n"
    "v_mfma_f32_32x32x16_bf16 v[0:15], v[80:83], v[64:67], v[0:15]\n"
    "ds_read_b64_tr_b16 v[112:113], v142 offset:40960\n"
    "ds_read_b64_tr_b16 v[114:115], v142 offset:41984\n"
    "s_waitcnt lgkmcnt(10)\n"
    "v_mfma_f32_32x32x16_bf16 v[32:47], v[80:83], v[72:75], v[32:47]\n"
    "ds_read_b128 v[96:99], v134 offset:32768\n"
    "ds_read_b128 v[104:107], v134 offset:36864\n"
    "s_waitcnt lgkmcnt(10)\n"
    "v_mfma_f32_32x32x16_bf16 v[16:31], v[88:91], v[64:67], v[16:31]\n"
    "ds_read_b64_tr_b16 v[120:121], v143 offset:40960\n"
    "ds_read_b64_tr_b16 v[122:123], v143 offset:41984\n"
    "v_mfma_f32_32x32x16_bf16 v[48:63], v[88:91], v[72:75], v[48:63]\n"
    "ds_read_b64_tr_b16 v[116:117], v142 offset:45056\n"
    "ds_read_b64_tr_b16 v[118:119], v142 offset:46080\n"
    "s_waitcnt lgkmcnt(11)\n"
    "v_mfma_f32_32x32x16_bf16 v[0:15], v[84:87], v[68:71], v[0:15]\n"
    "ds_read_b128 v[100:103], v135 offset:32768\n"
    "ds_read_b128 v[108:111], v135 offset:36864\n"
    "s_waitcnt lgkmcnt(12)\n"
    "v_mfma_f32_32x32x16_bf16 v[32:47], v[84:87], v[76:79], v[32:47]\n"
    "ds_read_b64_tr_b16 v[124:125], v143 offset:45056\n"
    "ds_read_b64_tr_b16 v[126:127], v143 offset:46080\n"
    "s_waitcnt lgkmcnt(12)\n"
    "v_mfma_f32_32x32x16_bf16 v[16:31], v[92:95], v[68:71], v[16:31]\n"
    "v_mfma_f32_32x32x16_bf16 v[48:63], v[92:95], v[76:79], v[48:63]\n"
    "s_waitcnt lgkmcnt(0)\n"
    "s_barrier\n"
    "v_mfma_f32_32x32x16_bf16 v[0:15], v[112:115], v[96:99], v[0:15]\n"
    "ds_read_b64_tr_b16 v[80:81], v144 offset:0\n"
    "ds_read_b64_tr_b16 v[82:83], v144 offset:1024\n"
    "v_mfma_f32_32x32x16_bf16 v[32:47], v[112:115], v[104:107], v[32:47]\n"
    "ds_read_b128 v[64:67], v136 offset:0\n"
    "ds_read_b128 v[72:75], v136 offset:4096\n"
    "v_mfma_f32_32x32x16_bf16 v[16:31], v[120:123], v[96:99], v[16:31]\n"
    "ds_read_b64_tr_b16 v[88:89], v145 offset:0\n"
    "ds_read_b64_tr_b16 v[90:91], v145 offset:1024\n"
    "v_mfma_f32_32x32x16_bf16 v[48:63], v[120:123], v[104:107], v[48:63]\n"
    "ds_read_b64_tr_b16 v[84:85], v144 offset:4096\n"
    "ds_read_b64_tr_b16 v[86:87], v144 offset:5120\n"
    "v_mfma_f32_32x32x16_bf16 v[0:15], v[116:119], v[100:103], v[0:15]\n"
    "ds_read_b128 v[68:71], v137 offset:0\n"
    "ds_read_b128 v[76:79], v137 offset:4096\n"
    "v_mfma_f32_32x32x16_bf16 v[32:47], v[116:119], v[108:111], v[32:47]\n"
    "ds_read_b64_tr_b16 v[92:93], v145 offset:4096\n"
    "ds_read_b64_tr_b16 v[94:95], v145 offset:5120\n"
    "v_mfma_f32_32x32x16_bf16 v[16:31], v[124:127], v[100:103], v[16:31]\n"
    "v_mfma_f32_32x32x16_bf16 v[48:63], v[124:127], v[108:111], v[48:63]\n"
    "s_waitcnt lgkmcnt(9)\n"
    "v_mfma_f32_32x32x16_bf16 v[0:15], v[80:83], v[64:67], v[0:15]\n"
    "ds_read_b64_tr_b16 v[112:113], v144 offset:8192\n"
    "ds_read_b64_tr_b16 v[114:115], v144 offset:9216\n"
    "s_waitcnt lgkmcnt(10)\n"
    "v_mfma_f32_32x32x16_bf16 v[32:47], v[80:83], v[72:75], v[32:47]\n"
    "ds_read_b128 v[96:99], v138 offset:0\n"
    "ds_read_b128 v[104:107], v138 offset:4096\n"
    "s_waitcnt lgkmcnt(10)\n"
    "v_mfma_f32_32x32x16_bf16 v[16:31], v[88:91], v[64:67], v[16:31]\n"
    "ds_read_b64_tr_b16 v[120:121], v145 offset:8192\n"
    "ds_read_b64_tr_b16 v[122:123], v145 offset:9216\n"
    "v_mfma_f32_32x32x16_bf16 v[48:63], v[88:91], v[72:75], v[48:63]\n"
    "ds_read_b64_tr_b16 v[116:117], v144 offset:12288\n"
    "ds_read_b64_tr_b16 v[118:119], v144 offset:13312\n"
    "s_waitcnt lgkmcnt(11)\n"
    "v_mfma_f32_32x32x16_bf16 v[0:15], v[84:87], v[68:71], v[0:15]\n"
    "ds_read_b128 v[100:103], v139 offset:0\n"
    "ds_read_b128 v[108:111], v139 offset:4096\n"
    "s_waitcnt lgkmcnt(12)\n"
    "v_mfma_f32_32x32x16_bf16 v[32:47], v[84:87], v[76:79], v[32:47]\n"
    "ds_read_b64_tr_b16 v[124:125], v145 offset:12288\n"
    "ds_read_b64_tr_b16 v[126:127], v145 offset:13312\n"
    "s_waitcnt lgkmcnt(12)\n"
    "v_mfma_f32_32x32x16_bf16 v[16:31], v[92:95], v[68:71], v[16:31]\n"
    "v_mfma_f32_32x32x16_bf16 v[48:63], v[92:95], v[76:79], v[48:63]\n"
    "s_waitcnt lgkmcnt(0)\n"
    "s_barrier\n"
    "v_mfma_f32_32x32x16_bf16 v[0:15], v[112:115], v[96:99], v[0:15]\n"
    "ds_read_b64_tr_b16 v[80:81], v140 offset:0\n"
    "ds_read_b64_tr_b16 v[82:83], v140 offset:1024\n"
    "v_mfma_f32_32x32x16_bf16 v[32:47], v[112:115], v[104:107], v[32:47]\n"
    "ds_read_b128 v[64:67], v128 offset:0\n"
    "ds_read_b128 v[72:75], v128 offset:4096\n"
    "v_mfma_f32_32x32x16_bf16 v[16:31], v[120:123], v[96:99], v[16:31]\n"
    "ds_read_b64_tr_b16 v[88:89], v141 offset:0\n"
    "ds_read_b64_tr_b16 v[90:91], v141 offset:1024\n"
    "v_mfma_f32_32x32x16_bf16 v[48:63], v[120:123], v[104:107], v[48:63]\n"
    "ds_read_b64_tr_b16 v[84:85], v140 offset:4096\n"
    "ds_read_b64_tr_b16 v[86:87], v140 offset:5120\n"
    "v_mfma_f32_32x32x16_bf16 v[0:15], v[116:119], v[100:103], v[0:15]\n"
    "ds_read_b128 v[68:71], v129 offset:0\n"
    "ds_read_b128 v[76:79], v129 offset:4096\n"
    "v_mfma_f32_32x32x16_bf16 v[32:47], v[116:119], v[108:111], v[32:47]\n"
    "ds_read_b64_tr_b16 v[92:93], v141 offset:4096\n"
    "ds_read_b64_tr_b16 v[94:95], v141 offset:5120\n"
    "v_mfma_f32_32x32x16_bf16 v[16:31], v[124:127], v[100:103], v[16:31]\n"
    "v_mfma_f32_32x32x16_bf16 v[48:63], v[124:127], v[108:111], v[48:63]\n"
    "s_nop 15\n"

            P3_OPS : P3_INS : "memory");
        P3_SCALE();

        asm volatile(
    "s_nop 1\n"
    "s_waitcnt lgkmcnt(9)\n"
    "v_mfma_f32_32x32x16_bf16 v[0:15], v[80:83], v[64:67], v[0:15]\n"
    "ds_read_b64_tr_b16 v[112:113], v140 offset:8192\n"
    "ds_read_b64_tr_b16 v[114:115], v140 offset:9216\n"
    "s_waitcnt lgkmcnt(10)\n"
    "v_mfma_f32_32x32x16_bf16 v[32:47], v[80:83], v[72:75], v[32:47]\n"
    "ds_read_b128 v[96:99], v130 offset:0\n"
    "ds_read_b128 v[104:107], v130 offset:4096\n"
    "s_waitcnt lgkmcnt(10)\n"
    "v_mfma_f32_32x32x16_bf16 v[16:31], v[88:91], v[64:67], v[16:31]\n"
    "ds_read_b64_tr_b16 v[120:121], v141 offset:8192\n"
    "ds_read_b64_tr_b16 v[122:123], v141 offset:9216\n"
    "v_mfma_f32_32x32x16_bf16 v[48:63], v[88:91], v[72:75], v[48:63]\n"
    "ds_read_b64_tr_b16 v[116:117], v140 offset:12288\n"
    "ds_read_b64_tr_b16 v[118:119], v140 offset:13312\n"
    "s_waitcnt lgkmcnt(11)\n"
    "v_mfma_f32_32x32x16_bf16 v[0:15], v[84:87], v[68:71], v[0:15]\n"
    "ds_read_b128 v[100:103], v131 offset:0\n"
    "ds_read_b128 v[108:111], v131 offset:4096\n"
    "s_waitcnt lgkmcnt(12)\n"
    "v_mfma_f32_32x32x16_bf16 v[32:47], v[84:87], v[76:79], v[32:47]\n"
    "ds_read_b64_tr_b16 v[124:125], v141 offset:12288\n"
    "ds_read_b64_tr_b16 v[126:127], v141 offset:13312\n"
    "s_waitcnt lgkmcnt(12)\n"
    "v_mfma_f32_32x32x16_bf16 v[16:31], v[92:95], v[68:71], v[16:31]\n"
    "v_mfma_f32_32x32x16_bf16 v[48:63], v[92:95], v[76:79], v[48:63]\n"
    "s_waitcnt lgkmcnt(0)\n"
    "s_barrier\n"
    "v_mfma_f32_32x32x16_bf16 v[0:15], v[112:115], v[96:99], v[0:15]\n"
    "ds_read_b64_tr_b16 v[80:81], v140 offset:32768\n"
    "ds_read_b64_tr_b16 v[82:83], v140 offset:33792\n"
    "v_mfma_f32_32x32x16_bf16 v[32:47], v[112:115], v[104:107], v[32:47]\n"
    "ds_read_b128 v[64:67], v128 offset:32768\n"
    "ds_read_b128 v[72:75], v128 offset:36864\n"
    "v_mfma_f32_32x32x16_bf16 v[16:31], v[120:123], v[96:99], v[16:31]\n"
    "ds_read_b64_tr_b16 v[88:89], v141 offset:32768\n"
    "ds_read_b64_tr_b16 v[90:91], v141 offset:33792\n"
    "v_mfma_f32_32x32x16_bf16 v[48:63], v[120:123], v[104:107], v[48:63]\n"
    "ds_read_b64_tr_b16 v[84:85], v140 offset:36864\n"
    "ds_read_b64_tr_b16 v[86:87], v140 offset:37888\n"
    "v_mfma_f32_32x32x16_bf16 v[0:15], v[116:119], v[100:103], v[0:15]\n"
    "ds_read_b128 v[68:71], v129 offset:32768\n"
    "ds_read_b128 v[76:79], v129 offset:36864\n"
    "v_mfma_f32_32x32x16_bf16 v[32:47], v[116:119], v[108:111], v[32:47]\n"
    "ds_read_b64_tr_b16 v[92:93], v141 offset:36864\n"
    "ds_read_b64_tr_b16 v[94:95], v141 offset:37888\n"
    "v_mfma_f32_32x32x16_bf16 v[16:31], v[124:127], v[100:103], v[16:31]\n"
    "v_mfma_f32_32x32x16_bf16 v[48:63], v[124:127], v[108:111], v[48:63]\n"
    "s_waitcnt lgkmcnt(9)\n"
    "v_mfma_f32_32x32x16_bf16 v[0:15], v[80:83], v[64:67], v[0:15]\n"
    "ds_read_b64_tr_b16 v[112:113], v140 offset:40960\n"
    "ds_read_b64_tr_b16 v[114:115], v140 offset:41984\n"
    "s_waitcnt lgkmcnt(10)\n"
    "v_mfma_f32_32x32x16_bf16 v[32:47], v[80:83], v[72:75], v[32:47]\n"
    "ds_read_b128 v[96:99], v130 offset:32768\n"
    "ds_read_b128 v[104:107], v130 offset:36864\n"
    "s_waitcnt lgkmcnt(10)\n"
    "v_mfma_f32_32x32x16_bf16 v[16:31], v[88:91], v[64:67], v[16:31]\n"
    "ds_read_b64_tr_b16 v[120:121], v141 offset:40960\n"
    "ds_read_b64_tr_b16 v[122:123], v141 offset:41984\n"
    "v_mfma_f32_32x32x16_bf16 v[48:63], v[88:91], v[72:75], v[48:63]\n"
    "ds_read_b64_tr_b16 v[116:117], v140 offset:45056\n"
    "ds_read_b64_tr_b16 v[118:119], v140 offset:46080\n"
    "s_waitcnt lgkmcnt(11)\n"
    "v_mfma_f32_32x32x16_bf16 v[0:15], v[84:87], v[68:71], v[0:15]\n"
    "ds_read_b128 v[100:103], v131 offset:32768\n"
    "ds_read_b128 v[108:111], v131 offset:36864\n"
    "s_waitcnt lgkmcnt(12)\n"
    "v_mfma_f32_32x32x16_bf16 v[32:47], v[84:87], v[76:79], v[32:47]\n"
    "ds_read_b64_tr_b16 v[124:125], v141 offset:45056\n"
    "ds_read_b64_tr_b16 v[126:127], v141 offset:46080\n"
    "s_waitcnt lgkmcnt(12)\n"
    "v_mfma_f32_32x32x16_bf16 v[16:31], v[92:95], v[68:71], v[16:31]\n"
    "v_mfma_f32_32x32x16_bf16 v[48:63], v[92:95], v[76:79], v[48:63]\n"
    "s_waitcnt lgkmcnt(0)\n"
    "s_barrier\n"
    "v_mfma_f32_32x32x16_bf16 v[0:15], v[112:115], v[96:99], v[0:15]\n"
    "ds_read_b64_tr_b16 v[80:81], v142 offset:0\n"
    "ds_read_b64_tr_b16 v[82:83], v142 offset:1024\n"
    "v_mfma_f32_32x32x16_bf16 v[32:47], v[112:115], v[104:107], v[32:47]\n"
    "ds_read_b128 v[64:67], v132 offset:0\n"
    "ds_read_b128 v[72:75], v132 offset:4096\n"
    "v_mfma_f32_32x32x16_bf16 v[16:31], v[120:123], v[96:99], v[16:31]\n"
    "ds_read_b64_tr_b16 v[88:89], v143 offset:0\n"
    "ds_read_b64_tr_b16 v[90:91], v143 offset:1024\n"
    "v_mfma_f32_32x32x16_bf16 v[48:63], v[120:123], v[104:107], v[48:63]\n"
    "ds_read_b64_tr_b16 v[84:85], v142 offset:4096\n"
    "ds_read_b64_tr_b16 v[86:87], v142 offset:5120\n"
    "v_mfma_f32_32x32x16_bf16 v[0:15], v[116:119], v[100:103], v[0:15]\n"
    "ds_read_b128 v[68:71], v133 offset:0\n"
    "ds_read_b128 v[76:79], v133 offset:4096\n"
    "v_mfma_f32_32x32x16_bf16 v[32:47], v[116:119], v[108:111], v[32:47]\n"
    "ds_read_b64_tr_b16 v[92:93], v143 offset:4096\n"
    "ds_read_b64_tr_b16 v[94:95], v143 offset:5120\n"
    "v_mfma_f32_32x32x16_bf16 v[16:31], v[124:127], v[100:103], v[16:31]\n"
    "v_mfma_f32_32x32x16_bf16 v[48:63], v[124:127], v[108:111], v[48:63]\n"
    "s_nop 15\n"

            P3_OPS : P3_INS : "memory");
        { int n = 2;
          asm volatile(
    "s_nop 1\n"
    "9:\n"
    "s_waitcnt lgkmcnt(9)\n"
    "v_mfma_f32_32x32x16_bf16 v[0:15], v[80:83], v[64:67], v[0:15]\n"
    "ds_read_b64_tr_b16 v[112:113], v142 offset:8192\n"
    "ds_read_b64_tr_b16 v[114:115], v142 offset:9216\n"
    "s_waitcnt lgkmcnt(10)\n"
    "v_mfma_f32_32x32x16_bf16 v[32:47], v[80:83], v[72:75], v[32:47]\n"
    "ds_read_b128 v[96:99], v134 offset:0\n"
    "ds_read_b128 v[104:107], v134 offset:4096\n"
    "s_waitcnt lgkmcnt(10)\n"
    "v_mfma_f32_32x32x16_bf16 v[16:31], v[88:91], v[64:67], v[16:31]\n"
    "ds_read_b64_tr_b16 v[120:121], v143 offset:8192\n"
    "ds_read_b64_tr_b16 v[122:123], v143 offset:9216\n"
    "v_mfma_f32_32x32x16_bf16 v[48:63], v[88:91], v[72:75], v[48:63]\n"
    "ds_read_b64_tr_b16 v[116:117], v142 offset:12288\n"
    "ds_read_b64_tr_b16 v[118:119], v142 offset:13312\n"
    "s_waitcnt lgkmcnt(11)\n"
    "v_mfma_f32_32x32x16_bf16 v[0:15], v[84:87], v[68:71], v[0:15]\n"
    "ds_read_b128 v[100:103], v135 offset:0\n"
    "ds_read_b128 v[108:111], v135 offset:4096\n"
    "s_waitcnt lgkmcnt(12)\n"
    "v_mfma_f32_32x32x16_bf16 v[32:47], v[84:87], v[76:79], v[32:47]\n"
    "ds_read_b64_tr_b16 v[124:125], v143 offset:12288\n"
    "ds_read_b64_tr_b16 v[126:127], v143 offset:13312\n"
    "s_waitcnt lgkmcnt(12)\n"
    "v_mfma_f32_32x32x16_bf16 v[16:31], v[92:95], v[68:71], v[16:31]\n"
    "v_mfma_f32_32x32x16_bf16 v[48:63], v[92:95], v[76:79], v[48:63]\n"
    "s_waitcnt lgkmcnt(0)\n"
    "s_barrier\n"
    "v_mfma_f32_32x32x16_bf16 v[0:15], v[112:115], v[96:99], v[0:15]\n"
    "ds_read_b64_tr_b16 v[80:81], v142 offset:32768\n"
    "ds_read_b64_tr_b16 v[82:83], v142 offset:33792\n"
    "v_mfma_f32_32x32x16_bf16 v[32:47], v[112:115], v[104:107], v[32:47]\n"
    "ds_read_b128 v[64:67], v132 offset:32768\n"
    "ds_read_b128 v[72:75], v132 offset:36864\n"
    "v_mfma_f32_32x32x16_bf16 v[16:31], v[120:123], v[96:99], v[16:31]\n"
    "ds_read_b64_tr_b16 v[88:89], v143 offset:32768\n"
    "ds_read_b64_tr_b16 v[90:91], v143 offset:33792\n"
    "v_mfma_f32_32x32x16_bf16 v[48:63], v[120:123], v[104:107], v[48:63]\n"
    "ds_read_b64_tr_b16 v[84:85], v142 offset:36864\n"
    "ds_read_b64_tr_b16 v[86:87], v142 offset:37888\n"
    "v_mfma_f32_32x32x16_bf16 v[0:15], v[116:119], v[100:103], v[0:15]\n"
    "ds_read_b128 v[68:71], v133 offset:32768\n"
    "ds_read_b128 v[76:79], v133 offset:36864\n"
    "v_mfma_f32_32x32x16_bf16 v[32:47], v[116:119], v[108:111], v[32:47]\n"
    "ds_read_b64_tr_b16 v[92:93], v143 offset:36864\n"
    "ds_read_b64_tr_b16 v[94:95], v143 offset:37888\n"
    "v_mfma_f32_32x32x16_bf16 v[16:31], v[124:127], v[100:103], v[16:31]\n"
    "v_mfma_f32_32x32x16_bf16 v[48:63], v[124:127], v[108:111], v[48:63]\n"
    "s_waitcnt lgkmcnt(9)\n"
    "v_mfma_f32_32x32x16_bf16 v[0:15], v[80:83], v[64:67], v[0:15]\n"
    "ds_read_b64_tr_b16 v[112:113], v142 offset:40960\n"
    "ds_read_b64_tr_b16 v[114:115], v142 offset:41984\n"
    "s_waitcnt lgkmcnt(10)\n"
    "v_mfma_f32_32x32x16_bf16 v[32:47], v[80:83], v[72:75], v[32:47]\n"
    "ds_read_b128 v[96:99], v134 offset:32768\n"
    "ds_read_b128 v[104:107], v134 offset:36864\n"
    "s_waitcnt lgkmcnt(10)\n"
    "v_mfma_f32_32x32x16_bf16 v[16:31], v[88:91], v[64:67], v[16:31]\n"
    "ds_read_b64_tr_b16 v[120:121], v143 offset:40960\n"
    "ds_read_b64_tr_b16 v[122:123], v143 offset:41984\n"
    "v_mfma_f32_32x32x16_bf16 v[48:63], v[88:91], v[72:75], v[48:63]\n"
    "ds_read_b64_tr_b16 v[116:117], v142 offset:45056\n"
    "ds_read_b64_tr_b16 v[118:119], v142 offset:46080\n"
    "s_waitcnt lgkmcnt(11)\n"
    "v_mfma_f32_32x32x16_bf16 v[0:15], v[84:87], v[68:71], v[0:15]\n"
    "ds_read_b128 v[100:103], v135 offset:32768\n"
    "ds_read_b128 v[108:111], v135 offset:36864\n"
    "s_waitcnt lgkmcnt(12)\n"
    "v_mfma_f32_32x32x16_bf16 v[32:47], v[84:87], v[76:79], v[32:47]\n"
    "ds_read_b64_tr_b16 v[124:125], v143 offset:45056\n"
    "ds_read_b64_tr_b16 v[126:127], v143 offset:46080\n"
    "s_waitcnt lgkmcnt(12)\n"
    "v_mfma_f32_32x32x16_bf16 v[16:31], v[92:95], v[68:71], v[16:31]\n"
    "v_mfma_f32_32x32x16_bf16 v[48:63], v[92:95], v[76:79], v[48:63]\n"
    "s_waitcnt lgkmcnt(0)\n"
    "s_barrier\n"
    "v_mfma_f32_32x32x16_bf16 v[0:15], v[112:115], v[96:99], v[0:15]\n"
    "ds_read_b64_tr_b16 v[80:81], v144 offset:0\n"
    "ds_read_b64_tr_b16 v[82:83], v144 offset:1024\n"
    "v_mfma_f32_32x32x16_bf16 v[32:47], v[112:115], v[104:107], v[32:47]\n"
    "ds_read_b128 v[64:67], v136 offset:0\n"
    "ds_read_b128 v[72:75], v136 offset:4096\n"
    "v_mfma_f32_32x32x16_bf16 v[16:31], v[120:123], v[96:99], v[16:31]\n"
    "ds_read_b64_tr_b16 v[88:89], v145 offset:0\n"
    "ds_read_b64_tr_b16 v[90:91], v145 offset:1024\n"
    "v_mfma_f32_32x32x16_bf16 v[48:63], v[120:123], v[104:107], v[48:63]\n"
    "ds_read_b64_tr_b16 v[84:85], v144 offset:4096\n"
    "ds_read_b64_tr_b16 v[86:87], v144 offset:5120\n"
    "v_mfma_f32_32x32x16_bf16 v[0:15], v[116:119], v[100:103], v[0:15]\n"
    "ds_read_b128 v[68:71], v137 offset:0\n"
    "ds_read_b128 v[76:79], v137 offset:4096\n"
    "v_mfma_f32_32x32x16_bf16 v[32:47], v[116:119], v[108:111], v[32:47]\n"
    "ds_read_b64_tr_b16 v[92:93], v145 offset:4096\n"
    "ds_read_b64_tr_b16 v[94:95], v145 offset:5120\n"
    "v_mfma_f32_32x32x16_bf16 v[16:31], v[124:127], v[100:103], v[16:31]\n"
    "v_mfma_f32_32x32x16_bf16 v[48:63], v[124:127], v[108:111], v[48:63]\n"
    "s_waitcnt lgkmcnt(9)\n"
    "v_mfma_f32_32x32x16_bf16 v[0:15], v[80:83], v[64:67], v[0:15]\n"
    "ds_read_b64_tr_b16 v[112:113], v144 offset:8192\n"
    "ds_read_b64_tr_b16 v[114:115], v144 offset:9216\n"
    "s_waitcnt lgkmcnt(10)\n"
    "v_mfma_f32_32x32x16_bf16 v[32:47], v[80:83], v[72:75], v[32:47]\n"
    "ds_read_b128 v[96:99], v138 offset:0\n"
    "ds_read_b128 v[104:107], v138 offset:4096\n"
    "s_waitcnt lgkmcnt(10)\n"
    "v_mfma_f32_32x32x16_bf16 v[16:31], v[88:91], v[64:67], v[16:31]\n"
    "ds_read_b64_tr_b16 v[120:121], v145 offset:8192\n"
    "ds_read_b64_tr_b16 v[122:123], v145 offset:9216\n"
    "v_mfma_f32_32x32x16_bf16 v[48:63], v[88:91], v[72:75], v[48:63]\n"
    "ds_read_b64_tr_b16 v[116:117], v144 offset:12288\n"
    "ds_read_b64_tr_b16 v[118:119], v144 offset:13312\n"
    "s_waitcnt lgkmcnt(11)\n"
    "v_mfma_f32_32x32x16_bf16 v[0:15], v[84:87], v[68:71], v[0:15]\n"
    "ds_read_b128 v[100:103], v139 offset:0\n"
    "ds_read_b128 v[108:111], v139 offset:4096\n"
    "s_waitcnt lgkmcnt(12)\n"
    "v_mfma_f32_32x32x16_bf16 v[32:47], v[84:87], v[76:79], v[32:47]\n"
    "ds_read_b64_tr_b16 v[124:125], v145 offset:12288\n"
    "ds_read_b64_tr_b16 v[126:127], v145 offset:13312\n"
    "s_waitcnt lgkmcnt(12)\n"
    "v_mfma_f32_32x32x16_bf16 v[16:31], v[92:95], v[68:71], v[16:31]\n"
    "v_mfma_f32_32x32x16_bf16 v[48:63], v[92:95], v[76:79], v[48:63]\n"
    "s_waitcnt lgkmcnt(0)\n"
    "s_barrier\n"
    "v_mfma_f32_32x32x16_bf16 v[0:15], v[112:115], v[96:99], v[0:15]\n"
    "ds_read_b64_tr_b16 v[80:81], v140 offset:0\n"
    "ds_read_b64_tr_b16 v[82:83], v140 offset:1024\n"
    "v_mfma_f32_32x32x16_bf16 v[32:47], v[112:115], v[104:107], v[32:47]\n"
    "ds_read_b128 v[64:67], v128 offset:0\n"
    "ds_read_b128 v[72:75], v128 offset:4096\n"
    "v_mfma_f32_32x32x16_bf16 v[16:31], v[120:123], v[96:99], v[16:31]\n"
    "ds_read_b64_tr_b16 v[88:89], v141 offset:0\n"
    "ds_read_b64_tr_b16 v[90:91], v141 offset:1024\n"
    "v_mfma_f32_32x32x16_bf16 v[48:63], v[120:123], v[104:107], v[48:63]\n"
    "ds_read_b64_tr_b16 v[84:85], v140 offset:4096\n"
    "ds_read_b64_tr_b16 v[86:87], v140 offset:5120\n"
    "v_mfma_f32_32x32x16_bf16 v[0:15], v[116:119], v[100:103], v[0:15]\n"
    "ds_read_b128 v[68:71], v129 offset:0\n"
    "ds_read_b128 v[76:79], v129 offset:4096\n"
    "v_mfma_f32_32x32x16_bf16 v[32:47], v[116:119], v[108:111], v[32:47]\n"
    "ds_read_b64_tr_b16 v[92:93], v141 offset:4096\n"
    "ds_read_b64_tr_b16 v[94:95], v141 offset:5120\n"
    "v_mfma_f32_32x32x16_bf16 v[16:31], v[124:127], v[100:103], v[16:31]\n"
    "v_mfma_f32_32x32x16_bf16 v[48:63], v[124:127], v[108:111], v[48:63]\n"
    "s_waitcnt lgkmcnt(9)\n"
    "v_mfma_f32_32x32x16_bf16 v[0:15], v[80:83], v[64:67], v[0:15]\n"
    "ds_read_b64_tr_b16 v[112:113], v140 offset:8192\n"
    "ds_read_b64_tr_b16 v[114:115], v140 offset:9216\n"
    "s_waitcnt lgkmcnt(10)\n"
    "v_mfma_f32_32x32x16_bf16 v[32:47], v[80:83], v[72:75], v[32:47]\n"
    "ds_read_b128 v[96:99], v130 offset:0\n"
    "ds_read_b128 v[104:107], v130 offset:4096\n"
    "s_waitcnt lgkmcnt(10)\n"
    "v_mfma_f32_32x32x16_bf16 v[16:31], v[88:91], v[64:67], v[16:31]\n"
    "ds_read_b64_tr_b16 v[120:121], v141 offset:8192\n"
    "ds_read_b64_tr_b16 v[122:123], v141 offset:9216\n"
    "v_mfma_f32_32x32x16_bf16 v[48:63], v[88:91], v[72:75], v[48:63]\n"
    "ds_read_b64_tr_b16 v[116:117], v140 offset:12288\n"
    "ds_read_b64_tr_b16 v[118:119], v140 offset:13312\n"
    "s_waitcnt lgkmcnt(11)\n"
    "v_mfma_f32_32x32x16_bf16 v[0:15], v[84:87], v[68:71], v[0:15]\n"
    "ds_read_b128 v[100:103], v131 offset:0\n"
    "ds_read_b128 v[108:111], v131 offset:4096\n"
    "s_waitcnt lgkmcnt(12)\n"
    "v_mfma_f32_32x32x16_bf16 v[32:47], v[84:87], v[76:79], v[32:47]\n"
    "ds_read_b64_tr_b16 v[124:125], v141 offset:12288\n"
    "ds_read_b64_tr_b16 v[126:127], v141 offset:13312\n"
    "s_waitcnt lgkmcnt(12)\n"
    "v_mfma_f32_32x32x16_bf16 v[16:31], v[92:95], v[68:71], v[16:31]\n"
    "v_mfma_f32_32x32x16_bf16 v[48:63], v[92:95], v[76:79], v[48:63]\n"
    "s_waitcnt lgkmcnt(0)\n"
    "s_barrier\n"
    "v_mfma_f32_32x32x16_bf16 v[0:15], v[112:115], v[96:99], v[0:15]\n"
    "ds_read_b64_tr_b16 v[80:81], v140 offset:32768\n"
    "ds_read_b64_tr_b16 v[82:83], v140 offset:33792\n"
    "v_mfma_f32_32x32x16_bf16 v[32:47], v[112:115], v[104:107], v[32:47]\n"
    "ds_read_b128 v[64:67], v128 offset:32768\n"
    "ds_read_b128 v[72:75], v128 offset:36864\n"
    "v_mfma_f32_32x32x16_bf16 v[16:31], v[120:123], v[96:99], v[16:31]\n"
    "ds_read_b64_tr_b16 v[88:89], v141 offset:32768\n"
    "ds_read_b64_tr_b16 v[90:91], v141 offset:33792\n"
    "v_mfma_f32_32x32x16_bf16 v[48:63], v[120:123], v[104:107], v[48:63]\n"
    "ds_read_b64_tr_b16 v[84:85], v140 offset:36864\n"
    "ds_read_b64_tr_b16 v[86:87], v140 offset:37888\n"
    "v_mfma_f32_32x32x16_bf16 v[0:15], v[116:119], v[100:103], v[0:15]\n"
    "ds_read_b128 v[68:71], v129 offset:32768\n"
    "ds_read_b128 v[76:79], v129 offset:36864\n"
    "v_mfma_f32_32x32x16_bf16 v[32:47], v[116:119], v[108:111], v[32:47]\n"
    "ds_read_b64_tr_b16 v[92:93], v141 offset:36864\n"
    "ds_read_b64_tr_b16 v[94:95], v141 offset:37888\n"
    "v_mfma_f32_32x32x16_bf16 v[16:31], v[124:127], v[100:103], v[16:31]\n"
    "v_mfma_f32_32x32x16_bf16 v[48:63], v[124:127], v[108:111], v[48:63]\n"
    "s_waitcnt lgkmcnt(9)\n"
    "v_mfma_f32_32x32x16_bf16 v[0:15], v[80:83], v[64:67], v[0:15]\n"
    "ds_read_b64_tr_b16 v[112:113], v140 offset:40960\n"
    "ds_read_b64_tr_b16 v[114:115], v140 offset:41984\n"
    "s_waitcnt lgkmcnt(10)\n"
    "v_mfma_f32_32x32x16_bf16 v[32:47], v[80:83], v[72:75], v[32:47]\n"
    "ds_read_b128 v[96:99], v130 offset:32768\n"
    "ds_read_b128 v[104:107], v130 offset:36864\n"
    "s_waitcnt lgkmcnt(10)\n"
    "v_mfma_f32_32x32x16_bf16 v[16:31], v[88:91], v[64:67], v[16:31]\n"
    "ds_read_b64_tr_b16 v[120:121], v141 offset:40960\n"
    "ds_read_b64_tr_b16 v[122:123], v141 offset:41984\n"
    "v_mfma_f32_32x32x16_bf16 v[48:63], v[88:91], v[72:75], v[48:63]\n"
    "ds_read_b64_tr_b16 v[116:117], v140 offset:45056\n"
    "ds_read_b64_tr_b16 v[118:119], v140 offset:46080\n"
    "s_waitcnt lgkmcnt(11)\n"
    "v_mfma_f32_32x32x16_bf16 v[0:15], v[84:87], v[68:71], v[0:15]\n"
    "ds_read_b128 v[100:103], v131 offset:32768\n"
    "ds_read_b128 v[108:111], v131 offset:36864\n"
    "s_waitcnt lgkmcnt(12)\n"
    "v_mfma_f32_32x32x16_bf16 v[32:47], v[84:87], v[76:79], v[32:47]\n"
    "ds_read_b64_tr_b16 v[124:125], v141 offset:45056\n"
    "ds_read_b64_tr_b16 v[126:127], v141 offset:46080\n"
    "s_waitcnt lgkmcnt(12)\n"
    "v_mfma_f32_32x32x16_bf16 v[16:31], v[92:95], v[68:71], v[16:31]\n"
    "v_mfma_f32_32x32x16_bf16 v[48:63], v[92:95], v[76:79], v[48:63]\n"
    "s_waitcnt lgkmcnt(0)\n"
    "s_barrier\n"
    "v_mfma_f32_32x32x16_bf16 v[0:15], v[112:115], v[96:99], v[0:15]\n"
    "ds_read_b64_tr_b16 v[80:81], v142 offset:0\n"
    "ds_read_b64_tr_b16 v[82:83], v142 offset:1024\n"
    "v_mfma_f32_32x32x16_bf16 v[32:47], v[112:115], v[104:107], v[32:47]\n"
    "ds_read_b128 v[64:67], v132 offset:0\n"
    "ds_read_b128 v[72:75], v132 offset:4096\n"
    "v_mfma_f32_32x32x16_bf16 v[16:31], v[120:123], v[96:99], v[16:31]\n"
    "ds_read_b64_tr_b16 v[88:89], v143 offset:0\n"
    "ds_read_b64_tr_b16 v[90:91], v143 offset:1024\n"
    "v_mfma_f32_32x32x16_bf16 v[48:63], v[120:123], v[104:107], v[48:63]\n"
    "ds_read_b64_tr_b16 v[84:85], v142 offset:4096\n"
    "ds_read_b64_tr_b16 v[86:87], v142 offset:5120\n"
    "v_mfma_f32_32x32x16_bf16 v[0:15], v[116:119], v[100:103], v[0:15]\n"
    "ds_read_b128 v[68:71], v133 offset:0\n"
    "ds_read_b128 v[76:79], v133 offset:4096\n"
    "v_mfma_f32_32x32x16_bf16 v[32:47], v[116:119], v[108:111], v[32:47]\n"
    "ds_read_b64_tr_b16 v[92:93], v143 offset:4096\n"
    "ds_read_b64_tr_b16 v[94:95], v143 offset:5120\n"
    "v_mfma_f32_32x32x16_bf16 v[16:31], v[124:127], v[100:103], v[16:31]\n"
    "v_mfma_f32_32x32x16_bf16 v[48:63], v[124:127], v[108:111], v[48:63]\n"
    "s_sub_u32 %[n], %[n], 1\n"
    "s_cmp_lg_u32 %[n], 0\n"
    "s_cbranch_scc1 9b\n"
    "s_nop 15\n"

            P3_OPS, [n] "+s"(n) : P3_INS : "memory", "scc"); }
        asm volatile(
    "s_nop 1\n"
    "s_waitcnt lgkmcnt(9)\n"
    "v_mfma_f32_32x32x16_bf16 v[0:15], v[80:83], v[64:67], v[0:15]\n"
    "ds_read_b64_tr_b16 v[112:113], v142 offset:8192\n"
    "ds_read_b64_tr_b16 v[114:115], v142 offset:9216\n"
    "s_waitcnt lgkmcnt(10)\n"
    "v_mfma_f32_32x32x16_bf16 v[32:47], v[80:83], v[72:75], v[32:47]\n"
    "ds_read_b128 v[96:99], v134 offset:0\n"
    "ds_read_b128 v[104:107], v134 offset:4096\n"
    "s_waitcnt lgkmcnt(10)\n"
    "v_mfma_f32_32x32x16_bf16 v[16:31], v[88:91], v[64:67], v[16:31]\n"
    "ds_read_b64_tr_b16 v[120:121], v143 offset:8192\n"
    "ds_read_b64_tr_b16 v[122:123], v143 offset:9216\n"
    "v_mfma_f32_32x32x16_bf16 v[48:63], v[88:91], v[72:75], v[48:63]\n"
    "ds_read_b64_tr_b16 v[116:117], v142 offset:12288\n"
    "ds_read_b64_tr_b16 v[118:119], v142 offset:13312\n"
    "s_waitcnt lgkmcnt(11)\n"
    "v_mfma_f32_32x32x16_bf16 v[0:15], v[84:87], v[68:71], v[0:15]\n"
    "ds_read_b128 v[100:103], v135 offset:0\n"
    "ds_read_b128 v[108:111], v135 offset:4096\n"
    "s_waitcnt lgkmcnt(12)\n"
    "v_mfma_f32_32x32x16_bf16 v[32:47], v[84:87], v[76:79], v[32:47]\n"
    "ds_read_b64_tr_b16 v[124:125], v143 offset:12288\n"
    "ds_read_b64_tr_b16 v[126:127], v143 offset:13312\n"
    "s_waitcnt lgkmcnt(12)\n"
    "v_mfma_f32_32x32x16_bf16 v[16:31], v[92:95], v[68:71], v[16:31]\n"
    "v_mfma_f32_32x32x16_bf16 v[48:63], v[92:95], v[76:79], v[48:63]\n"
    "s_waitcnt lgkmcnt(0)\n"
    "s_barrier\n"
    "v_mfma_f32_32x32x16_bf16 v[0:15], v[112:115], v[96:99], v[0:15]\n"
    "ds_read_b64_tr_b16 v[80:81], v142 offset:32768\n"
    "ds_read_b64_tr_b16 v[82:83], v142 offset:33792\n"
    "v_mfma_f32_32x32x16_bf16 v[32:47], v[112:115], v[104:107], v[32:47]\n"
    "ds_read_b128 v[64:67], v132 offset:32768\n"
    "ds_read_b128 v[72:75], v132 offset:36864\n"
    "v_mfma_f32_32x32x16_bf16 v[16:31], v[120:123], v[96:99], v[16:31]\n"
    "ds_read_b64_tr_b16 v[88:89], v143 offset:32768\n"
    "ds_read_b64_tr_b16 v[90:91], v143 offset:33792\n"
    "v_mfma_f32_32x32x16_bf16 v[48:63], v[120:123], v[104:107], v[48:63]\n"
    "ds_read_b64_tr_b16 v[84:85], v142 offset:36864\n"
    "ds_read_b64_tr_b16 v[86:87], v142 offset:37888\n"
    "v_mfma_f32_32x32x16_bf16 v[0:15], v[116:119], v[100:103], v[0:15]\n"
    "ds_read_b128 v[68:71], v133 offset:32768\n"
    "ds_read_b128 v[76:79], v133 offset:36864\n"
    "v_mfma_f32_32x32x16_bf16 v[32:47], v[116:119], v[108:111], v[32:47]\n"
    "ds_read_b64_tr_b16 v[92:93], v143 offset:36864\n"
    "ds_read_b64_tr_b16 v[94:95], v143 offset:37888\n"
    "v_mfma_f32_32x32x16_bf16 v[16:31], v[124:127], v[100:103], v[16:31]\n"
    "v_mfma_f32_32x32x16_bf16 v[48:63], v[124:127], v[108:111], v[48:63]\n"
    "s_waitcnt lgkmcnt(9)\n"
    "v_mfma_f32_32x32x16_bf16 v[0:15], v[80:83], v[64:67], v[0:15]\n"
    "ds_read_b64_tr_b16 v[112:113], v142 offset:40960\n"
    "ds_read_b64_tr_b16 v[114:115], v142 offset:41984\n"
    "s_waitcnt lgkmcnt(10)\n"
    "v_mfma_f32_32x32x16_bf16 v[32:47], v[80:83], v[72:75], v[32:47]\n"
    "ds_read_b128 v[96:99], v134 offset:32768\n"
    "ds_read_b128 v[104:107], v134 offset:36864\n"
    "s_waitcnt lgkmcnt(10)\n"
    "v_mfma_f32_32x32x16_bf16 v[16:31], v[88:91], v[64:67], v[16:31]\n"
    "ds_read_b64_tr_b16 v[120:121], v143 offset:40960\n"
    "ds_read_b64_tr_b16 v[122:123], v143 offset:41984\n"
    "v_mfma_f32_32x32x16_bf16 v[48:63], v[88:91], v[72:75], v[48:63]\n"
    "ds_read_b64_tr_b16 v[116:117], v142 offset:45056\n"
    "ds_read_b64_tr_b16 v[118:119], v142 offset:46080\n"
    "s_waitcnt lgkmcnt(11)\n"
    "v_mfma_f32_32x32x16_bf16 v[0:15], v[84:87], v[68:71], v[0:15]\n"
    "ds_read_b128 v[100:103], v135 offset:32768\n"
    "ds_read_b128 v[108:111], v135 offset:36864\n"
    "s_waitcnt lgkmcnt(12)\n"
    "v_mfma_f32_32x32x16_bf16 v[32:47], v[84:87], v[76:79], v[32:47]\n"
    "ds_read_b64_tr_b16 v[124:125], v143 offset:45056\n"
    "ds_read_b64_tr_b16 v[126:127], v143 offset:46080\n"
    "s_waitcnt lgkmcnt(12)\n"
    "v_mfma_f32_32x32x16_bf16 v[16:31], v[92:95], v[68:71], v[16:31]\n"
    "v_mfma_f32_32x32x16_bf16 v[48:63], v[92:95], v[76:79], v[48:63]\n"
    "s_waitcnt lgkmcnt(0)\n"
    "s_barrier\n"
    "v_mfma_f32_32x32x16_bf16 v[0:15], v[112:115], v[96:99], v[0:15]\n"
    "ds_read_b64_tr_b16 v[80:81], v144 offset:0\n"
    "ds_read_b64_tr_b16 v[82:83], v144 offset:1024\n"
    "v_mfma_f32_32x32x16_bf16 v[32:47], v[112:115], v[104:107], v[32:47]\n"
    "ds_read_b128 v[64:67], v136 offset:0\n"
    "ds_read_b128 v[72:75], v136 offset:4096\n"
    "v_mfma_f32_32x32x16_bf16 v[16:31], v[120:123], v[96:99], v[16:31]\n"
    "ds_read_b64_tr_b16 v[88:89], v145 offset:0\n"
    "ds_read_b64_tr_b16 v[90:91], v145 offset:1024\n"
    "v_mfma_f32_32x32x16_bf16 v[48:63], v[120:123], v[104:107], v[48:63]\n"
    "ds_read_b64_tr_b16 v[84:85], v144 offset:4096\n"
    "ds_read_b64_tr_b16 v[86:87], v144 offset:5120\n"
    "v_mfma_f32_32x32x16_bf16 v[0:15], v[116:119], v[100:103], v[0:15]\n"
    "ds_read_b128 v[68:71], v137 offset:0\n"
    "ds_read_b128 v[76:79], v137 offset:4096\n"
    "v_mfma_f32_32x32x16_bf16 v[32:47], v[116:119], v[108:111], v[32:47]\n"
    "ds_read_b64_tr_b16 v[92:93], v145 offset:4096\n"
    "ds_read_b64_tr_b16 v[94:95], v145 offset:5120\n"
    "v_mfma_f32_32x32x16_bf16 v[16:31], v[124:127], v[100:103], v[16:31]\n"
    "v_mfma_f32_32x32x16_bf16 v[48:63], v[124:127], v[108:111], v[48:63]\n"
    "s_waitcnt lgkmcnt(9)\n"
    "v_mfma_f32_32x32x16_bf16 v[0:15], v[80:83], v[64:67], v[0:15]\n"
    "ds_read_b64_tr_b16 v[112:113], v144 offset:8192\n"
    "ds_read_b64_tr_b16 v[114:115], v144 offset:9216\n"
    "s_waitcnt lgkmcnt(10)\n"
    "v_mfma_f32_32x32x16_bf16 v[32:47], v[80:83], v[72:75], v[32:47]\n"
    "ds_read_b128 v[96:99], v138 offset:0\n"
    "ds_read_b128 v[104:107], v138 offset:4096\n"
    "s_waitcnt lgkmcnt(10)\n"
    "v_mfma_f32_32x32x16_bf16 v[16:31], v[88:91], v[64:67], v[16:31]\n"
    "ds_read_b64_tr_b16 v[120:121], v145 offset:8192\n"
    "ds_read_b64_tr_b16 v[122:123], v145 offset:9216\n"
    "v_mfma_f32_32x32x16_bf16 v[48:63], v[88:91], v[72:75], v[48:63]\n"
    "ds_read_b64_tr_b16 v[116:117], v144 offset:12288\n"
    "ds_read_b64_tr_b16 v[118:119], v144 offset:13312\n"
    "s_waitcnt lgkmcnt(11)\n"
    "v_mfma_f32_32x32x16_bf16 v[0:15], v[84:87], v[68:71], v[0:15]\n"
    "ds_read_b128 v[100:103], v139 offset:0\n"
    "ds_read_b128 v[108:111], v139 offset:4096\n"
    "s_waitcnt lgkmcnt(12)\n"
    "v_mfma_f32_32x32x16_bf16 v[32:47], v[84:87], v[76:79], v[32:47]\n"
    "ds_read_b64_tr_b16 v[124:125], v145 offset:12288\n"
    "ds_read_b64_tr_b16 v[126:127], v145 offset:13312\n"
    "s_waitcnt lgkmcnt(12)\n"
    "v_mfma_f32_32x32x16_bf16 v[16:31], v[92:95], v[68:71], v[16:31]\n"
    "v_mfma_f32_32x32x16_bf16 v[48:63], v[92:95], v[76:79], v[48:63]\n"
    "s_waitcnt lgkmcnt(0)\n"
    "s_barrier\n"
    "v_mfma_f32_32x32x16_bf16 v[0:15], v[112:115], v[96:99], v[0:15]\n"
    "ds_read_b64_tr_b16 v[80:81], v140 offset:0\n"
    "ds_read_b64_tr_b16 v[82:83], v140 offset:1024\n"
    "v_mfma_f32_32x32x16_bf16 v[32:47], v[112:115], v[104:107], v[32:47]\n"
    "ds_read_b128 v[64:67], v128 offset:0\n"
    "ds_read_b128 v[72:75], v128 offset:4096\n"
    "v_mfma_f32_32x32x16_bf16 v[16:31], v[120:123], v[96:99], v[16:31]\n"
    "ds_read_b64_tr_b16 v[88:89], v141 offset:0\n"
    "ds_read_b64_tr_b16 v[90:91], v141 offset:1024\n"
    "v_mfma_f32_32x32x16_bf16 v[48:63], v[120:123], v[104:107], v[48:63]\n"
    "ds_read_b64_tr_b16 v[84:85], v140 offset:4096\n"
    "ds_read_b64_tr_b16 v[86:87], v140 offset:5120\n"
    "v_mfma_f32_32x32x16_bf16 v[0:15], v[116:119], v[100:103], v[0:15]\n"
    "ds_read_b128 v[68:71], v129 offset:0\n"
    "ds_read_b128 v[76:79], v129 offset:4096\n"
    "v_mfma_f32_32x32x16_bf16 v[32:47], v[116:119], v[108:111], v[32:47]\n"
    "ds_read_b64_tr_b16 v[92:93], v141 offset:4096\n"
    "ds_read_b64_tr_b16 v[94:95], v141 offset:5120\n"
    "v_mfma_f32_32x32x16_bf16 v[16:31], v[124:127], v[100:103], v[16:31]\n"
    "v_mfma_f32_32x32x16_bf16 v[48:63], v[124:127], v[108:111], v[48:63]\n"
    "s_waitcnt lgkmcnt(9)\n"
    "v_mfma_f32_32x32x16_bf16 v[0:15], v[80:83], v[64:67], v[0:15]\n"
    "ds_read_b64_tr_b16 v[112:113], v140 offset:8192\n"
    "ds_read_b64_tr_b16 v[114:115], v140 offset:9216\n"
    "s_waitcnt lgkmcnt(10)\n"
    "v_mfma_f32_32x32x16_bf16 v[32:47], v[80:83], v[72:75], v[32:47]\n"
    "ds_read_b128 v[96:99], v130 offset:0\n"
    "ds_read_b128 v[104:107], v130 offset:4096\n"
    "s_waitcnt lgkmcnt(10)\n"
    "v_mfma_f32_32x32x16_bf16 v[16:31], v[88:91], v[64:67], v[16:31]\n"
    "ds_read_b64_tr_b16 v[120:121], v141 offset:8192\n"
    "ds_read_b64_tr_b16 v[122:123], v141 offset:9216\n"
    "v_mfma_f32_32x32x16_bf16 v[48:63], v[88:91], v[72:75], v[48:63]\n"
    "ds_read_b64_tr_b16 v[116:117], v140 offset:12288\n"
    "ds_read_b64_tr_b16 v[118:119], v140 offset:13312\n"
    "s_waitcnt lgkmcnt(11)\n"
    "v_mfma_f32_32x32x16_bf16 v[0:15], v[84:87], v[68:71], v[0:15]\n"
    "ds_read_b128 v[100:103], v131 offset:0\n"
    "ds_read_b128 v[108:111], v131 offset:4096\n"
    "s_waitcnt lgkmcnt(12)\n"
    "v_mfma_f32_32x32x16_bf16 v[32:47], v[84:87], v[76:79], v[32:47]\n"
    "ds_read_b64_tr_b16 v[124:125], v141 offset:12288\n"
    "ds_read_b64_tr_b16 v[126:127], v141 offset:13312\n"
    "s_waitcnt lgkmcnt(12)\n"
    "v_mfma_f32_32x32x16_bf16 v[16:31], v[92:95], v[68:71], v[16:31]\n"
    "v_mfma_f32_32x32x16_bf16 v[48:63], v[92:95], v[76:79], v[48:63]\n"
    "s_waitcnt lgkmcnt(9)\n"
    "v_mfma_f32_32x32x16_bf16 v[0:15], v[112:115], v[96:99], v[0:15]\n"
    "s_waitcnt lgkmcnt(8)\n"
    "v_mfma_f32_32x32x16_bf16 v[32:47], v[112:115], v[104:107], v[32:47]\n"
    "s_waitcnt lgkmcnt(6)\n"
    "v_mfma_f32_32x32x16_bf16 v[16:31], v[120:123], v[96:99], v[16:31]\n"
    "v_mfma_f32_32x32x16_bf16 v[48:63], v[120:123], v[104:107], v[48:63]\n"
    "s_waitcnt lgkmcnt(3)\n"
    "v_mfma_f32_32x32x16_bf16 v[0:15], v[116:119], v[100:103], v[0:15]\n"
    "s_waitcnt lgkmcnt(2)\n"
    "v_mfma_f32_32x32x16_bf16 v[32:47], v[116:119], v[108:111], v[32:47]\n"
    "s_waitcnt lgkmcnt(0)\n"
    "v_mfma_f32_32x32x16_bf16 v[16:31], v[124:127], v[100:103], v[16:31]\n"
    "v_mfma_f32_32x32x16_bf16 v[48:63], v[124:127], v[108:111], v[48:63]\n"
    "s_nop 15\n"

            P3_OPS : P3_INS : "memory");

#pragma unroll
        for (int i = 0; i < 4; ++i)
#pragma unroll
            for (int k = 0; k < 4; ++k) st16w(xch + (i * 4 + k) * 256, (f32x4){acc[i][4 * k], acc[i][4 * k + 1], acc[i][4 * k + 2], acc[i][4 * k + 3]});
        asm volatile("s_waitcnt vmcnt(0)\n\ts_barrier" ::: "memory");
        if (w == 0 && lane == 0) flag_set(a.flags + FLAG_X + tile * 16, tag);
    } else {
        { int n = 7;
          asm volatile(
    "s_nop 1\n"
    "9:\n"
    "s_waitcnt lgkmcnt(9)\n"
    "v_mfma_f32_32x32x16_bf16 v[0:15], v[80:83], v[64:67], v[0:15]\n"
    "ds_read_b64_tr_b16 v[112:113], v142 offset:8192\n"
    "ds_read_b64_tr_b16 v[114:115], v142 offset:9216\n"
    "s_waitcnt lgkmcnt(10)\n"
    "v_mfma_f32_32x32x16_bf16 v[32:47], v[80:83], v[72:75], v[32:47]\n"
    "ds_read_b128 v[96:99], v134 offset:0\n"
    "ds_read_b128 v[104:107], v134 offset:4096\n"
    "s_waitcnt lgkmcnt(10)\n"
    "v_mfma_f32_32x32x16_bf16 v[16:31], v[88:91], v[64:67], v[16:31]\n"
    "ds_read_b64_tr_b16 v[120:121], v143 offset:8192\n"
    "ds_read_b64_tr_b16 v[122:123], v143 offset:9216\n"
    "v_mfma_f32_32x32x16_bf16 v[48:63], v[88:91], v[72:75], v[48:63]\n"
    "ds_read_b64_tr_b16 v[116:117], v142 offset:12288\n"
    "ds_read_b64_tr_b16 v[118:119], v142 offset:13312\n"
    "s_waitcnt lgkmcnt(11)\n"
    "v_mfma_f32_32x32x16_bf16 v[0:15], v[84:87], v[68:71], v[0:15]\n"
    "ds_read_b128 v[100:103], v135 offset:0\n"
    "ds_read_b128 v[108:111], v135 offset:4096\n"
    "s_waitcnt lgkmcnt(12)\n"
    "v_mfma_f32_32x32x16_bf16 v[32:47], v[84:87], v[76:79], v[32:47]\n"
    "ds_read_b64_tr_b16 v[124:125], v143 offset:12288\n"
    "ds_read_b64_tr_b16 v[126:127], v143 offset:13312\n"
    "s_waitcnt lgkmcnt(12)\n"
    "v_mfma_f32_32x32x16_bf16 v[16:31], v[92:95], v[68:71], v[16:31]\n"
    "v_mfma_f32_32x32x16_bf16 v[48:63], v[92:95], v[76:79], v[48:63]\n"
    "s_waitcnt lgkmcnt(0)\n"
    "s_barrier\n"
    "v_mfma_f32_32x32x16_bf16 v[0:15], v[112:115], v[96:99], v[0:15]\n"
    "ds_read_b64_tr_b16 v[80:81], v142 offset:32768\n"
    "ds_read_b64_tr_b16 v[82:83], v142 offset:33792\n"
    "v_mfma_f32_32x32x16_bf16 v[32:47], v[112:115], v[104:107], v[32:47]\n"
    "ds_read_b128 v[64:67], v132 offset:32768\n"
    "ds_read_b128 v[72:75], v132 offset:36864\n"
    "v_mfma_f32_32x32x16_bf16 v[16:31], v[120:123], v[96:99], v[16:31]\n"
    "ds_read_b64_tr_b16 v[88:89], v143 offset:32768\n"
    "ds_read_b64_tr_b16 v[90:91], v143 offset:33792\n"
    "v_mfma_f32_32x32x16_bf16 v[48:63], v[120:123], v[104:107], v[48:63]\n"
    "ds_read_b64_tr_b16 v[84:85], v142 offset:36864\n"
    "ds_read_b64_tr_b16 v[86:87], v142 offset:37888\n"
    "v_mfma_f32_32x32x16_bf16 v[0:15], v[116:119], v[100:103], v[0:15]\n"
    "ds_read_b128 v[68:71], v133 offset:32768\n"
    "ds_read_b128 v[76:79], v133 offset:36864\n"
    "v_mfma_f32_32x32x16_bf16 v[32:47], v[116:119], v[108:111], v[32:47]\n"
    "ds_read_b64_tr_b16 v[92:93], v143 offset:36864\n"
    "ds_read_b64_tr_b16 v[94:95], v143 offset:37888\n"
    "v_mfma_f32_32x32x16_bf16 v[16:31], v[124:127], v[100:103], v[16:31]\n"
    "v_mfma_f32_32x32x16_bf16 v[48:63], v[124:127], v[108:111], v[48:63]\n"
    "s_waitcnt lgkmcnt(9)\n"
    "v_mfma_f32_32x32x16_bf16 v[0:15], v[80:83], v[64:67], v[0:15]\n"
    "ds_read_b64_tr_b16 v[112:113], v142 offset:40960\n"
    "ds_read_b64_tr_b16 v[114:115], v142 offset:41984\n"
    "s_waitcnt lgkmcnt(10)\n"
    "v_mfma_f32_32x32x16_bf16 v[32:47], v[80:83], v[72:75], v[32:47]\n"
    "ds_read_b128 v[96:99], v134 offset:32768\n"
    "ds_read_b128 v[104:107], v134 offset:36864\n"
    "s_waitcnt lgkmcnt(10)\n"
    "v_mfma_f32_32x32x16_bf16 v[16:31], v[88:91], v[64:67], v[16:31]\n"
    "ds_read_b64_tr_b16 v[120:121], v143 offset:40960\n"
    "ds_read_b64_tr_b16 v[122:123], v143 offset:41984\n"
    "v_mfma_f32_32x32x16_bf16 v[48:63], v[88:91], v[72:75], v[48:63]\n"
    "ds_read_b64_tr_b16 v[116:117], v142 offset:45056\n"
    "ds_read_b64_tr_b16 v[118:119], v142 offset:46080\n"
    "s_waitcnt lgkmcnt(11)\n"
    "v_mfma_f32_32x32x16_bf16 v[0:15], v[84:87], v[68:71], v[0:15]\n"
    "ds_read_b128 v[100:103], v135 offset:32768\n"
    "ds_read_b128 v[108:111], v135 offset:36864\n"
    "s_waitcnt lgkmcnt(12)\n"
    "v_mfma_f32_32x32x16_bf16 v[32:47], v[84:87], v[76:79], v[32:47]\n"
    "ds_read_b64_tr_b16 v[124:125], v143 offset:45056\n"
    "ds_read_b64_tr_b16 v[126:127], v143 offset:46080\n"
    "s_waitcnt lgkmcnt(12)\n"
    "v_mfma_f32_32x32x16_bf16 v[16:31], v[92:95], v[68:71], v[16:31]\n"
    "v_mfma_f32_32x32x16_bf16 v[48:63], v[92:95], v[76:79], v[48:63]\n"
    "s_waitcnt lgkmcnt(0)\n"
    "s_barrier\n"
    "v_mfma_f32_32x32x16_bf16 v[0:15], v[112:115], v[96:99], v[0:15]\n"
    "ds_read_b64_tr_b16 v[80:81], v144 offset:0\n"
    "ds_read_b64_tr_b16 v[82:83], v144 offset:1024\n"
    "v_mfma_f32_32x32x16_bf16 v[32:47], v[112:115], v[104:107], v[32:47]\n"
    "ds_read_b128 v[64:67], v136 offset:0\n"
    "ds_read_b128 v[72:75], v136 offset:4096\n"
    "v_mfma_f32_32x32x16_bf16 v[16:31], v[120:123], v[96:99], v[16:31]\n"
    "ds_read_b64_tr_b16 v[88:89], v145 offset:0\n"
    "ds_read_b64_tr_b16 v[90:91], v145 offset:1024\n"
    "v_mfma_f32_32x32x16_bf16 v[48:63], v[120:123], v[104:107], v[48:63]\n"
    "ds_read_b64_tr_b16 v[84:85], v144 offset:4096\n"
    "ds_read_b64_tr_b16 v[86:87], v144 offset:5120\n"
    "v_mfma_f32_32x32x16_bf16 v[0:15], v[116:119], v[100:103], v[0:15]\n"
    "ds_read_b128 v[68:71], v137 offset:0\n"
    "ds_read_b128 v[76:79], v137 offset:4096\n"
    "v_mfma_f32_32x32x16_bf16 v[32:47], v[116:119], v[108:111], v[32:47]\n"
    "ds_read_b64_tr_b16 v[92:93], v145 offset:4096\n"
    "ds_read_b64_tr_b16 v[94:95], v145 offset:5120\n"
    "v_mfma_f32_32x32x16_bf16 v[16:31], v[124:127], v[100:103], v[16:31]\n"
    "v_mfma_f32_32x32x16_bf16 v[48:63], v[124:127], v[108:111], v[48:63]\n"
    "s_waitcnt lgkmcnt(9)\n"
    "v_mfma_f32_32x32x16_bf16 v[0:15], v[80:83], v[64:67], v[0:15]\n"
    "ds_read_b64_tr_b16 v[112:113], v144 offset:8192\n"
    "ds_read_b64_tr_b16 v[114:115], v144 offset:9216\n"
    "s_waitcnt lgkmcnt(10)\n"
    "v_mfma_f32_32x32x16_bf16 v[32:47], v[80:83], v[72:75], v[32:47]\n"
    "ds_read_b128 v[96:99], v138 offset:0\n"
    "ds_read_b128 v[104:107], v138 offset:4096\n"
    "s_waitcnt lgkmcnt(10)\n"
    "v_mfma_f32_32x32x16_bf16 v[16:31], v[88:91], v[64:67], v[16:31]\n"
    "ds_read_b64_tr_b16 v[120:121], v145 offset:8192\n"
    "ds_read_b64_tr_b16 v[122:123], v145 offset:9216\n"
    "v_mfma_f32_32x32x16_bf16 v[48:63], v[88:91], v[72:75], v[48:63]\n"
    "ds_read_b64_tr_b16 v[116:117], v144 offset:12288\n"
    "ds_read_b64_tr_b16 v[118:119], v144 offset:13312\n"
    "s_waitcnt lgkmcnt(11)\n"
    "v_mfma_f32_32x32x16_bf16 v[0:15], v[84:87], v[68:71], v[0:15]\n"
    "ds_read_b128 v[100:103], v139 offset:0\n"
    "ds_read_b128 v[108:111], v139 offset:4096\n"
    "s_waitcnt lgkmcnt(12)\n"
    "v_mfma_f32_32x32x16_bf16 v[32:47], v[84:87], v[76:79], v[32:47]\n"
    "ds_read_b64_tr_b16 v[124:125], v145 offset:12288\n"
    "ds_read_b64_tr_b16 v[126:127], v145 offset:13312\n"
    "s_waitcnt lgkmcnt(12)\n"
    "v_mfma_f32_32x32x16_bf16 v[16:31], v[92:95], v[68:71], v[16:31]\n"
    "v_mfma_f32_32x32x16_bf16 v[48:63], v[92:95], v[76:79], v[48:63]\n"
    "s_waitcnt lgkmcnt(0)\n"
    "s_barrier\n"
    "v_mfma_f32_32x32x16_bf16 v[0:15], v[112:115], v[96:99], v[0:15]\n"
    "ds_read_b64_tr_b16 v[80:81], v140 offset:0\n"
    "ds_read_b64_tr_b16 v[82:83], v140 offset:1024\n"
    "v_mfma_f32_32x32x16_bf16 v[32:47], v[112:115], v[104:107], v[32:47]\n"
    "ds_read_b128 v[64:67], v128 offset:0\n"
    "ds_read_b128 v[72:75], v128 offset:4096\n"
    "v_mfma_f32_32x32x16_bf16 v[16:31], v[120:123], v[96:99], v[16:31]\n"
    "ds_read_b64_tr_b16 v[88:89], v141 offset:0\n"
    "ds_read_b64_tr_b16 v[90:91], v141 offset:1024\n"
    "v_mfma_f32_32x32x16_bf16 v[48:63], v[120:123], v[104:107], v[48:63]\n"
    "ds_read_b64_tr_b16 v[84:85], v140 offset:4096\n"
    "ds_read_b64_tr_b16 v[86:87], v140 offset:5120\n"
    "v_mfma_f32_32x32x16_bf16 v[0:15], v[116:119], v[100:103], v[0:15]\n"
    "ds_read_b128 v[68:71], v129 offset:0\n"
    "ds_read_b128 v[76:79], v129 offset:4096\n"
    "v_mfma_f32_32x32x16_bf16 v[32:47], v[116:119], v[108:111], v[32:47]\n"
    "ds_read_b64_tr_b16 v[92:93], v141 offset:4096\n"
    "ds_read_b64_tr_b16 v[94:95], v141 offset:5120\n"
    "v_mfma_f32_32x32x16_bf16 v[16:31], v[124:127], v[100:103], v[16:31]\n"
    "v_mfma_f32_32x32x16_bf16 v[48:63], v[124:127], v[108:111], v[48:63]\n"
    "s_waitcnt lgkmcnt(9)\n"
    "v_mfma_f32_32x32x16_bf16 v[0:15], v[80:83], v[64:67], v[0:15]\n"
    "ds_read_b64_tr_b16 v[112:113], v140 offset:8192\n"
    "ds_read_b64_tr_b16 v[114:115], v140 offset:9216\n"
    "s_waitcnt lgkmcnt(10)\n"
    "v_mfma_f32_32x32x16_bf16 v[32:47], v[80:83], v[72:75], v[32:47]\n"
    "ds_read_b128 v[96:99], v130 offset:0\n"
    "ds_read_b128 v[104:107], v130 offset:4096\n"
    "s_waitcnt lgkmcnt(10)\n"
    "v_mfma_f32_32x32x16_bf16 v[16:31], v[88:91], v[64:67], v[16:31]\n"
    "ds_read_b64_tr_b16 v[120:121], v141 offset:8192\n"
    "ds_read_b64_tr_b16 v[122:123], v141 offset:9216\n"
    "v_mfma_f32_32x32x16_bf16 v[48:63], v[88:91], v[72:75], v[48:63]\n"
    "ds_read_b64_tr_b16 v[116:117], v140 offset:12288\n"
    "ds_read_b64_tr_b16 v[118:119], v140 offset:13312\n"
    "s_waitcnt lgkmcnt(11)\n"
    "v_mfma_f32_32x32x16_bf16 v[0:15], v[84:87], v[68:71], v[0:15]\n"
    "ds_read_b128 v[100:103], v131 offset:0\n"
    "ds_read_b128 v[108:111], v131 offset:4096\n"
    "s_waitcnt lgkmcnt(12)\n"
    "v_mfma_f32_32x32x16_bf16 v[32:47], v[84:87], v[76:79], v[32:47]\n"
    "ds_read_b64_tr_b16 v[124:125], v141 offset:12288\n"
    "ds_read_b64_tr_b16 v[126:127], v141 offset:13312\n"
    "s_waitcnt lgkmcnt(12)\n"
    "v_mfma_f32_32x32x16_bf16 v[16:31], v[92:95], v[68:71], v[16:31]\n"
    "v_mfma_f32_32x32x16_bf16 v[48:63], v[92:95], v[76:79], v[48:63]\n"
    "s_waitcnt lgkmcnt(0)\n"
    "s_barrier\n"
    "v_mfma_f32_32x32x16_bf16 v[0:15], v[112:115], v[96:99], v[0:15]\n"
    "ds_read_b64_tr_b16 v[80:81], v140 offset:32768\n"
    "ds_read_b64_tr_b16 v[82:83], v140 offset:33792\n"
    "v_mfma_f32_32x32x16_bf16 v[32:47], v[112:115], v[104:107], v[32:47]\n"
    "ds_read_b128 v[64:67], v128 offset:32768\n"
    "ds_read_b128 v[72:75], v128 offset:36864\n"
    "v_mfma_f32_32x32x16_bf16 v[16:31], v[120:123], v[96:99], v[16:31]\n"
    "ds_read_b64_tr_b16 v[88:89], v141 offset:32768\n"
    "ds_read_b64_tr_b16 v[90:91], v141 offset:33792\n"
    "v_mfma_f32_32x32x16_bf16 v[48:63], v[120:123], v[104:107], v[48:63]\n"
    "ds_read_b64_tr_b16 v[84:85], v140 offset:36864\n"
    "ds_read_b64_tr_b16 v[86:87], v140 offset:37888\n"
    "v_mfma_f32_32x32x16_bf16 v[0:15], v[116:119], v[100:103], v[0:15]\n"
    "ds_read_b128 v[68:71], v129 offset:32768\n"
    "ds_read_b128 v[76:79], v129 offset:36864\n"
    "v_mfma_f32_32x32x16_bf16 v[32:47], v[116:119], v[108:111], v[32:47]\n"
    "ds_read_b64_tr_b16 v[92:93], v141 offset:36864\n"
    "ds_read_b64_tr_b16 v[94:95], v141 offset:37888\n"
    "v_mfma_f32_32x32x16_bf16 v[16:31], v[124:127], v[100:103], v[16:31]\n"
    "v_mfma_f32_32x32x16_bf16 v[48:63], v[124:127], v[108:111], v[48:63]\n"
    "s_waitcnt lgkmcnt(9)\n"
    "v_mfma_f32_32x32x16_bf16 v[0:15], v[80:83], v[64:67], v[0:15]\n"
    "ds_read_b64_tr_b16 v[112:113], v140 offset:40960\n"
    "ds_read_b64_tr_b16 v[114:115], v140 offset:41984\n"
    "s_waitcnt lgkmcnt(10)\n"
    "v_mfma_f32_32x32x16_bf16 v[32:47], v[80:83], v[72:75], v[32:47]\n"
    "ds_read_b128 v[96:99], v130 offset:32768\n"
    "ds_read_b128 v[104:107], v130 offset:36864\n"
    "s_waitcnt lgkmcnt(10)\n"
    "v_mfma_f32_32x32x16_bf16 v[16:31], v[88:91], v[64:67], v[16:31]\n"
    "ds_read_b64_tr_b16 v[120:121], v141 offset:40960\n"
    "ds_read_b64_tr_b16 v[122:123], v141 offset:41984\n"
    "v_mfma_f32_32x32x16_bf16 v[48:63], v[88:91], v[72:75], v[48:63]\n"
    "ds_read_b64_tr_b16 v[116:117], v140 offset:45056\n"
    "ds_read_b64_tr_b16 v[118:119], v140 offset:46080\n"
    "s_waitcnt lgkmcnt(11)\n"
    "v_mfma_f32_32x32x16_bf16 v[0:15], v[84:87], v[68:71], v[0:15]\n"
    "ds_read_b128 v[100:103], v131 offset:32768\n"
    "ds_read_b128 v[108:111], v131 offset:36864\n"
    "s_waitcnt lgkmcnt(12)\n"
    "v_mfma_f32_32x32x16_bf16 v[32:47], v[84:87], v[76:79], v[32:47]\n"
    "ds_read_b64_tr_b16 v[124:125], v141 offset:45056\n"
    "ds_read_b64_tr_b16 v[126:127], v141 offset:46080\n"
    "s_waitcnt lgkmcnt(12)\n"
    "v_mfma_f32_32x32x16_bf16 v[16:31], v[92:95], v[68:71], v[16:31]\n"
    "v_mfma_f32_32x32x16_bf16 v[48:63], v[92:95], v[76:79], v[48:63]\n"
    "s_waitcnt lgkmcnt(0)\n"
    "s_barrier\n"
    "v_mfma_f32_32x32x16_bf16 v[0:15], v[112:115], v[96:99], v[0:15]\n"
    "ds_read_b64_tr_b16 v[80:81], v142 offset:0\n"
    "ds_read_b64_tr_b16 v[82:83], v142 offset:1024\n"
    "v_mfma_f32_32x32x16_bf16 v[32:47], v[112:115], v[104:107], v[32:47]\n"
    "ds_read_b128 v[64:67], v132 offset:0\n"
    "ds_read_b128 v[72:75], v132 offset:4096\n"
    "v_mfma_f32_32x32x16_bf16 v[16:31], v[120:123], v[96:99], v[16:31]\n"
    "ds_read_b64_tr_b16 v[88:89], v143 offset:0\n"
    "ds_read_b64_tr_b16 v[90:91], v143 offset:1024\n"
    "v_mfma_f32_32x32x16_bf16 v[48:63], v[120:123], v[104:107], v[48:63]\n"
    "ds_read_b64_tr_b16 v[84:85], v142 offset:4096\n"
    "ds_read_b64_tr_b16 v[86:87], v142 offset:5120\n"
    "v_mfma_f32_32x32x16_bf16 v[0:15], v[116:119], v[100:103], v[0:15]\n"
    "ds_read_b128 v[68:71], v133 offset:0\n"
    "ds_read_b128 v[76:79], v133 offset:4096\n"
    "v_mfma_f32_32x32x16_bf16 v[32:47], v[116:119], v[108:111], v[32:47]\n"
    "ds_read_b64_tr_b16 v[92:93], v143 offset:4096\n"
    "ds_read_b64_tr_b16 v[94:95], v143 offset:5120\n"
    "v_mfma_f32_32x32x16_bf16 v[16:31], v[124:127], v[100:103], v[16:31]\n"
    "v_mfma_f32_32x32x16_bf16 v[48:63], v[124:127], v[108:111], v[48:63]\n"
    "s_sub_u32 %[n], %[n], 1\n"
    "s_cmp_lg_u32 %[n], 0\n"
    "s_cbranch_scc1 9b\n"
    "s_nop 15\n"

            P3_OPS, [n] "+s"(n) : P3_INS : "memory", "scc"); }
        asm volatile(
    "s_nop 1\n"
    "s_waitcnt lgkmcnt(9)\n"
    "v_mfma_f32_32x32x16_bf16 v[0:15], v[80:83], v[64:67], v[0:15]\n"
    "ds_read_b64_tr_b16 v[112:113], v142 offset:8192\n"
    "ds_read_b64_tr_b16 v[114:115], v142 offset:9216\n"
    "s_waitcnt lgkmcnt(10)\n"
    "v_mfma_f32_32x32x16_bf16 v[32:47], v[80:83], v[72:75], v[32:47]\n"
    "ds_read_b128 v[96:99], v134 offset:0\n"
    "ds_read_b128 v[104:107], v134 offset:4096\n"
    "s_waitcnt lgkmcnt(10)\n"
    "v_mfma_f32_32x32x16_bf16 v[16:31], v[88:91], v[64:67], v[16:31]\n"
    "ds_read_b64_tr_b16 v[120:121], v143 offset:8192\n"
    "ds_read_b64_tr_b16 v[122:123], v143 offset:9216\n"
    "v_mfma_f32_32x32x16_bf16 v[48:63], v[88:91], v[72:75], v[48:63]\n"
    "ds_read_b64_tr_b16 v[116:117], v142 offset:12288\n"
    "ds_read_b64_tr_b16 v[118:119], v142 offset:13312\n"
    "s_waitcnt lgkmcnt(11)\n"
    "v_mfma_f32_32x32x16_bf16 v[0:15], v[84:87], v[68:71], v[0:15]\n"
    "ds_read_b128 v[100:103], v135 offset:0\n"
    "ds_read_b128 v[108:111], v135 offset:4096\n"
    "s_waitcnt lgkmcnt(12)\n"
    "v_mfma_f32_32x32x16_bf16 v[32:47], v[84:87], v[76:79], v[32:47]\n"
    "ds_read_b64_tr_b16 v[124:125], v143 offset:12288\n"
    "ds_read_b64_tr_b16 v[126:127], v143 offset:13312\n"
    "s_waitcnt lgkmcnt(12)\n"
    "v_mfma_f32_32x32x16_bf16 v[16:31], v[92:95], v[68:71], v[16:31]\n"
    "v_mfma_f32_32x32x16_bf16 v[48:63], v[92:95], v[76:79], v[48:63]\n"
    "s_waitcnt lgkmcnt(0)\n"
    "s_barrier\n"
    "v_mfma_f32_32x32x16_bf16 v[0:15], v[112:115], v[96:99], v[0:15]\n"
    "ds_read_b64_tr_b16 v[80:81], v142 offset:32768\n"
    "ds_read_b64_tr_b16 v[82:83], v142 offset:33792\n"
    "v_mfma_f32_32x32x16_bf16 v[32:47], v[112:115], v[104:107], v[32:47]\n"
    "ds_read_b128 v[64:67], v132 offset:32768\n"
    "ds_read_b128 v[72:75], v132 offset:36864\n"
    "v_mfma_f32_32x32x16_bf16 v[16:31], v[120:123], v[96:99], v[16:31]\n"
    "ds_read_b64_tr_b16 v[88:89], v143 offset:32768\n"
    "ds_read_b64_tr_b16 v[90:91], v143 offset:33792\n"
    "v_mfma_f32_32x32x16_bf16 v[48:63], v[120:123], v[104:107], v[48:63]\n"
    "ds_read_b64_tr_b16 v[84:85], v142 offset:36864\n"
    "ds_read_b64_tr_b16 v[86:87], v142 offset:37888\n"
    "v_mfma_f32_32x32x16_bf16 v[0:15], v[116:119], v[100:103], v[0:15]\n"
    "ds_read_b128 v[68:71], v133 offset:32768\n"
    "ds_read_b128 v[76:79], v133 offset:36864\n"
    "v_mfma_f32_32x32x16_bf16 v[32:47], v[116:119], v[108:111], v[32:47]\n"
    "ds_read_b64_tr_b16 v[92:93], v143 offset:36864\n"
    "ds_read_b64_tr_b16 v[94:95], v143 offset:37888\n"
    "v_mfma_f32_32x32x16_bf16 v[16:31], v[124:127], v[100:103], v[16:31]\n"
    "v_mfma_f32_32x32x16_bf16 v[48:63], v[124:127], v[108:111], v[48:63]\n"
    "s_nop 15\n"

            P3_OPS : P3_INS : "memory");
        f32x4 pv[16];
#pragma unroll
        for (int i = 0; i < 16; ++i) asm volatile("global_load_dwordx4 %0, %1, off sc1" : "=v"(pv[i]) : "v"(xch + i * 256) : "memory");
        asm volatile(
    "s_nop 1\n"
    "s_waitcnt lgkmcnt(9)\n"
    "v_mfma_f32_32x32x16_bf16 v[0:15], v[80:83], v[64:67], v[0:15]\n"
    "ds_read_b64_tr_b16 v[112:113], v142 offset:40960\n"
    "ds_read_b64_tr_b16 v[114:115], v142 offset:41984\n"
    "s_waitcnt lgkmcnt(10)\n"
    "v_mfma_f32_32x32x16_bf16 v[32:47], v[80:83], v[72:75], v[32:47]\n"
    "ds_read_b128 v[96:99], v134 offset:32768\n"
    "ds_read_b128 v[104:107], v134 offset:36864\n"
    "s_waitcnt lgkmcnt(10)\n"
    "v_mfma_f32_32x32x16_bf16 v[16:31], v[88:91], v[64:67], v[16:31]\n"
    "ds_read_b64_tr_b16 v[120:121], v143 offset:40960\n"
    "ds_read_b64_tr_b16 v[122:123], v143 offset:41984\n"
    "v_mfma_f32_32x32x16_bf16 v[48:63], v[88:91], v[72:75], v[48:63]\n"
    "ds_read_b64_tr_b16 v[116:117], v142 offset:45056\n"
    "ds_read_b64_tr_b16 v[118:119], v142 offset:46080\n"
    "s_waitcnt lgkmcnt(11)\n"
    "v_mfma_f32_32x32x16_bf16 v[0:15], v[84:87], v[68:71], v[0:15]\n"
    "ds_read_b128 v[100:103], v135 offset:32768\n"
    "ds_read_b128 v[108:111], v135 offset:36864\n"
    "s_waitcnt lgkmcnt(12)\n"
    "v_mfma_f32_32x32x16_bf16 v[32:47], v[84:87], v[76:79], v[32:47]\n"
    "ds_read_b64_tr_b16 v[124:125], v143 offset:45056\n"
    "ds_read_b64_tr_b16 v[126:127], v143 offset:46080\n"
    "s_waitcnt lgkmcnt(12)\n"
    "v_mfma_f32_32x32x16_bf16 v[16:31], v[92:95], v[68:71], v[16:31]\n"
    "v_mfma_f32_32x32x16_bf16 v[48:63], v[92:95], v[76:79], v[48:63]\n"
    "s_waitcnt lgkmcnt(0)\n"
    "s_barrier\n"
    "v_mfma_f32_32x32x16_bf16 v[0:15], v[112:115], v[96:99], v[0:15]\n"
    "ds_read_b64_tr_b16 v[80:81], v144 offset:0\n"
    "ds_read_b64_tr_b16 v[82:83], v144 offset:1024\n"
    "v_mfma_f32_32x32x16_bf16 v[32:47], v[112:115], v[104:107], v[32:47]\n"
    "ds_read_b128 v[64:67], v136 offset:0\n"
    "ds_read_b128 v[72:75], v136 offset:4096\n"
    "v_mfma_f32_32x32x16_bf16 v[16:31], v[120:123], v[96:99], v[16:31]\n"
    "ds_read_b64_tr_b16 v[88:89], v145 offset:0\n"
    "ds_read_b64_tr_b16 v[90:91], v145 offset:1024\n"
    "v_mfma_f32_32x32x16_bf16 v[48:63], v[120:123], v[104:107], v[48:63]\n"
    "ds_read_b64_tr_b16 v[84:85], v144 offset:4096\n"
    "ds_read_b64_tr_b16 v[86:87], v144 offset:5120\n"
    "v_mfma_f32_32x32x16_bf16 v[0:15], v[116:119], v[100:103], v[0:15]\n"
    "ds_read_b128 v[68:71], v137 offset:0\n"
    "ds_read_b128 v[76:79], v137 offset:4096\n"
    "v_mfma_f32_32x32x16_bf16 v[32:47], v[116:119], v[108:111], v[32:47]\n"
    "ds_read_b64_tr_b16 v[92:93], v145 offset:4096\n"
    "ds_read_b64_tr_b16 v[94:95], v145 offset:5120\n"
    "v_mfma_f32_32x32x16_bf16 v[16:31], v[124:127], v[100:103], v[16:31]\n"
    "v_mfma_f32_32x32x16_bf16 v[48:63], v[124:127], v[108:111], v[48:63]\n"
    "s_waitcnt lgkmcnt(9)\n"
    "v_mfma_f32_32x32x16_bf16 v[0:15], v[80:83], v[64:67], v[0:15]\n"
    "ds_read_b64_tr_b16 v[112:113], v144 offset:8192\n"
    "ds_read_b64_tr_b16 v[114:115], v144 offset:9216\n"
    "s_waitcnt lgkmcnt(10)\n"
    "v_mfma_f32_32x32x16_bf16 v[32:47], v[80:83], v[72:75], v[32:47]\n"
    "ds_read_b128 v[96:99], v138 offset:0\n"
    "ds_read_b128 v[104:107], v138 offset:4096\n"
    "s_waitcnt lgkmcnt(10)\n"
    "v_mfma_f32_32x32x16_bf16 v[16:31], v[88:91], v[64:67], v[16:31]\n"
    "ds_read_b64_tr_b16 v[120:121], v145 offset:8192\n"
    "ds_read_b64_tr_b16 v[122:123], v145 offset:9216\n"
    "v_mfma_f32_32x32x16_bf16 v[48:63], v[88:91], v[72:75], v[48:63]\n"
    "ds_read_b64_tr_b16 v[116:117], v144 offset:12288\n"
    "ds_read_b64_tr_b16 v[118:119], v144 offset:13312\n"
    "s_waitcnt lgkmcnt(11)\n"
    "v_mfma_f32_32x32x16_bf16 v[0:15], v[84:87], v[68:71], v[0:15]\n"
    "ds_read_b128 v[100:103], v139 offset:0\n"
    "ds_read_b128 v[108:111], v139 offset:4096\n"
    "s_waitcnt lgkmcnt(12)\n"
    "v_mfma_f32_32x32x16_bf16 v[32:47], v[84:87], v[76:79], v[32:47]\n"
    "ds_read_b64_tr_b16 v[124:125], v145 offset:12288\n"
    "ds_read_b64_tr_b16 v[126:127], v145 offset:13312\n"
    "s_waitcnt lgkmcnt(12)\n"
    "v_mfma_f32_32x32x16_bf16 v[16:31], v[92:95], v[68:71], v[16:31]\n"
    "v_mfma_f32_32x32x16_bf16 v[48:63], v[92:95], v[76:79], v[48:63]\n"
    "s_waitcnt lgkmcnt(9)\n"
    "v_mfma_f32_32x32x16_bf16 v[0:15], v[112:115], v[96:99], v[0:15]\n"
    "s_waitcnt lgkmcnt(8)\n"
    "v_mfma_f32_32x32x16_bf16 v[32:47], v[112:115], v[104:107], v[32:47]\n"
    "s_waitcnt lgkmcnt(6)\n"
    "v_mfma_f32_32x32x16_bf16 v[16:31], v[120:123], v[96:99], v[16:31]\n"
    "v_mfma_f32_32x32x16_bf16 v[48:63], v[120:123], v[104:107], v[48:63]\n"
    "s_waitcnt lgkmcnt(3)\n"
    "v_mfma_f32_32x32x16_bf16 v[0:15], v[116:119], v[100:103], v[0:15]\n"
    "s_waitcnt lgkmcnt(2)\n"
    "v_mfma_f32_32x32x16_bf16 v[32:47], v[116:119], v[108:111], v[32:47]\n"
    "s_waitcnt lgkmcnt(0)\n"
    "v_mfma_f32_32x32x16_bf16 v[16:31], v[124:127], v[100:103], v[16:31]\n"
    "v_mfma_f32_32x32x16_bf16 v[48:63], v[124:127], v[108:111], v[48:63]\n"
    "s_nop 15\n"

            P3_OPS : P3_INS : "memory");
        P3_SCALE();

        asm volatile("s_waitcnt vmcnt(0)" : "+v"(pv[0]), "+v"(pv[1]), "+v"(pv[2]), "+v"(pv[3]), "+v"(pv[4]), "+v"(pv[5]), "+v"(pv[6]), "+v"(pv[7]),
                     "+v"(pv[8]), "+v"(pv[9]), "+v"(pv[10]), "+v"(pv[11]), "+v"(pv[12]), "+v"(pv[13]), "+v"(pv[14]), "+v"(pv[15]) :: "memory");
#pragma unroll
        for (int mi = 0; mi < 2; ++mi)
#pragma unroll
            for (int ni = 0; ni < 2; ++ni) {
                f32x16 v = acc[2 * mi + ni];
#pragma unroll
                for (int G = 0; G < 4; ++G)
#pragma unroll
                    for (int e = 0; e < 4; ++e) v[4 * G + e] += pv[(2 * mi + ni) * 4 + G][e];
                store_tile32(a.out + (size_t)(rb * 128 + wr * 64 + mi * 32) * D_ + cb * 128 + wc * 64 + ni * 32, D_, v, lane);
            }
    }
#undef P3_OPS
#undef P3_INS
#undef P3_SCALE
}
DI void body(const Params& a, const unsigned lds0, const int w, const unsigned tag) {
    if (kh_of()) { if (w >= 4) producer<1>(a, lds0, w, tag); else consumer<1>(a, lds0, w, tag); }
    else { if (w >= 4) producer<0>(a, lds0, w, tag); else consumer<0>(a, lds0, w, tag); }
}
}

constexpr int LDS_BYTES = 163840;

__global__ __launch_bounds__(512, 2) void fused_kernel(Params a) {
    extern __shared__ __attribute__((aligned(16))) char smem[];
    const unsigned lds0 = (unsigned)(uintptr_t)smem;
    const int w = __builtin_amdgcn_readfirstlane(threadIdx.x >> 6);
    const int xg = blockIdx.x & 7, j = blockIdx.x >> 3;
    const int rb = j & 7, ct = xg * 4 + (j >> 3);

    unsigned codepf = 0;
    if (w == 7) {
        unsigned long long pc; unsigned len;
        asm volatile("s_getpc_b64 %0\n.Lfused_pc:\n\ts_mov_b32 %1, .Lfused_end-.Lfused_pc" : "=s"(pc), "=s"(len));
        unsigned off = (unsigned)(j * 64 + lane_id()) * 128u; off = off < len ? off : 0u;
        asm volatile("global_load_dword %0, %1, %2" : "+v"(codepf) : "v"(off), "s"(pc & ~3ull) : "memory");
    }
    unsigned tag;
    bool early; float gval;
    {
        f32x16 acc[10];
        p1::mainloop(a, lds0, w, rb, ct, acc, tag, gval);

        early = p1::epilogue(a, lds0, acc, w, rb, ct, tag, gval);
    }
    const int lane = lane_id();

    p2::Ctx c;
    c.h = ct; c.qb = rb; c.rb3h = p3::rb_of() >> 1; c.tag = tag;
    if (w >= 4) {
        p2::helper(a, lds0, c, w, early);
    } else {
        f32x16 oa, ob; float la, lb, ga, gb; bf16x8 q[2][2];
        const bool ok = p2::attn_fast(a, lds0, c, w, oa, ob, la, lb, ga, gb, q);

        if (__builtin_amdgcn_ballot_w64(!ok) != 0ull) p2::attn_safe(a, c, q, oa, ob, la, lb);
        const int row = rb * 256 + w * 64;
        p2::store_o(a, c, row, lane, oa, ga * __builtin_amdgcn_rcpf(la));
        p2::store_o(a, c, row + 32, lane, ob, gb * __builtin_amdgcn_rcpf(lb));
    }

    p3::body(a, lds0, w, tag);

    if (blockIdx.x == 0 && w == 0) {
        unsigned spins = 0;
        for (int i = 0; i < 4; ++i)
            while (__builtin_amdgcn_ballot_w64(flag_get(a.flags + FLAG_H + i * 64 + lane) != tag) != 0ull) { __builtin_amdgcn_s_sleep(2); if (++spins > SPIN_MAX) break; }
        if (lane == 0) flag_set(a.flags + FLAG_EPOCH, tag);
    }
    asm volatile(".Lfused_end:" :: "v"(codepf));
}

extern "C" void kernel_launch(void* const* d_in, const int* in_sizes, int n_in, void* d_out, int out_size, void* d_ws, size_t ws_size, hipStream_t stream) {
    char* ws = (char*)d_ws;
    Params p;
    p.x = (const bf16_t*)d_in[0]; p.wqkv = (const bf16_t*)d_in[1]; p.wo = (const bf16_t*)d_in[2]; p.wg = (const bf16_t*)d_in[4]; p.wu = (const bf16_t*)d_in[5];
    p.wd = (const bf16_t*)d_in[6]; p.gw = (const bf16_t*)d_in[7]; p.out = (bf16_t*)d_out;
    p.kv = (bf16_t*)ws; p.ho = (bf16_t*)(ws + (16 << 20)); p.g = (float*)(ws + (36 << 20)); p.xch = (float*)(ws + (40 << 20)); p.flags = (unsigned*)(ws + (50 << 20));
    (void)hipFuncSetAttribute((const void*)fused_kernel, hipFuncAttributeMaxDynamicSharedMemorySize, LDS_BYTES);
    fused_kernel<<<256, 512, LDS_BYTES, stream>>>(p);
}
```
